```python
import torch

def _softmax(x, axis=-1):
    return torch.softmax(x, dim=axis)

def _gelu(x):
    return torch.nn.functional.gelu(x, approximate='tanh')

def _silu(x):
    return torch.nn.functional.silu(x)

def _sigmoid(x):
    return torch.sigmoid(x)

def _layernorm(x, axis=-1, eps=1e-05):
    m = x.mean(dim=axis, keepdim=True)
    v = x.var(dim=axis, keepdim=True, unbiased=False)
    return (x - m) / torch.sqrt(v + eps)

def _scatter_add(tgt, idx, src, axis=0):
    return tgt.clone().index_add_(axis, idx, src)

def _segment_sum(src, seg, n):
    out = torch.zeros((n,) + src.shape[1:], dtype=src.dtype)
    return out.index_add_(0, seg, src)

def _pad(x, pw):
    flat = tuple((v for pair in reversed(pw) for v in pair))
    return torch.nn.functional.pad(x, flat)
kernel_name = 'sequential_preLN__spectral_fnet__mixer_gmlp__prompt'

def setup_inputs():

    _w = lambda a: (lambda t: t.to(torch.bfloat16) if t.is_floating_point() else t)(torch.as_tensor(a))

    def _A_setup():
        B, S, D = (1, 2048, 640)
        ns = torch.arange(S)[:, None] * torch.arange(S)[None, :]
        nd = torch.arange(D)[:, None] * torch.arange(D)[None, :]
        Ws_re = torch.cos(-2.0 * torch.pi * ns / S).to(float)
        Ws_im = torch.sin(-2.0 * torch.pi * ns / S).to(float)
        Wd_re = torch.cos(-2.0 * torch.pi * nd / D).to(float)
        Wd_im = torch.sin(-2.0 * torch.pi * nd / D).to(float)
        return {'x': _w((lambda *s: torch.randn(*s, dtype=torch.bfloat16) * 0.05)(B, S, D)), 'dft_seq_re': _w(Ws_re), 'dft_seq_im': _w(Ws_im), 'dft_hid_re': _w(Wd_re), 'dft_hid_im': _w(Wd_im)}
    _A_in = _A_setup()

    def _B_setup():
        B, S, D, F = (1, 2048, 640, 1536)
        Fh = F // 2
        return {'x': _w((lambda *s: torch.randn(*s, dtype=torch.bfloat16) * 0.05)(B, S, D)), 'w_in': _w((lambda *s: torch.randn(*s, dtype=torch.bfloat16) * 0.8)(D, 2 * Fh)), 'w_sp': _w(0.01 * (lambda *s: torch.randn(*s, dtype=torch.bfloat16) * 0.8)(S, S)), 'b_sp': _w(1.0 + 0.01 * (lambda *s: torch.randn(*s, dtype=torch.bfloat16) * 0.8)(S)), 'w_out': _w((lambda *s: torch.randn(*s, dtype=torch.bfloat16) * 0.8)(Fh, D))}
    _B_in = _B_setup()
    return {'A_x': _A_in['x'], 'A_dft_seq_re': _A_in['dft_seq_re'], 'A_dft_seq_im': _A_in['dft_seq_im'], 'A_dft_hid_re': _A_in['dft_hid_re'], 'A_dft_hid_im': _A_in['dft_hid_im'], 'B_x': _B_in['x'], 'B_w_in': _B_in['w_in'], 'B_w_sp': _B_in['w_sp'], 'B_b_sp': _B_in['b_sp'], 'B_w_out': _B_in['w_out']}

def reference(A_x, A_dft_seq_re, A_dft_seq_im, A_dft_hid_re, A_dft_hid_im, B_x, B_w_in, B_w_sp, B_b_sp, B_w_out):

    def _A_ref(x, dft_seq_re, dft_seq_im, dft_hid_re, dft_hid_im):
        a = torch.einsum('bsd,ed->bse', x, dft_hid_re)
        b = torch.einsum('bsd,ed->bse', x, dft_hid_im)
        y = torch.einsum('ts,bse->bte', dft_seq_re, a) - torch.einsum('ts,bse->bte', dft_seq_im, b)
        return y

    def _B_ref(x, w_in, w_sp, b_sp, w_out):
        B, S, D = x.shape
        F = w_in.shape[-1]
        Fh = F // 2
        z = _gelu(torch.einsum('bsd,df->bsf', x, w_in))
        z1 = z[..., :Fh].clone()
        z2 = _layernorm(z[..., Fh:], axis=-1)
        g = torch.einsum('ts,bsf->btf', w_sp, z2) + b_sp[None, :, None]
        return torch.einsum('bsf,fd->bsd', z1 * g, w_out)
    x = A_x
    h = x + _A_ref(_layernorm(x, axis=-1), A_dft_seq_re, A_dft_seq_im, A_dft_hid_re, A_dft_hid_im)
    h2 = h + _B_ref(_layernorm(h, axis=-1), B_w_in, B_w_sp, B_b_sp, B_w_out)
    return h2
```

```cpp
#include <hip/hip_runtime.h>
#include <hip/hip_bf16.h>
#include <stdint.h>
typedef unsigned short u16;
using bf16x8 = __attribute__((ext_vector_type(8))) short;
using f32x4 = __attribute__((ext_vector_type(4))) float;
static __device__ __forceinline__ float bf2f(u16 v){ return __uint_as_float(((unsigned)v)<<16); }
static __device__ __forceinline__ u16 f2bf(float f){ __hip_bfloat16 b = __float2bfloat16(f); return *(u16*)&b; }

constexpr int S=2048, D=640, F=1536, FH=768;

__global__ void ln_rows(const u16* in, int ld_in, int n, u16* out, int ld_out, int transpose){
  int row = blockIdx.x; int t = threadIdx.x;
  __shared__ float red[4];
  float s=0; for(int i=t;i<n;i+=256) s+=bf2f(in[(size_t)row*ld_in+i]);
  for(int o=32;o;o>>=1) s+=__shfl_xor(s,o);
  if((t&63)==0) red[t>>6]=s; __syncthreads();
  float m=(red[0]+red[1]+red[2]+red[3])/n; __syncthreads();
  float v=0; for(int i=t;i<n;i+=256){ float d=bf2f(in[(size_t)row*ld_in+i])-m; v+=d*d; }
  for(int o=32;o;o>>=1) v+=__shfl_xor(v,o);
  if((t&63)==0) red[t>>6]=v; __syncthreads();
  float var=(red[0]+red[1]+red[2]+red[3])/n;
  float r=rsqrtf(var+1e-5f);
  for(int i=t;i<n;i+=256){ float d=(bf2f(in[(size_t)row*ld_in+i])-m)*r;
    if(transpose) out[(size_t)i*ld_out+row]=f2bf(d); else out[(size_t)row*ld_out+i]=f2bf(d); }
}
__global__ void transpose_k(const u16* in, int R, int C, u16* out){
  int c=blockIdx.x*16+(threadIdx.x&15), r=blockIdx.y*16+(threadIdx.x>>4);
  out[(size_t)c*R+r]=in[(size_t)r*C+c];
}
template<int MODE>
__global__ void gemm(const u16* A,int lda,const u16* A2,const u16* Bt,int ldb,const u16* Bt2,int K,
                     u16* out,int ldo,const u16* e0,const u16* e1){
  int lane=threadIdx.x&63, w=threadIdx.x>>6;
  int m0=blockIdx.y*64+(w>>1)*32, n0=blockIdx.x*64+(w&1)*32;
  int fr=lane&15,fq=lane>>4;
  f32x4 acc[2][2]={};
  for(int pass=0;pass<(A2?2:1);++pass){
    const u16* a=pass?A2:A; const u16* b=pass?Bt2:Bt;
    if(pass) for(int i=0;i<2;++i)for(int j=0;j<2;++j) acc[i][j]=-acc[i][j];
    for(int k=0;k<K;k+=32){
      bf16x8 af[2],bfr[2];
      for(int i=0;i<2;++i) af[i]=*(const bf16x8*)(a+(size_t)(m0+i*16+fr)*lda+k+fq*8);
      for(int j=0;j<2;++j) bfr[j]=*(const bf16x8*)(b+(size_t)(n0+j*16+fr)*ldb+k+fq*8);
      for(int i=0;i<2;++i)for(int j=0;j<2;++j) acc[i][j]=__builtin_amdgcn_mfma_f32_16x16x32_bf16(af[i],bfr[j],acc[i][j],0,0,0);
    }
    if(pass) for(int i=0;i<2;++i)for(int j=0;j<2;++j) acc[i][j]=-acc[i][j];
  }
  for(int i=0;i<2;++i)for(int j=0;j<2;++j)for(int r=0;r<4;++r){
    int m=m0+i*16+fq*4+r, n=n0+j*16+fr; float v=acc[i][j][r];
    if(MODE==0){ out[(size_t)n*ldo+m]=f2bf(v); }
    else if(MODE==1){ out[(size_t)m*ldo+n]=f2bf(v+bf2f(e0[(size_t)m*ldo+n])); }
    else if(MODE==2){ float u=0.7978845608f*(v+0.044715f*v*v*v); float g=v/(1.f+__expf(-2.f*u)); out[(size_t)m*ldo+n]=f2bf(g);}
    else if(MODE==3){ float g=v+bf2f(e1[m]); out[(size_t)m*ldo+n]=f2bf(g*bf2f(e0[(size_t)m*F+n])); }
  }
}
extern "C" void kernel_launch(void* const* d_in, const int* in_sizes, int n_in, void* d_out, int out_size, void* d_ws, size_t ws_size, hipStream_t stream){
  const u16* x=(const u16*)d_in[0]; const u16* wsre=(const u16*)d_in[1]; const u16* wsim=(const u16*)d_in[2];
  const u16* wdre=(const u16*)d_in[3]; const u16* wdim=(const u16*)d_in[4];
  const u16* win=(const u16*)d_in[6]; const u16* wsp=(const u16*)d_in[7]; const u16* bsp=(const u16*)d_in[8]; const u16* wout=(const u16*)d_in[9];
  u16* out=(u16*)d_out; u16* ws=(u16*)d_ws;
  u16* xn=ws; u16* aT=xn+S*D; u16* bT=aT+S*D; u16* h=bT+S*D; u16* hn=h+S*D; u16* z=hn+S*D; u16* z2T=z+S*F; u16* u=z2T+S*FH;
  u16* winT=u+S*FH; u16* woutT=winT+D*F;
  ln_rows<<<S,256,0,stream>>>(x,D,D,xn,D,0);
  transpose_k<<<dim3(F/16,D/16),256,0,stream>>>(win,D,F,winT);
  transpose_k<<<dim3(D/16,FH/16),256,0,stream>>>(wout,FH,D,woutT);
  gemm<0><<<dim3(D/64,S/64),256,0,stream>>>(xn,D,nullptr,wdre,D,nullptr,D,aT,S,nullptr,nullptr);
  gemm<0><<<dim3(D/64,S/64),256,0,stream>>>(xn,D,nullptr,wdim,D,nullptr,D,bT,S,nullptr,nullptr);
  gemm<1><<<dim3(D/64,S/64),256,0,stream>>>(wsre,S,wsim,aT,S,bT,S,h,D,x,nullptr);
  ln_rows<<<S,256,0,stream>>>(h,D,D,hn,D,0);
  gemm<2><<<dim3(F/64,S/64),256,0,stream>>>(hn,D,nullptr,winT,D,nullptr,D,z,F,nullptr,nullptr);
  ln_rows<<<S,256,0,stream>>>(z+FH,F,FH,z2T,S,1);
  gemm<3><<<dim3(FH/64,S/64),256,0,stream>>>(wsp,S,nullptr,z2T,S,nullptr,S,u,FH,z,bsp);
  gemm<1><<<dim3(D/64,S/64),256,0,stream>>>(u,FH,nullptr,woutT,FH,nullptr,FH,out,D,h,nullptr);
}
```

```cpp
#include <hip/hip_runtime.h>
#include <hip/hip_bf16.h>
#include <stdint.h>
typedef unsigned short u16;
using bf16x8 = __attribute__((ext_vector_type(8))) short;
using f32x4 = __attribute__((ext_vector_type(4))) float;
static __device__ __forceinline__ float bf2f(u16 v){ return __uint_as_float(((unsigned)v)<<16); }
static __device__ __forceinline__ u16 f2bf(float f){ __hip_bfloat16 b = __float2bfloat16(f); return *(u16*)&b; }

constexpr int S=2048, D=640, F=1536, FH=768;

__global__ void ln_rows(const u16* in, int ld_in, int n, u16* out, int ld_out, int transpose){
  int row = blockIdx.x; int t = threadIdx.x;
  __shared__ float red[4];
  float s=0; for(int i=t;i<n;i+=256) s+=bf2f(in[(size_t)row*ld_in+i]);
  for(int o=32;o;o>>=1) s+=__shfl_xor(s,o);
  if((t&63)==0) red[t>>6]=s; __syncthreads();
  float m=(red[0]+red[1]+red[2]+red[3])/n; __syncthreads();
  float v=0; for(int i=t;i<n;i+=256){ float d=bf2f(in[(size_t)row*ld_in+i])-m; v+=d*d; }
  for(int o=32;o;o>>=1) v+=__shfl_xor(v,o);
  if((t&63)==0) red[t>>6]=v; __syncthreads();
  float var=(red[0]+red[1]+red[2]+red[3])/n;
  float r=rsqrtf(var+1e-5f);
  for(int i=t;i<n;i+=256){ float d=(bf2f(in[(size_t)row*ld_in+i])-m)*r;
    if(transpose) out[(size_t)i*ld_out+row]=f2bf(d); else out[(size_t)row*ld_out+i]=f2bf(d); }
}
__global__ void transpose_k(const u16* in, int R, int C, u16* out){
  int c=blockIdx.x*16+(threadIdx.x&15), r=blockIdx.y*16+(threadIdx.x>>4);
  out[(size_t)c*R+r]=in[(size_t)r*C+c];
}
template<int MODE>
__global__ void gemm(const u16* A,int lda,const u16* A2,const u16* Bt,int ldb,const u16* Bt2,int K,
                     u16* out,int ldo,const u16* e0,const u16* e1){
  int lane=threadIdx.x&63, w=threadIdx.x>>6;
  int m0=blockIdx.y*64+(w>>1)*32, n0=blockIdx.x*64+(w&1)*32;
  int fr=lane&15,fq=lane>>4;
  f32x4 acc[2][2]={};
  for(int pass=0;pass<(A2?2:1);++pass){
    const u16* a=pass?A2:A; const u16* b=pass?Bt2:Bt;
    if(pass) for(int i=0;i<2;++i)for(int j=0;j<2;++j) acc[i][j]=-acc[i][j];
    for(int k=0;k<K;k+=32){
      bf16x8 af[2],bfr[2];
      for(int i=0;i<2;++i) af[i]=*(const bf16x8*)(a+(size_t)(m0+i*16+fr)*lda+k+fq*8);
      for(int j=0;j<2;++j) bfr[j]=*(const bf16x8*)(b+(size_t)(n0+j*16+fr)*ldb+k+fq*8);
      for(int i=0;i<2;++i)for(int j=0;j<2;++j) acc[i][j]=__builtin_amdgcn_mfma_f32_16x16x32_bf16(af[i],bfr[j],acc[i][j],0,0,0);
    }
    if(pass) for(int i=0;i<2;++i)for(int j=0;j<2;++j) acc[i][j]=-acc[i][j];
  }
  for(int i=0;i<2;++i)for(int j=0;j<2;++j)for(int r=0;r<4;++r){
    int m=m0+i*16+fq*4+r, n=n0+j*16+fr; float v=acc[i][j][r];
    if(MODE==0){ out[(size_t)n*ldo+m]=f2bf(v); }
    else if(MODE==1){ out[(size_t)m*ldo+n]=f2bf(v+bf2f(e0[(size_t)m*ldo+n])); }
    else if(MODE==2){ float u=0.7978845608f*(v+0.044715f*v*v*v); float g=v/(1.f+__expf(-2.f*u)); out[(size_t)m*ldo+n]=f2bf(g);}
    else if(MODE==3){ float g=v+bf2f(e1[m]); out[(size_t)m*ldo+n]=f2bf(g*bf2f(e0[(size_t)m*F+n])); }
  }
}

__global__ void chase_k(const char* p, int n, int stride, u16* o){
  unsigned off=0; unsigned acc=0;
  for(int i=0;i<n;++i){
    unsigned v;
    asm volatile("global_load_dword %0, %1, %2\n\ts_waitcnt vmcnt(0)" : "=v"(v) : "v"(off), "s"(p) : "memory");
    acc^=v; off+=stride;
  }
  if(acc==0x12345678 && blockIdx.x==77) o[0]=1;
}
extern "C" void kernel_launch(void* const* d_in, const int* in_sizes, int n_in, void* d_out, int out_size, void* d_ws, size_t ws_size, hipStream_t stream){
  const u16* x=(const u16*)d_in[0]; const u16* wsre=(const u16*)d_in[1]; const u16* wsim=(const u16*)d_in[2];
  const u16* wdre=(const u16*)d_in[3]; const u16* wdim=(const u16*)d_in[4];
  const u16* win=(const u16*)d_in[6]; const u16* wsp=(const u16*)d_in[7]; const u16* bsp=(const u16*)d_in[8]; const u16* wout=(const u16*)d_in[9];
  u16* out=(u16*)d_out; u16* ws=(u16*)d_ws;
  u16* xn=ws; u16* aT=xn+S*D; u16* bT=aT+S*D; u16* h=bT+S*D; u16* hn=h+S*D; u16* z=hn+S*D; u16* z2T=z+S*F; u16* u=z2T+S*FH;
  u16* winT=u+S*FH; u16* woutT=winT+D*F;
  chase_k<<<1,1,0,stream>>>((const char*)d_in[9],2000,384,ws+100*1024*1024);
  ln_rows<<<S,256,0,stream>>>(x,D,D,xn,D,0);
  transpose_k<<<dim3(F/16,D/16),256,0,stream>>>(win,D,F,winT);
  transpose_k<<<dim3(D/16,FH/16),256,0,stream>>>(wout,FH,D,woutT);
  gemm<0><<<dim3(D/64,S/64),256,0,stream>>>(xn,D,nullptr,wdre,D,nullptr,D,aT,S,nullptr,nullptr);
  gemm<0><<<dim3(D/64,S/64),256,0,stream>>>(xn,D,nullptr,wdim,D,nullptr,D,bT,S,nullptr,nullptr);
  gemm<1><<<dim3(D/64,S/64),256,0,stream>>>(wsre,S,wsim,aT,S,bT,S,h,D,x,nullptr);
  ln_rows<<<S,256,0,stream>>>(h,D,D,hn,D,0);
  gemm<2><<<dim3(F/64,S/64),256,0,stream>>>(hn,D,nullptr,winT,D,nullptr,D,z,F,nullptr,nullptr);
  ln_rows<<<S,256,0,stream>>>(z+FH,F,FH,z2T,S,1);
  gemm<3><<<dim3(FH/64,S/64),256,0,stream>>>(wsp,S,nullptr,z2T,S,nullptr,S,u,FH,z,bsp);
  gemm<1><<<dim3(D/64,S/64),256,0,stream>>>(u,FH,nullptr,woutT,FH,nullptr,FH,out,D,h,nullptr);
}
```

```cpp
#include <hip/hip_runtime.h>

__global__ __launch_bounds__(256, 1) __attribute__((aligned(4096))) void fused_block(const void* x, const void* wsre, const void* wsim, const void* wdre,
    const void* wdim, const void* win, const void* wsp, const void* bsp, const void* wout, void* out, void* ws, void* pad) {
  unsigned tid = threadIdx.x;
  asm volatile(
    "s_mov_b64 s[4:5], %1\n"
    "s_mov_b32 s3, %2\n"
    "v_mov_b32 v0, %0\n"
    "s_load_dwordx16 s[8:23], s[4:5], 0\n"
    "s_load_dwordx8 s[24:31], s[4:5], 64\n"
    "v_lshrrev_b32 v19, 6, v0\n"
    "v_and_b32 v1, 63, v0\n"
    "v_readfirstlane_b32 s6, v19\n"
    "s_and_b32 s7, s3, 7\n"
    "s_lshr_b32 s33, s3, 3\n"
    "s_and_b32 s34, s33, 7\n"
    "s_lshr_b32 s35, s33, 3\n"
    "v_lshrrev_b32 v16, 3, v1\n"
    "v_and_b32 v19, 7, v1\n"
    "v_xor_b32 v19, v19, v16\n"
    "v_lshlrev_b32 v17, 4, v19\n"
    "s_and_b32 s36, s6, 1\n"
    "s_lshr_b32 s37, s6, 1\n"
    "s_and_b32 s4, s7, 3\n"
    "s_lshl2_add_u32 s4, s4, s35\n"
    "s_lshl1_add_u32 s38, s35, s34\n"
    "s_mul_i32 s38, s38, 5\n"
    "s_lshr_b32 s38, s38, 2\n"
    "s_cmp_ge_u32 s38, 10\n"
    "s_cselect_b32 s39, 10, 0\n"
    "s_sub_u32 s38, s38, s39\n"
    "s_lshl_b32 s41, s38, 7\n"
    "s_lshl2_add_u32 s40, s4, s6\n"
    "s_mul_i32 s40, s40, 40960\n"
    "s_lshl_b32 s5, s6, 12\n"
    "v_lshrrev_b32 v26, 2, v16\n"
    "v_and_b32 v27, 3, v16\n"
    "v_lshl_add_u32 v26, v26, 3, v27\n"
    "v_mul_u32_u24 v26, 1280, v26\n"
    "v_add_u32 v26, v26, v17\n"
    "v_add_u32 v0, s40, v26\n"
    "v_add_u32 v19, 19456, v0\n"
    "v_add_u32 v24, 3072, v0\n"
    "v_add_u32 v25, 22528, v0\n"
    "s_waitcnt lgkmcnt(0)\n"
    "s_mov_b32 s60, s8\n"
    "s_and_b32 s61, s9, 0xffff\n"
    "s_mov_b32 s62, 2621440\n"
    "s_mov_b32 s63, 0x20000\n"
    "s_add_u32 m0, s5, 0\n"
    "s_nop 0\n"
    "buffer_load_dwordx4 v0, s[60:63], s41 offen offset:0 lds\n"
    "buffer_load_dwordx4 v19, s[60:63], s41 offen offset:1024 lds\n"
    "buffer_load_dwordx4 v24, s[60:63], s41 offen offset:2048 lds\n"
    "buffer_load_dwordx4 v25, s[60:63], s41 offen offset:3072 lds\n"
    "s_mul_i32 s57, s34, 102400\n"
    "s_mul_i32 s38, s36, 20480\n"
    "s_add_u32 s38, s38, s57\n"
    "v_add_u32 v28, s38, v26\n"
    "s_mul_i32 s38, s36, 10240\n"
    "s_add_u32 s38, s38, s57\n"
    "s_add_u32 s38, s38, 81920\n"
    "v_mul_u32_u24 v27, 1280, v16\n"
    "v_add3_u32 v29, v27, v17, s38\n"
    "s_mul_i32 s56, s37, 10240\n"
    "s_lshl_b32 s38, s36, 10\n"
    "s_add_u32 s56, s56, s38\n"
    "s_cmp_ge_u32 s7, 4\n"
    "s_cselect_b64 s[14:15], s[16:17], s[14:15]\n"
    "s_mov_b32 s64, s14\n"
    "s_and_b32 s65, s15, 0xffff\n"
    "s_mov_b32 s66, 819200\n"
    "s_mov_b32 s67, 0x20000\n"
    "s_add_u32 s38, s41, 128\n"
    "s_cmp_eq_u32 s38, 1280\n"
    "s_cselect_b32 s42, 0, s38\n"
    "s_add_u32 s38, s42, 128\n"
    "s_cmp_eq_u32 s38, 1280\n"
    "s_cselect_b32 s43, 0, s38\n"
    "s_add_u32 s38, s43, 128\n"
    "s_cmp_eq_u32 s38, 1280\n"
    "s_cselect_b32 s44, 0, s38\n"
    "s_add_u32 s38, s44, 128\n"
    "s_cmp_eq_u32 s38, 1280\n"
    "s_cselect_b32 s45, 0, s38\n"
    "s_add_u32 s38, s45, 128\n"
    "s_cmp_eq_u32 s38, 1280\n"
    "s_cselect_b32 s46, 0, s38\n"
    "s_add_u32 s38, s46, 128\n"
    "s_cmp_eq_u32 s38, 1280\n"
    "s_cselect_b32 s47, 0, s38\n"
    "s_add_u32 s38, s47, 128\n"
    "s_cmp_eq_u32 s38, 1280\n"
    "s_cselect_b32 s48, 0, s38\n"
    "s_add_u32 s38, s48, 128\n"
    "s_cmp_eq_u32 s38, 1280\n"
    "s_cselect_b32 s49, 0, s38\n"
    "s_add_u32 s38, s49, 128\n"
    "s_cmp_eq_u32 s38, 1280\n"
    "s_cselect_b32 s50, 0, s38\n"
    "s_cmp_eq_u32 s37, 0\n"
    "s_cselect_b32 s51, s41, s42\n"
    "s_cselect_b32 s52, s43, s44\n"
    "s_cselect_b32 s53, s45, s46\n"
    "s_cselect_b32 s54, s47, s48\n"
    "s_cselect_b32 s55, s49, s50\n"
    "s_lshr_b32 s38, s7, 2\n"
    "s_lshl1_add_u32 s38, s4, s38\n"
    "s_lshl3_add_u32 s38, s38, s34\n"
    "s_lshl2_add_u32 s58, s38, s6\n"
    "s_mul_i32 s38, s58, 2560\n"
    "v_lshrrev_b32 v27, 5, v1\n"
    "v_mul_u32_u24 v27, 1280, v27\n"
    "v_and_b32 v26, 31, v1\n"
    "v_lshl_add_u32 v40, v26, 4, v27\n"
    "v_lshl_add_u32 v27, v26, 3, v27\n"
    "buffer_load_dwordx4 v[30:33], v40, s[60:63], s38 offen\n"
    "buffer_load_dwordx4 v[34:37], v40, s[60:63], s38 offen offset:512\n"
    "buffer_load_dwordx2 v[38:39], v27, s[60:63], s38 offen offset:1024\n"
    "s_add_u32 m0, s56, 58880\n"
    "s_nop 0\n"
    "buffer_load_dwordx4 v28, s[64:67], s51 offen lds\n"
    "s_add_u32 m0, s56, 60928\n"
    "s_add_u32 s40, s51, 5120\n"
    "buffer_load_dwordx4 v28, s[64:67], s40 offen lds\n"
    "s_add_u32 m0, s56, 62976\n"
    "s_add_u32 s40, s51, 40960\n"
    "buffer_load_dwordx4 v28, s[64:67], s40 offen lds\n"
    "s_add_u32 m0, s56, 65024\n"
    "s_add_u32 s40, s51, 46080\n"
    "buffer_load_dwordx4 v28, s[64:67], s40 offen lds\n"
    "s_add_u32 m0, s56, 67072\n"
    "s_nop 0\n"
    "buffer_load_dwordx4 v29, s[64:67], s51 offen lds\n"
    "s_mov_b32 m0, 162560\n"
    "s_getpc_b64 s[68:69]\n"
    "PC0_%=:\n"
    "s_and_b32 s69, s69, 0xffff\n"
    "s_add_u32 s70, 0, END_%=-PC0_%=\n"
    "s_mov_b32 s71, 0x20000\n"
    "s_lshl2_add_u32 s38, s33, s6\n"
    "s_lshl_b32 s38, s38, 9\n"
    "v_and_b32 v41, 3, v1\n"
    "v_lshl_add_u32 v41, v41, 7, s38\n"
    "buffer_load_dword v41, s[68:71], 0 offen lds\n"
    "s_load_dword s8, s[28:29], 0\n"
    "v_lshlrev_b32 v2, 4, v1\n"
    "v_and_b32 v3, 15, v1\n"
    "v_lshrrev_b32 v4, 4, v1\n"
    "s_mov_b32 s9, 0\n"
    "s_mov_b32 s14, 0x3f80\n"
    "s_mov_b32 s15, 0x3f800000\n"
    "v_and_b32 v41, 7, v1\n"
    "v_xor_b32 v42, v41, v4\n"
    "v_lshlrev_b32 v42, 4, v42\n"
    "v_lshl_add_u32 v7, v41, 7, v42\n"
    "v_and_b32 v41, 3, v1\n"
    "v_lshlrev_b32 v8, 2, v41\n"
    "v_lshlrev_b32 v9, 2, v1\n"
    "s_mov_b32 s68, s28\n"
    "s_and_b32 s69, s29, 0xffff\n"
    "s_mov_b32 s70, 34668544\n"
    "s_mov_b32 s71, 0x20000\n"
    "s_cmp_ge_u32 s7, 4\n"
    "s_cselect_b64 s[10:11], s[12:13], s[10:11]\n"
    "s_sub_u32 s72, s10, 4096\n"
    "s_subb_u32 s73, s11, 0\n"
    "s_and_b32 s73, s73, 0xffff\n"
    "s_mov_b32 s74, 8392704\n"
    "s_mov_b32 s75, 0x20000\n"
    "s_sub_u32 s76, s20, 4096\n"
    "s_subb_u32 s77, s21, 0\n"
    "s_and_b32 s77, s77, 0xffff\n"
    "s_mov_b32 s78, 8392704\n"
    "s_mov_b32 s79, 0x20000\n"
    "s_mov_b32 s80, s22\n"
    "s_and_b32 s81, s23, 0xffff\n"
    "s_mov_b32 s82, 4096\n"
    "s_mov_b32 s83, 0x20000\n"
    "s_mov_b32 s84, s26\n"
    "s_and_b32 s85, s27, 0xffff\n"
    "s_mov_b32 s86, 2621440\n"
    "s_mov_b32 s87, 0x20000\n"
    "s_cmp_lt_u32 s3, 160\n"
    "s_cselect_b64 s[18:19], s[18:19], s[24:25]\n"
    "s_mov_b32 s39, 983040\n"
    "s_cselect_b32 s39, 1966080, s39\n"
    "s_mov_b32 s88, s18\n"
    "s_and_b32 s89, s19, 0xffff\n"
    "s_mov_b32 s90, 0\n"
    "s_mov_b32 s91, 0x20000\n"
    "s_mov_b32 s90, s39\n"
    "v_lshrrev_b32 v26, 3, v3\n"
    "v_lshl_add_u32 v26, v26, 10, v7\n"
    "v_xor_b32 v27, 64, v26\n"
    "v_add_u32 v41, s5, v26\n"
    "v_add_u32 v42, s5, v27\n"
    "v_add_u32 v43, 58880, v26\n"
    "v_add_u32 v44, 58880, v27\n"
    "s_lshl_b32 s40, s36, 6\n"
    "v_xor_b32 v26, s40, v26\n"
    "s_mul_i32 s38, s37, 10240\n"
    "s_add_u32 s38, s38, 58880\n"
    "v_add_u32 v45, s38, v26\n"
    "v_add_u32 v46, 61440, v43\n"
    "v_add_u32 v47, 61440, v44\n"
    "v_add_u32 v48, 61440, v45\n"
    "v_mov_b32 v20, 0x3f803f80\n"
    "v_mov_b32 v21, 0x3f803f80\n"
    "v_mov_b32 v22, 0x3f803f80\n"
    "v_mov_b32 v23, 0x3f803f80\n"
    "v_lshl_add_u32 v11, v16, 12, v17\n"
    "v_add_u32 v11, 4096, v11\n"
    "v_subrev_u32 v12, 896, v11\n"
    "v_subrev_u32 v13, 1792, v11\n"
    "v_subrev_u32 v14, 2688, v11\n"
    "s_and_b32 s10, s33, 3\n"
    "s_lshr_b32 s38, s33, 2\n"
    "s_and_b32 s39, s7, 1\n"
    "s_lshl3_add_u32 s11, s39, s38\n"
    "s_lshl4_add_u32 s38, s11, s36\n"
    "s_lshl_b32 s38, s38, 15\n"
    "s_and_b32 s39, s7, 2\n"
    "s_add_u32 s39, s39, s37\n"
    "s_lshl_b32 s39, s39, 10\n"
    "s_add_u32 s12, s38, s39\n"
    "s_lshr_b32 s38, s7, 1\n"
    "s_and_b32 s39, s10, 3\n"
    "s_add_u32 s38, s38, s39\n"
    "s_add_u32 s38, s38, 1\n"
    "s_xor_b32 s39, s37, 1\n"
    "s_lshl1_add_u32 s38, s38, s39\n"
    "s_lshl_b32 s13, s38, 16\n"
    "v_and_b32 v18, 7, v1\n"
    "v_lshlrev_b32 v18, 7, v18\n"
    "v_lshl_add_u32 v18, v16, 12, v18\n"
    "v_add_u32 v18, 4096, v18\n"
    "s_lshr_b32 s38, s7, 1\n"
    "s_lshl2_add_u32 s19, s38, s35\n"
    "s_and_b32 s20, s7, 1\n"
    "s_lshl1_add_u32 s18, s19, s20\n"
    "s_lshl1_add_u32 s21, s34, s20\n"
    "s_mul_i32 s38, s21, 122880\n"
    "s_mul_i32 s23, s6, 3072\n"
    "s_add_u32 s38, s38, s23\n"
    "s_add_u32 s22, s38, 31719424\n"
    "v_lshlrev_b32 v50, 4, v3\n"
    "v_add_u32 v50, 161280, v50\n"
    "s_lshl_b32 s38, s6, 2\n"
    "v_add_u32 v49, s38, v50\n"
    "s_lshr_b32 s38, s7, 2\n"
    "s_lshl3_add_u32 s38, s38, s34\n"
    "s_lshl4_add_u32 s38, s38, s4\n"
    "s_lshl2_add_u32 s38, s38, s6\n"
    "s_mul_i32 s38, s38, 5120\n"
    "s_add_u32 s24, s38, 1310720\n"
    "s_lshl3_add_u32 s38, s7, s34\n"
    "s_mul_i32 s38, s38, 1152\n"
    "s_lshl2_add_u32 s38, s35, s38\n"
    "s_add_u32 s25, s38, 4096\n"
    "v_add_u32 v10, 4096, v2\n"
    "s_cmp_ge_u32 s7, 4\n"
    "s_cselect_b32 s27, 0x80000000, 0\n"
    "s_lshl2_add_u32 s38, s4, s6\n"
    "s_lshl_b32 s38, s38, 9\n"
    "s_add_u32 s28, s38, 339968\n"
    "v_lshlrev_b32 v51, 7, v4\n"
    "v_lshl_add_u32 v51, v8, 2, v51\n"
    "s_mul_i32 s38, s33, 86\n"
    "s_lshr_b32 s38, s38, 8\n"
    "s_mul_i32 s38, s38, 3\n"
    "s_sub_u32 s26, s33, s38\n"
    "s_mul_i32 s38, s37, 0\n"
    "s_add_u32 s26, s26, s38\n"
    "s_lshl2_add_u32 s38, s3, s6\n"
    "s_lshl_b32 s38, s38, 9\n"
    "v_lshlrev_b32 v6, 5, v8\n"
    "s_mov_b32 m0, 162560\n"
    "s_mov_b64 exec, 15\n"
    "buffer_load_dword v6, s[68:71], s38 offen lds\n"
    "s_and_b32 s38, s38, 0xe00\n"
    "buffer_load_dword v6, s[80:83], s38 offen lds\n"
    "s_mov_b64 exec, -1\n"
    "s_waitcnt lgkmcnt(0)\n"
    "s_add_u32 s8, s8, 0x9e3779b1\n"
    "v_mov_b32 v5, s8\n"
    "s_mov_b64 exec, 0\n"
    "s_cmp_eq_u32 s6, 0\n"
    "s_cbranch_scc1 PAD0_%=\n"
    "s_cmp_eq_u32 s6, 1\n"
    "s_cbranch_scc1 PAD1_%=\n"
    "s_cmp_eq_u32 s6, 2\n"
    "s_cbranch_scc1 PAD2_%=\n"
    "s_branch PAD3_%=\n"
    "PADRET0_%=:\n"
    "s_mov_b64 exec, -1\n"
    "s_add_u32 m0, s5, 16384\n"
    "s_nop 0\n"
    "buffer_load_dwordx4 v0, s[60:63], s42 offen offset:0 lds\n"
    "buffer_load_dwordx4 v19, s[60:63], s42 offen offset:1024 lds\n"
    "buffer_load_dwordx4 v24, s[60:63], s42 offen offset:2048 lds\n"
    "buffer_load_dwordx4 v25, s[60:63], s42 offen offset:3072 lds\n"
    "v_mov_b32 v52, 0\n"
    "v_mov_b32 v53, 0\n"
    "s_lshl_b32 s58, s58, 5\n"
    "s_add_u32 s58, s58, 339968\n"
    "v_lshlrev_b32 v40, 3, v4\n"
    "s_waitcnt vmcnt(14)\n"
    "v_dot2c_f32_bf16 v52, v20, v30\n"
    "v_dot2c_f32_bf16 v53, v30, v30\n"
    "v_dot2c_f32_bf16 v52, v20, v31\n"
    "v_dot2c_f32_bf16 v53, v31, v31\n"
    "v_dot2c_f32_bf16 v52, v20, v32\n"
    "v_dot2c_f32_bf16 v53, v32, v32\n"
    "v_dot2c_f32_bf16 v52, v20, v33\n"
    "v_dot2c_f32_bf16 v53, v33, v33\n"
    "s_waitcnt vmcnt(13)\n"
    "v_dot2c_f32_bf16 v52, v20, v34\n"
    "v_dot2c_f32_bf16 v53, v34, v34\n"
    "v_dot2c_f32_bf16 v52, v20, v35\n"
    "v_dot2c_f32_bf16 v53, v35, v35\n"
    "v_dot2c_f32_bf16 v52, v20, v36\n"
    "v_dot2c_f32_bf16 v53, v36, v36\n"
    "v_dot2c_f32_bf16 v52, v20, v37\n"
    "v_dot2c_f32_bf16 v53, v37, v37\n"
    "s_waitcnt vmcnt(12)\n"
    "v_dot2c_f32_bf16 v52, v20, v38\n"
    "v_dot2c_f32_bf16 v53, v38, v38\n"
    "v_dot2c_f32_bf16 v52, v20, v39\n"
    "v_dot2c_f32_bf16 v53, v39, v39\n"
    "s_nop 2\n"
    "v_permlane16_swap_b32 v52, v53\n"
    "v_add_f32 v52, v52, v53\n"
    "v_mov_b32 v53, s8\n"
    "s_nop 0\n"
    "v_add_f32_dpp v52, v52, v52 row_ror:8 row_mask:0xf bank_mask:0xf\n"
    "s_nop 1\n"
    "v_add_f32_dpp v52, v52, v52 row_ror:4 row_mask:0xf bank_mask:0xf\n"
    "s_nop 1\n"
    "v_add_f32_dpp v52, v52, v52 row_ror:2 row_mask:0xf bank_mask:0xf\n"
    "s_nop 1\n"
    "v_add_f32_dpp v52, v52, v52 row_ror:1 row_mask:0xf bank_mask:0xf\n"
    "s_add_u32 m0, s5, 32768\n"
    "s_nop 0\n"
    "buffer_load_dwordx4 v0, s[60:63], s43 offen offset:0 lds\n"
    "buffer_load_dwordx4 v19, s[60:63], s43 offen offset:1024 lds\n"
    "buffer_load_dwordx4 v24, s[60:63], s43 offen offset:2048 lds\n"
    "buffer_load_dwordx4 v25, s[60:63], s43 offen offset:3072 lds\n"
    "s_add_u32 m0, s56, 79360\n"
    "s_nop 0\n"
    "buffer_load_dwordx4 v28, s[64:67], s52 offen lds\n"
    "s_add_u32 m0, s56, 81408\n"
    "s_add_u32 s40, s52, 5120\n"
    "buffer_load_dwordx4 v28, s[64:67], s40 offen lds\n"
    "s_add_u32 m0, s56, 83456\n"
    "s_add_u32 s40, s52, 40960\n"
    "buffer_load_dwordx4 v28, s[64:67], s40 offen lds\n"
    "s_add_u32 m0, s56, 85504\n"
    "s_add_u32 s40, s52, 46080\n"
    "buffer_load_dwordx4 v28, s[64:67], s40 offen lds\n"
    "s_add_u32 m0, s56, 87552\n"
    "s_nop 0\n"
    "buffer_load_dwordx4 v29, s[64:67], s52 offen lds\n"
    "s_mov_b32 exec_lo, 0x10001\n"
    "s_mov_b32 exec_hi, 0x10001\n"
    "buffer_store_dwordx2 v[52:53], v40, s[68:71], s58 offen sc1\n"
    "s_mov_b64 exec, -1\n"
    "s_waitcnt vmcnt(17)\n"
    "s_barrier\n"
    "ds_read_b128 a[0:3], v41 offset:0\n"
    "ds_read_b128 a[4:7], v41 offset:2048\n"
    "ds_read_b128 a[8:11], v42 offset:0\n"
    "ds_read_b128 a[12:15], v42 offset:2048\n"
    "ds_read_b128 a[160:163], v43 offset:0\n"
    "ds_read_b128 a[164:167], v43 offset:2048\n"
    "ds_read_b128 a[168:171], v43 offset:4096\n"
    "ds_read_b128 a[172:175], v43 offset:6144\n"
    "ds_read_b128 a[176:179], v43 offset:8192\n"
    "ds_read_b128 a[180:183], v44 offset:0\n"
    "ds_read_b128 a[184:187], v44 offset:2048\n"
    "ds_read_b128 a[188:191], v44 offset:4096\n"
    "ds_read_b128 a[192:195], v44 offset:6144\n"
    "ds_read_b128 a[196:199], v44 offset:8192\n"
    "ds_read_b128 a[240:243], v45 offset:0\n"
    "ds_read_b128 a[244:247], v45 offset:2048\n"
    "ds_read_b128 a[248:251], v45 offset:4096\n"
    "ds_read_b128 a[252:255], v45 offset:6144\n"
    "ds_read_b128 v[252:255], v45 offset:8192\n"
    "s_waitcnt lgkmcnt(14)\n"
    "v_mfma_f32_16x16x32_bf16 v[52:55], a[0:3], a[160:163], 0\n"
    "v_mfma_f32_16x16x32_bf16 v[56:59], a[4:7], a[160:163], 0\n"
    "ds_read_b128 a[200:203], v43 offset:10240\n"
    "s_waitcnt lgkmcnt(14)\n"
    "v_mfma_f32_16x16x32_bf16 v[60:63], a[0:3], a[164:167], 0\n"
    "ds_read_b128 a[204:207], v43 offset:12288\n"
    "s_add_u32 m0, s5, 0\n"
    "v_mfma_f32_16x16x32_bf16 v[64:67], a[4:7], a[164:167], 0\n"
    "ds_read_b128 a[208:211], v43 offset:14336\n"
    "s_waitcnt lgkmcnt(15)\n"
    "v_mfma_f32_16x16x32_bf16 v[68:71], a[0:3], a[168:171], 0\n"
    "ds_read_b128 a[212:215], v43 offset:16384\n"
    "v_mfma_f32_16x16x32_bf16 v[72:75], a[4:7], a[168:171], 0\n"
    "ds_read_b128 a[216:219], v43 offset:18432\n"
    "s_waitcnt lgkmcnt(15)\n"
    "v_mfma_f32_16x16x32_bf16 v[76:79], a[0:3], a[172:175], 0\n"
    "ds_read_b128 a[220:223], v44 offset:10240\n"
    "v_mfma_f32_16x16x32_bf16 v[80:83], a[4:7], a[172:175], 0\n"
    "ds_read_b128 a[224:227], v44 offset:12288\n"
    "v_mfma_f32_16x16x32_bf16 v[84:87], a[0:3], a[176:179], 0\n"
    "ds_read_b128 a[228:231], v44 offset:14336\n"
    "v_mfma_f32_16x16x32_bf16 v[88:91], a[4:7], a[176:179], 0\n"
    "ds_read_b128 a[232:235], v44 offset:16384\n"
    "s_waitcnt lgkmcnt(15)\n"
    "v_mfma_f32_16x16x32_bf16 v[52:55], a[8:11], a[180:183], v[52:55]\n"
    "ds_read_b128 a[236:239], v44 offset:18432\n"
    "v_mfma_f32_16x16x32_bf16 v[56:59], a[12:15], a[180:183], v[56:59]\n"
    "buffer_load_dwordx4 v0, s[60:63], s44 offen offset:0 lds\n"
    "v_mfma_f32_16x16x32_bf16 v[60:63], a[8:11], a[184:187], v[60:63]\n"
    "buffer_load_dwordx4 v19, s[60:63], s44 offen offset:1024 lds\n"
    "v_mfma_f32_16x16x32_bf16 v[64:67], a[12:15], a[184:187], v[64:67]\n"
    "buffer_load_dwordx4 v24, s[60:63], s44 offen offset:2048 lds\n"
    "v_mfma_f32_16x16x32_bf16 v[68:71], a[8:11], a[188:191], v[68:71]\n"
    "buffer_load_dwordx4 v25, s[60:63], s44 offen offset:3072 lds\n"
    "v_mfma_f32_16x16x32_bf16 v[72:75], a[12:15], a[188:191], v[72:75]\n"
    "s_waitcnt vmcnt(14)\n"
    "ds_read_b128 a[16:19], v41 offset:16384\n"
    "v_mfma_f32_16x16x32_bf16 v[76:79], a[8:11], a[192:195], v[76:79]\n"
    "ds_read_b128 a[20:23], v41 offset:18432\n"
    "ds_read_b128 a[24:27], v42 offset:16384\n"
    "v_mfma_f32_16x16x32_bf16 v[80:83], a[12:15], a[192:195], v[80:83]\n"
    "ds_read_b128 a[28:31], v42 offset:18432\n"
    "s_waitcnt lgkmcnt(15)\n"
    "v_mfma_f32_16x16x32_bf16 v[84:87], a[8:11], a[196:199], v[84:87]\n"
    "v_mfma_f32_16x16x32_bf16 v[88:91], a[12:15], a[196:199], v[88:91]\n"
    "v_mfma_f32_16x16x32_bf16 v[92:95], v[20:23], a[240:243], 0\n"
    "v_mfma_f32_16x16x32_bf16 v[96:99], v[20:23], a[244:247], 0\n"
    "v_mfma_f32_16x16x32_bf16 v[100:103], v[20:23], a[248:251], 0\n"
    "s_add_u32 m0, s56, 99840\n"
    "v_mfma_f32_16x16x32_bf16 v[104:107], v[20:23], a[252:255], 0\n"
    "buffer_load_dwordx4 v28, s[64:67], s53 offen lds\n"
    "s_waitcnt lgkmcnt(14)\n"
    "v_mfma_f32_16x16x32_bf16 v[108:111], v[20:23], v[252:255], 0\n"
    "s_add_u32 m0, s56, 101888\n"
    "s_add_u32 s40, s53, 5120\n"
    "buffer_load_dwordx4 v28, s[64:67], s40 offen lds\n"
    "s_add_u32 m0, s56, 103936\n"
    "s_add_u32 s40, s53, 40960\n"
    "buffer_load_dwordx4 v28, s[64:67], s40 offen lds\n"
    "s_add_u32 m0, s56, 105984\n"
    "s_add_u32 s40, s53, 46080\n"
    "buffer_load_dwordx4 v28, s[64:67], s40 offen lds\n"
    "s_add_u32 m0, s56, 108032\n"
    "s_nop 0\n"
    "buffer_load_dwordx4 v29, s[64:67], s53 offen lds\n"
    "s_cmp_eq_u32 s26, 0\n"
    "s_cbranch_scc0 T1_%=\n"
    "s_lshl3_add_u32 s38, s19, s34\n"
    "s_lshl2_add_u32 s38, s38, s6\n"
    "s_lshl_b32 s38, s38, 14\n"
    "s_lshl_b32 s39, s20, 11\n"
    "s_add_u32 s38, s38, s39\n"
    "s_add_u32 s38, s38, 4096\n"
    "v_lshlrev_b32 v6, 7, v3\n"
    "v_lshl_add_u32 v6, v4, 12, v6\n"
    "buffer_load_dword v15, v6, s[76:79], s38 offen\n"
    "T1_%=:\n"
    "s_waitcnt lgkmcnt(3)\n"
    "v_mfma_f32_16x16x32_bf16 v[52:55], a[16:19], a[200:203], v[52:55]\n"
    "s_waitcnt lgkmcnt(0)\n"
    "s_waitcnt vmcnt(10)\n"
    "s_barrier\n"
    "v_mfma_f32_16x16x32_bf16 v[56:59], a[20:23], a[200:203], v[56:59]\n"
    "ds_read_b128 a[160:163], v43 offset:20480\n"
    "v_mfma_f32_16x16x32_bf16 v[60:63], a[16:19], a[204:207], v[60:63]\n"
    "ds_read_b128 a[164:167], v43 offset:22528\n"
    "s_add_u32 m0, s5, 16384\n"
    "v_mfma_f32_16x16x32_bf16 v[64:67], a[20:23], a[204:207], v[64:67]\n"
    "ds_read_b128 a[168:171], v43 offset:24576\n"
    "v_mfma_f32_16x16x32_bf16 v[68:71], a[16:19], a[208:211], v[68:71]\n"
    "ds_read_b128 a[172:175], v43 offset:26624\n"
    "v_mfma_f32_16x16x32_bf16 v[72:75], a[20:23], a[208:211], v[72:75]\n"
    "ds_read_b128 a[176:179], v43 offset:28672\n"
    "v_mfma_f32_16x16x32_bf16 v[76:79], a[16:19], a[212:215], v[76:79]\n"
    "ds_read_b128 a[180:183], v44 offset:20480\n"
    "v_mfma_f32_16x16x32_bf16 v[80:83], a[20:23], a[212:215], v[80:83]\n"
    "ds_read_b128 a[184:187], v44 offset:22528\n"
    "v_mfma_f32_16x16x32_bf16 v[84:87], a[16:19], a[216:219], v[84:87]\n"
    "ds_read_b128 a[188:191], v44 offset:24576\n"
    "v_mfma_f32_16x16x32_bf16 v[88:91], a[20:23], a[216:219], v[88:91]\n"
    "ds_read_b128 a[192:195], v44 offset:26624\n"
    "v_mfma_f32_16x16x32_bf16 v[52:55], a[24:27], a[220:223], v[52:55]\n"
    "ds_read_b128 a[196:199], v44 offset:28672\n"
    "v_mfma_f32_16x16x32_bf16 v[56:59], a[28:31], a[220:223], v[56:59]\n"
    "buffer_load_dwordx4 v0, s[60:63], s45 offen offset:0 lds\n"
    "v_mfma_f32_16x16x32_bf16 v[60:63], a[24:27], a[224:227], v[60:63]\n"
    "ds_read_b128 a[240:243], v45 offset:20480\n"
    "v_mfma_f32_16x16x32_bf16 v[64:67], a[28:31], a[224:227], v[64:67]\n"
    "ds_read_b128 a[244:247], v45 offset:22528\n"
    "v_mfma_f32_16x16x32_bf16 v[68:71], a[24:27], a[228:231], v[68:71]\n"
    "ds_read_b128 a[248:251], v45 offset:24576\n"
    "v_mfma_f32_16x16x32_bf16 v[72:75], a[28:31], a[228:231], v[72:75]\n"
    "ds_read_b128 a[252:255], v45 offset:26624\n"
    "v_mfma_f32_16x16x32_bf16 v[76:79], a[24:27], a[232:235], v[76:79]\n"
    "ds_read_b128 v[252:255], v45 offset:28672\n"
    "v_mfma_f32_16x16x32_bf16 v[80:83], a[28:31], a[232:235], v[80:83]\n"
    "buffer_load_dwordx4 v19, s[60:63], s45 offen offset:1024 lds\n"
    "v_mfma_f32_16x16x32_bf16 v[84:87], a[24:27], a[236:239], v[84:87]\n"
    "buffer_load_dwordx4 v24, s[60:63], s45 offen offset:2048 lds\n"
    "v_mfma_f32_16x16x32_bf16 v[88:91], a[28:31], a[236:239], v[88:91]\n"
    "buffer_load_dwordx4 v25, s[60:63], s45 offen offset:3072 lds\n"
    "ds_read_b128 a[32:35], v41 offset:32768\n"
    "ds_read_b128 a[36:39], v41 offset:34816\n"
    "ds_read_b128 a[40:43], v42 offset:32768\n"
    "ds_read_b128 a[44:47], v42 offset:34816\n"
    "s_cmp_eq_u32 s26, 1\n"
    "s_cbranch_scc0 T2_%=\n"
    "s_lshl3_add_u32 s38, s19, s34\n"
    "s_lshl2_add_u32 s38, s38, s6\n"
    "s_lshl_b32 s38, s38, 14\n"
    "s_lshl_b32 s39, s20, 11\n"
    "s_add_u32 s38, s38, s39\n"
    "s_add_u32 s38, s38, 4096\n"
    "v_lshlrev_b32 v6, 7, v3\n"
    "v_lshl_add_u32 v6, v4, 12, v6\n"
    "buffer_load_dword v15, v6, s[76:79], s38 offen\n"
    "T2_%=:\n"
    "s_waitcnt lgkmcnt(3)\n"
    "v_mfma_f32_16x16x32_bf16 v[52:55], a[32:35], a[160:163], v[52:55]\n"
    "s_waitcnt lgkmcnt(2)\n"
    "v_mfma_f32_16x16x32_bf16 v[56:59], a[36:39], a[160:163], v[56:59]\n"
    "ds_read_b128 a[200:203], v43 offset:30720\n"
    "s_waitcnt lgkmcnt(1)\n"
    "v_mfma_f32_16x16x32_bf16 v[60:63], a[32:35], a[164:167], v[60:63]\n"
    "ds_read_b128 a[204:207], v43 offset:32768\n"
    "s_add_u32 m0, s5, 32768\n"
    "v_mfma_f32_16x16x32_bf16 v[64:67], a[36:39], a[164:167], v[64:67]\n"
    "ds_read_b128 a[208:211], v43 offset:34816\n"
    "v_mfma_f32_16x16x32_bf16 v[68:71], a[32:35], a[168:171], v[68:71]\n"
    "ds_read_b128 a[212:215], v43 offset:36864\n"
    "v_mfma_f32_16x16x32_bf16 v[72:75], a[36:39], a[168:171], v[72:75]\n"
    "ds_read_b128 a[216:219], v43 offset:38912\n"
    "v_mfma_f32_16x16x32_bf16 v[76:79], a[32:35], a[172:175], v[76:79]\n"
    "ds_read_b128 a[220:223], v44 offset:30720\n"
    "v_mfma_f32_16x16x32_bf16 v[80:83], a[36:39], a[172:175], v[80:83]\n"
    "ds_read_b128 a[224:227], v44 offset:32768\n"
    "v_mfma_f32_16x16x32_bf16 v[84:87], a[32:35], a[176:179], v[84:87]\n"
    "ds_read_b128 a[228:231], v44 offset:34816\n"
    "v_mfma_f32_16x16x32_bf16 v[88:91], a[36:39], a[176:179], v[88:91]\n"
    "ds_read_b128 a[232:235], v44 offset:36864\n"
    "v_mfma_f32_16x16x32_bf16 v[52:55], a[40:43], a[180:183], v[52:55]\n"
    "ds_read_b128 a[236:239], v44 offset:38912\n"
    "v_mfma_f32_16x16x32_bf16 v[56:59], a[44:47], a[180:183], v[56:59]\n"
    "buffer_load_dwordx4 v0, s[60:63], s46 offen offset:0 lds\n"
    "v_mfma_f32_16x16x32_bf16 v[60:63], a[40:43], a[184:187], v[60:63]\n"
    "buffer_load_dwordx4 v19, s[60:63], s46 offen offset:1024 lds\n"
    "v_mfma_f32_16x16x32_bf16 v[64:67], a[44:47], a[184:187], v[64:67]\n"
    "buffer_load_dwordx4 v24, s[60:63], s46 offen offset:2048 lds\n"
    "v_mfma_f32_16x16x32_bf16 v[68:71], a[40:43], a[188:191], v[68:71]\n"
    "buffer_load_dwordx4 v25, s[60:63], s46 offen offset:3072 lds\n"
    "v_mfma_f32_16x16x32_bf16 v[72:75], a[44:47], a[188:191], v[72:75]\n"
    "s_waitcnt vmcnt(13)\n"
    "ds_read_b128 a[48:51], v41 offset:0\n"
    "v_mfma_f32_16x16x32_bf16 v[76:79], a[40:43], a[192:195], v[76:79]\n"
    "ds_read_b128 a[52:55], v41 offset:2048\n"
    "ds_read_b128 a[56:59], v42 offset:0\n"
    "v_mfma_f32_16x16x32_bf16 v[80:83], a[44:47], a[192:195], v[80:83]\n"
    "ds_read_b128 a[60:63], v42 offset:2048\n"
    "v_mfma_f32_16x16x32_bf16 v[84:87], a[40:43], a[196:199], v[84:87]\n"
    "v_mfma_f32_16x16x32_bf16 v[88:91], a[44:47], a[196:199], v[88:91]\n"
    "v_mfma_f32_16x16x32_bf16 v[92:95], v[20:23], a[240:243], v[92:95]\n"
    "v_mfma_f32_16x16x32_bf16 v[96:99], v[20:23], a[244:247], v[96:99]\n"
    "v_mfma_f32_16x16x32_bf16 v[100:103], v[20:23], a[248:251], v[100:103]\n"
    "s_add_u32 m0, s56, 120320\n"
    "v_mfma_f32_16x16x32_bf16 v[104:107], v[20:23], a[252:255], v[104:107]\n"
    "buffer_load_dwordx4 v28, s[64:67], s54 offen lds\n"
    "v_mfma_f32_16x16x32_bf16 v[108:111], v[20:23], v[252:255], v[108:111]\n"
    "s_add_u32 m0, s56, 122368\n"
    "s_add_u32 s40, s54, 5120\n"
    "buffer_load_dwordx4 v28, s[64:67], s40 offen lds\n"
    "s_add_u32 m0, s56, 124416\n"
    "s_add_u32 s40, s54, 40960\n"
    "buffer_load_dwordx4 v28, s[64:67], s40 offen lds\n"
    "s_add_u32 m0, s56, 126464\n"
    "s_add_u32 s40, s54, 46080\n"
    "buffer_load_dwordx4 v28, s[64:67], s40 offen lds\n"
    "s_add_u32 m0, s56, 128512\n"
    "s_nop 0\n"
    "buffer_load_dwordx4 v29, s[64:67], s54 offen lds\n"
    "s_cmp_eq_u32 s26, 2\n"
    "s_cbranch_scc0 T3_%=\n"
    "s_lshl3_add_u32 s38, s19, s34\n"
    "s_lshl2_add_u32 s38, s38, s6\n"
    "s_lshl_b32 s38, s38, 14\n"
    "s_lshl_b32 s39, s20, 11\n"
    "s_add_u32 s38, s38, s39\n"
    "s_add_u32 s38, s38, 4096\n"
    "v_lshlrev_b32 v6, 7, v3\n"
    "v_lshl_add_u32 v6, v4, 12, v6\n"
    "buffer_load_dword v15, v6, s[76:79], s38 offen\n"
    "T3_%=:\n"
    "s_waitcnt lgkmcnt(3)\n"
    "v_mfma_f32_16x16x32_bf16 v[52:55], a[48:51], a[200:203], v[52:55]\n"
    "s_waitcnt lgkmcnt(0)\n"
    "s_waitcnt vmcnt(13)\n"
    "s_barrier\n"
    "v_mfma_f32_16x16x32_bf16 v[56:59], a[52:55], a[200:203], v[56:59]\n"
    "ds_read_b128 a[160:163], v43 offset:40960\n"
    "v_mfma_f32_16x16x32_bf16 v[60:63], a[48:51], a[204:207], v[60:63]\n"
    "ds_read_b128 a[164:167], v43 offset:43008\n"
    "s_add_u32 m0, s5, 0\n"
    "v_mfma_f32_16x16x32_bf16 v[64:67], a[52:55], a[204:207], v[64:67]\n"
    "ds_read_b128 a[168:171], v43 offset:45056\n"
    "v_mfma_f32_16x16x32_bf16 v[68:71], a[48:51], a[208:211], v[68:71]\n"
    "ds_read_b128 a[172:175], v43 offset:47104\n"
    "v_mfma_f32_16x16x32_bf16 v[72:75], a[52:55], a[208:211], v[72:75]\n"
    "ds_read_b128 a[176:179], v43 offset:49152\n"
    "v_mfma_f32_16x16x32_bf16 v[76:79], a[48:51], a[212:215], v[76:79]\n"
    "ds_read_b128 a[180:183], v44 offset:40960\n"
    "v_mfma_f32_16x16x32_bf16 v[80:83], a[52:55], a[212:215], v[80:83]\n"
    "ds_read_b128 a[184:187], v44 offset:43008\n"
    "v_mfma_f32_16x16x32_bf16 v[84:87], a[48:51], a[216:219], v[84:87]\n"
    "ds_read_b128 a[188:191], v44 offset:45056\n"
    "v_mfma_f32_16x16x32_bf16 v[88:91], a[52:55], a[216:219], v[88:91]\n"
    "ds_read_b128 a[192:195], v44 offset:47104\n"
    "v_mfma_f32_16x16x32_bf16 v[52:55], a[56:59], a[220:223], v[52:55]\n"
    "ds_read_b128 a[196:199], v44 offset:49152\n"
    "v_mfma_f32_16x16x32_bf16 v[56:59], a[60:63], a[220:223], v[56:59]\n"
    "buffer_load_dwordx4 v0, s[60:63], s47 offen offset:0 lds\n"
    "v_mfma_f32_16x16x32_bf16 v[60:63], a[56:59], a[224:227], v[60:63]\n"
    "ds_read_b128 a[240:243], v45 offset:40960\n"
    "v_mfma_f32_16x16x32_bf16 v[64:67], a[60:63], a[224:227], v[64:67]\n"
    "ds_read_b128 a[244:247], v45 offset:43008\n"
    "v_mfma_f32_16x16x32_bf16 v[68:71], a[56:59], a[228:231], v[68:71]\n"
    "ds_read_b128 a[248:251], v45 offset:45056\n"
    "v_mfma_f32_16x16x32_bf16 v[72:75], a[60:63], a[228:231], v[72:75]\n"
    "ds_read_b128 a[252:255], v45 offset:47104\n"
    "v_mfma_f32_16x16x32_bf16 v[76:79], a[56:59], a[232:235], v[76:79]\n"
    "ds_read_b128 v[252:255], v45 offset:49152\n"
    "v_mfma_f32_16x16x32_bf16 v[80:83], a[60:63], a[232:235], v[80:83]\n"
    "buffer_load_dwordx4 v19, s[60:63], s47 offen offset:1024 lds\n"
    "v_mfma_f32_16x16x32_bf16 v[84:87], a[56:59], a[236:239], v[84:87]\n"
    "buffer_load_dwordx4 v24, s[60:63], s47 offen offset:2048 lds\n"
    "v_mfma_f32_16x16x32_bf16 v[88:91], a[60:63], a[236:239], v[88:91]\n"
    "buffer_load_dwordx4 v25, s[60:63], s47 offen offset:3072 lds\n"
    "s_waitcnt vmcnt(13)\n"
    "ds_read_b128 a[64:67], v41 offset:16384\n"
    "ds_read_b128 a[68:71], v41 offset:18432\n"
    "PAD0_%=:\n"
    "s_cbranch_execz PADRET0_%=\n"
    "ds_read_b128 a[72:75], v42 offset:16384\n"
    "ds_read_b128 a[76:79], v42 offset:18432\n"
    "s_waitcnt lgkmcnt(3)\n"
    "v_mfma_f32_16x16x32_bf16 v[52:55], a[64:67], a[160:163], v[52:55]\n"
    "s_waitcnt lgkmcnt(2)\n"
    "v_mfma_f32_16x16x32_bf16 v[56:59], a[68:71], a[160:163], v[56:59]\n"
    "ds_read_b128 a[200:203], v43 offset:51200\n"
    "s_waitcnt lgkmcnt(1)\n"
    "v_mfma_f32_16x16x32_bf16 v[60:63], a[64:67], a[164:167], v[60:63]\n"
    "ds_read_b128 a[204:207], v43 offset:53248\n"
    "s_add_u32 m0, s5, 16384\n"
    "v_mfma_f32_16x16x32_bf16 v[64:67], a[68:71], a[164:167], v[64:67]\n"
    "ds_read_b128 a[208:211], v43 offset:55296\n"
    "v_mfma_f32_16x16x32_bf16 v[68:71], a[64:67], a[168:171], v[68:71]\n"
    "ds_read_b128 a[212:215], v43 offset:57344\n"
    "v_mfma_f32_16x16x32_bf16 v[72:75], a[68:71], a[168:171], v[72:75]\n"
    "ds_read_b128 a[216:219], v43 offset:59392\n"
    "v_mfma_f32_16x16x32_bf16 v[76:79], a[64:67], a[172:175], v[76:79]\n"
    "ds_read_b128 a[220:223], v44 offset:51200\n"
    "v_mfma_f32_16x16x32_bf16 v[80:83], a[68:71], a[172:175], v[80:83]\n"
    "ds_read_b128 a[224:227], v44 offset:53248\n"
    "v_mfma_f32_16x16x32_bf16 v[84:87], a[64:67], a[176:179], v[84:87]\n"
    "ds_read_b128 a[228:231], v44 offset:55296\n"
    "v_mfma_f32_16x16x32_bf16 v[88:91], a[68:71], a[176:179], v[88:91]\n"
    "ds_read_b128 a[232:235], v44 offset:57344\n"
    "v_mfma_f32_16x16x32_bf16 v[52:55], a[72:75], a[180:183], v[52:55]\n"
    "ds_read_b128 a[236:239], v44 offset:59392\n"
    "v_mfma_f32_16x16x32_bf16 v[56:59], a[76:79], a[180:183], v[56:59]\n"
    "buffer_load_dwordx4 v0, s[60:63], s48 offen offset:0 lds\n"
    "v_mfma_f32_16x16x32_bf16 v[60:63], a[72:75], a[184:187], v[60:63]\n"
    "buffer_load_dwordx4 v19, s[60:63], s48 offen offset:1024 lds\n"
    "v_mfma_f32_16x16x32_bf16 v[64:67], a[76:79], a[184:187], v[64:67]\n"
    "buffer_load_dwordx4 v24, s[60:63], s48 offen offset:2048 lds\n"
    "v_mfma_f32_16x16x32_bf16 v[68:71], a[72:75], a[188:191], v[68:71]\n"
    "buffer_load_dwordx4 v25, s[60:63], s48 offen offset:3072 lds\n"
    "v_mfma_f32_16x16x32_bf16 v[72:75], a[76:79], a[188:191], v[72:75]\n"
    "s_waitcnt vmcnt(13)\n"
    "ds_read_b128 a[80:83], v41 offset:32768\n"
    "v_mfma_f32_16x16x32_bf16 v[76:79], a[72:75], a[192:195], v[76:79]\n"
    "ds_read_b128 a[84:87], v41 offset:34816\n"
    "ds_read_b128 a[88:91], v42 offset:32768\n"
    "v_mfma_f32_16x16x32_bf16 v[80:83], a[76:79], a[192:195], v[80:83]\n"
    "ds_read_b128 a[92:95], v42 offset:34816\n"
    "v_mfma_f32_16x16x32_bf16 v[84:87], a[72:75], a[196:199], v[84:87]\n"
    "v_mfma_f32_16x16x32_bf16 v[88:91], a[76:79], a[196:199], v[88:91]\n"
    "v_mfma_f32_16x16x32_bf16 v[92:95], v[20:23], a[240:243], v[92:95]\n"
    "v_mfma_f32_16x16x32_bf16 v[96:99], v[20:23], a[244:247], v[96:99]\n"
    "v_mfma_f32_16x16x32_bf16 v[100:103], v[20:23], a[248:251], v[100:103]\n"
    "s_add_u32 m0, s56, 140800\n"
    "v_mfma_f32_16x16x32_bf16 v[104:107], v[20:23], a[252:255], v[104:107]\n"
    "buffer_load_dwordx4 v28, s[64:67], s55 offen lds\n"
    "v_mfma_f32_16x16x32_bf16 v[108:111], v[20:23], v[252:255], v[108:111]\n"
    "s_add_u32 m0, s56, 142848\n"
    "s_add_u32 s40, s55, 5120\n"
    "buffer_load_dwordx4 v28, s[64:67], s40 offen lds\n"
    "s_add_u32 m0, s56, 144896\n"
    "s_add_u32 s40, s55, 40960\n"
    "buffer_load_dwordx4 v28, s[64:67], s40 offen lds\n"
    "s_add_u32 m0, s56, 146944\n"
    "s_add_u32 s40, s55, 46080\n"
    "buffer_load_dwordx4 v28, s[64:67], s40 offen lds\n"
    "s_add_u32 m0, s56, 148992\n"
    "s_nop 0\n"
    "buffer_load_dwordx4 v29, s[64:67], s55 offen lds\n"
    "s_waitcnt lgkmcnt(3)\n"
    "v_mfma_f32_16x16x32_bf16 v[52:55], a[80:83], a[200:203], v[52:55]\n"
    "s_waitcnt lgkmcnt(0)\n"
    "s_waitcnt vmcnt(13)\n"
    "s_barrier\n"
    "v_mfma_f32_16x16x32_bf16 v[56:59], a[84:87], a[200:203], v[56:59]\n"
    "ds_read_b128 a[160:163], v43 offset:61440\n"
    "v_mfma_f32_16x16x32_bf16 v[60:63], a[80:83], a[204:207], v[60:63]\n"
    "ds_read_b128 a[164:167], v43 offset:63488\n"
    "s_add_u32 m0, s5, 32768\n"
    "v_mfma_f32_16x16x32_bf16 v[64:67], a[84:87], a[204:207], v[64:67]\n"
    "ds_read_b128 a[168:171], v46 offset:4096\n"
    "v_mfma_f32_16x16x32_bf16 v[68:71], a[80:83], a[208:211], v[68:71]\n"
    "ds_read_b128 a[172:175], v46 offset:6144\n"
    "v_mfma_f32_16x16x32_bf16 v[72:75], a[84:87], a[208:211], v[72:75]\n"
    "ds_read_b128 a[176:179], v46 offset:8192\n"
    "v_mfma_f32_16x16x32_bf16 v[76:79], a[80:83], a[212:215], v[76:79]\n"
    "ds_read_b128 a[180:183], v44 offset:61440\n"
    "v_mfma_f32_16x16x32_bf16 v[80:83], a[84:87], a[212:215], v[80:83]\n"
    "ds_read_b128 a[184:187], v44 offset:63488\n"
    "v_mfma_f32_16x16x32_bf16 v[84:87], a[80:83], a[216:219], v[84:87]\n"
    "ds_read_b128 a[188:191], v47 offset:4096\n"
    "v_mfma_f32_16x16x32_bf16 v[88:91], a[84:87], a[216:219], v[88:91]\n"
    "ds_read_b128 a[192:195], v47 offset:6144\n"
    "v_mfma_f32_16x16x32_bf16 v[52:55], a[88:91], a[220:223], v[52:55]\n"
    "ds_read_b128 a[196:199], v47 offset:8192\n"
    "v_mfma_f32_16x16x32_bf16 v[56:59], a[92:95], a[220:223], v[56:59]\n"
    "buffer_load_dwordx4 v0, s[60:63], s49 offen offset:0 lds\n"
    "v_mfma_f32_16x16x32_bf16 v[60:63], a[88:91], a[224:227], v[60:63]\n"
    "ds_read_b128 a[240:243], v48 offset:0\n"
    "v_mfma_f32_16x16x32_bf16 v[64:67], a[92:95], a[224:227], v[64:67]\n"
    "ds_read_b128 a[244:247], v48 offset:2048\n"
    "v_mfma_f32_16x16x32_bf16 v[68:71], a[88:91], a[228:231], v[68:71]\n"
    "ds_read_b128 a[248:251], v48 offset:4096\n"
    "v_mfma_f32_16x16x32_bf16 v[72:75], a[92:95], a[228:231], v[72:75]\n"
    "ds_read_b128 a[252:255], v48 offset:6144\n"
    "v_mfma_f32_16x16x32_bf16 v[76:79], a[88:91], a[232:235], v[76:79]\n"
    "ds_read_b128 v[252:255], v48 offset:8192\n"
    "v_mfma_f32_16x16x32_bf16 v[80:83], a[92:95], a[232:235], v[80:83]\n"
    "buffer_load_dwordx4 v19, s[60:63], s49 offen offset:1024 lds\n"
    "v_mfma_f32_16x16x32_bf16 v[84:87], a[88:91], a[236:239], v[84:87]\n"
    "buffer_load_dwordx4 v24, s[60:63], s49 offen offset:2048 lds\n"
    "v_mfma_f32_16x16x32_bf16 v[88:91], a[92:95], a[236:239], v[88:91]\n"
    "buffer_load_dwordx4 v25, s[60:63], s49 offen offset:3072 lds\n"
    "s_waitcnt vmcnt(13)\n"
    "ds_read_b128 a[96:99], v41 offset:0\n"
    "ds_read_b128 a[100:103], v41 offset:2048\n"
    "ds_read_b128 a[104:107], v42 offset:0\n"
    "ds_read_b128 a[108:111], v42 offset:2048\n"
    "s_waitcnt lgkmcnt(3)\n"
    "v_mfma_f32_16x16x32_bf16 v[52:55], a[96:99], a[160:163], v[52:55]\n"
    "buffer_load_dwordx4 v[32:35], v51, s[68:71], s28 offen offset:0 sc1\n"
    "s_waitcnt lgkmcnt(2)\n"
    "v_mfma_f32_16x16x32_bf16 v[56:59], a[100:103], a[160:163], v[56:59]\n"
    "ds_read_b128 a[200:203], v46 offset:10240\n"
    "s_waitcnt lgkmcnt(1)\n"
    "v_mfma_f32_16x16x32_bf16 v[60:63], a[96:99], a[164:167], v[60:63]\n"
    "ds_read_b128 a[204:207], v46 offset:12288\n"
    "s_add_u32 m0, s5, 0\n"
    "v_mfma_f32_16x16x32_bf16 v[64:67], a[100:103], a[164:167], v[64:67]\n"
    "ds_read_b128 a[208:211], v46 offset:14336\n"
    "v_mfma_f32_16x16x32_bf16 v[68:71], a[96:99], a[168:171], v[68:71]\n"
    "ds_read_b128 a[212:215], v46 offset:16384\n"
    "v_mfma_f32_16x16x32_bf16 v[72:75], a[100:103], a[168:171], v[72:75]\n"
    "ds_read_b128 a[216:219], v46 offset:18432\n"
    "v_mfma_f32_16x16x32_bf16 v[76:79], a[96:99], a[172:175], v[76:79]\n"
    "ds_read_b128 a[220:223], v47 offset:10240\n"
    "v_mfma_f32_16x16x32_bf16 v[80:83], a[100:103], a[172:175], v[80:83]\n"
    "ds_read_b128 a[224:227], v47 offset:12288\n"
    "v_mfma_f32_16x16x32_bf16 v[84:87], a[96:99], a[176:179], v[84:87]\n"
    "ds_read_b128 a[228:231], v47 offset:14336\n"
    "v_mfma_f32_16x16x32_bf16 v[88:91], a[100:103], a[176:179], v[88:91]\n"
    "ds_read_b128 a[232:235], v47 offset:16384\n"
    "v_mfma_f32_16x16x32_bf16 v[52:55], a[104:107], a[180:183], v[52:55]\n"
    "ds_read_b128 a[236:239], v47 offset:18432\n"
    "v_mfma_f32_16x16x32_bf16 v[56:59], a[108:111], a[180:183], v[56:59]\n"
    "buffer_load_dwordx4 v0, s[60:63], s50 offen offset:0 lds\n"
    "v_mfma_f32_16x16x32_bf16 v[60:63], a[104:107], a[184:187], v[60:63]\n"
    "buffer_load_dwordx4 v19, s[60:63], s50 offen offset:1024 lds\n"
    "v_mfma_f32_16x16x32_bf16 v[64:67], a[108:111], a[184:187], v[64:67]\n"
    "buffer_load_dwordx4 v24, s[60:63], s50 offen offset:2048 lds\n"
    "v_mfma_f32_16x16x32_bf16 v[68:71], a[104:107], a[188:191], v[68:71]\n"
    "buffer_load_dwordx4 v25, s[60:63], s50 offen offset:3072 lds\n"
    "v_mfma_f32_16x16x32_bf16 v[72:75], a[108:111], a[188:191], v[72:75]\n"
    "s_waitcnt vmcnt(14)\n"
    "ds_read_b128 a[112:115], v41 offset:16384\n"
    "v_mfma_f32_16x16x32_bf16 v[76:79], a[104:107], a[192:195], v[76:79]\n"
    "ds_read_b128 a[116:119], v41 offset:18432\n"
    "ds_read_b128 a[120:123], v42 offset:16384\n"
    "v_mfma_f32_16x16x32_bf16 v[80:83], a[108:111], a[192:195], v[80:83]\n"
    "ds_read_b128 a[124:127], v42 offset:18432\n"
    "v_mfma_f32_16x16x32_bf16 v[84:87], a[104:107], a[196:199], v[84:87]\n"
    "buffer_load_dwordx4 v[36:39], v51, s[68:71], s28 offen offset:64 sc1\n"
    "v_mfma_f32_16x16x32_bf16 v[88:91], a[108:111], a[196:199], v[88:91]\n"
    "v_mfma_f32_16x16x32_bf16 v[92:95], v[20:23], a[240:243], v[92:95]\n"
    "v_mfma_f32_16x16x32_bf16 v[96:99], v[20:23], a[244:247], v[96:99]\n"
    "v_mfma_f32_16x16x32_bf16 v[100:103], v[20:23], a[248:251], v[100:103]\n"
    "v_mfma_f32_16x16x32_bf16 v[104:107], v[20:23], a[252:255], v[104:107]\n"
    "v_mfma_f32_16x16x32_bf16 v[108:111], v[20:23], v[252:255], v[108:111]\n"
    "s_waitcnt lgkmcnt(3)\n"
    "v_mfma_f32_16x16x32_bf16 v[52:55], a[112:115], a[200:203], v[52:55]\n"
    "s_waitcnt lgkmcnt(0)\n"
    "s_waitcnt vmcnt(10)\n"
    "s_barrier\n"
    "v_mfma_f32_16x16x32_bf16 v[56:59], a[116:119], a[200:203], v[56:59]\n"
    "ds_read_b128 a[160:163], v46 offset:20480\n"
    "s_waitcnt vmcnt(6)\n"
    "v_mfma_f32_16x16x32_bf16 v[60:63], a[112:115], a[204:207], v[60:63]\n"
    "ds_read_b128 a[164:167], v46 offset:22528\n"
    "ds_read_b128 a[128:131], v41 offset:32768\n"
    "v_mfma_f32_16x16x32_bf16 v[64:67], a[116:119], a[204:207], v[64:67]\n"
    "ds_read_b128 a[168:171], v46 offset:24576\n"
    "ds_read_b128 a[132:135], v41 offset:34816\n"
    "v_mfma_f32_16x16x32_bf16 v[68:71], a[112:115], a[208:211], v[68:71]\n"
    "ds_read_b128 a[172:175], v46 offset:26624\n"
    "ds_read_b128 a[136:139], v42 offset:32768\n"
    "v_mfma_f32_16x16x32_bf16 v[72:75], a[116:119], a[208:211], v[72:75]\n"
    "ds_read_b128 a[176:179], v46 offset:28672\n"
    "ds_read_b128 a[140:143], v42 offset:34816\n"
    "v_mfma_f32_16x16x32_bf16 v[76:79], a[112:115], a[212:215], v[76:79]\n"
    "ds_read_b128 a[180:183], v47 offset:20480\n"
    "s_add_u32 s17, s13, 0\n"
    "v_mfma_f32_16x16x32_bf16 v[80:83], a[116:119], a[212:215], v[80:83]\n"
    "ds_read_b128 a[184:187], v47 offset:22528\n"
    "v_mfma_f32_16x16x32_bf16 v[84:87], a[112:115], a[216:219], v[84:87]\n"
    "ds_read_b128 a[188:191], v47 offset:24576\n"
    "v_mfma_f32_16x16x32_bf16 v[88:91], a[116:119], a[216:219], v[88:91]\n"
    "ds_read_b128 a[192:195], v47 offset:26624\n"
    "v_mfma_f32_16x16x32_bf16 v[52:55], a[120:123], a[220:223], v[52:55]\n"
    "ds_read_b128 a[196:199], v47 offset:28672\n"
    "v_mfma_f32_16x16x32_bf16 v[56:59], a[124:127], a[220:223], v[56:59]\n"
    "s_and_b32 s17, s17, 0x7ffff\n"
    "s_add_u32 s17, s17, s12\n"
    "v_mfma_f32_16x16x32_bf16 v[60:63], a[120:123], a[224:227], v[60:63]\n"
    "ds_read_b128 a[240:243], v48 offset:20480\n"
    "s_add_u32 m0, s5, 49152\n"
    "v_mfma_f32_16x16x32_bf16 v[64:67], a[124:127], a[224:227], v[64:67]\n"
    "ds_read_b128 a[244:247], v48 offset:22528\n"
    "v_mfma_f32_16x16x32_bf16 v[68:71], a[120:123], a[228:231], v[68:71]\n"
    "ds_read_b128 a[248:251], v48 offset:24576\n"
    "v_mfma_f32_16x16x32_bf16 v[72:75], a[124:127], a[228:231], v[72:75]\n"
    "ds_read_b128 a[252:255], v48 offset:26624\n"
    "v_mfma_f32_16x16x32_bf16 v[76:79], a[120:123], a[232:235], v[76:79]\n"
    "ds_read_b128 v[252:255], v48 offset:28672\n"
    "v_mfma_f32_16x16x32_bf16 v[80:83], a[124:127], a[232:235], v[80:83]\n"
    "buffer_load_dwordx4 v11, s[72:75], s17 offen offset:0 lds\n"
    "v_mfma_f32_16x16x32_bf16 v[84:87], a[120:123], a[236:239], v[84:87]\n"
    "buffer_load_dwordx4 v12, s[72:75], s17 offen offset:1024 lds\n"
    "v_mfma_f32_16x16x32_bf16 v[88:91], a[124:127], a[236:239], v[88:91]\n"
    "buffer_load_dwordx4 v13, s[72:75], s17 offen offset:2048 lds\n"
    "buffer_load_dwordx4 v14, s[72:75], s17 offen offset:3072 lds\n"
    "s_waitcnt lgkmcnt(15)\n"
    "v_mfma_f32_16x16x32_bf16 v[52:55], a[128:131], a[160:163], v[52:55]\n"
    "s_waitcnt lgkmcnt(14)\n"
    "v_mfma_f32_16x16x32_bf16 v[56:59], a[132:135], a[160:163], v[56:59]\n"
    "ds_read_b128 a[200:203], v46 offset:30720\n"
    "s_waitcnt vmcnt(5)\n"
    "v_mfma_f32_16x16x32_bf16 v[60:63], a[128:131], a[164:167], v[60:63]\n"
    "ds_read_b128 a[204:207], v46 offset:32768\n"
    "ds_read_b128 a[144:147], v41 offset:0\n"
    "v_mfma_f32_16x16x32_bf16 v[64:67], a[132:135], a[164:167], v[64:67]\n"
    "ds_read_b128 a[208:211], v46 offset:34816\n"
    "ds_read_b128 a[148:151], v41 offset:2048\n"
    "v_mfma_f32_16x16x32_bf16 v[68:71], a[128:131], a[168:171], v[68:71]\n"
    "ds_read_b128 a[212:215], v46 offset:36864\n"
    "ds_read_b128 a[152:155], v42 offset:0\n"
    "v_mfma_f32_16x16x32_bf16 v[72:75], a[132:135], a[168:171], v[72:75]\n"
    "ds_read_b128 a[216:219], v46 offset:38912\n"
    "ds_read_b128 a[156:159], v42 offset:2048\n"
    "s_waitcnt lgkmcnt(15)\n"
    "v_mfma_f32_16x16x32_bf16 v[76:79], a[128:131], a[172:175], v[76:79]\n"
    "ds_read_b128 a[220:223], v47 offset:30720\n"
    "v_mfma_f32_16x16x32_bf16 v[80:83], a[132:135], a[172:175], v[80:83]\n"
    "ds_read_b128 a[224:227], v47 offset:32768\n"
    "v_mfma_f32_16x16x32_bf16 v[84:87], a[128:131], a[176:179], v[84:87]\n"
    "ds_read_b128 a[228:231], v47 offset:34816\n"
    "v_xor_b32 v33, s8, v33\n"
    "v_mfma_f32_16x16x32_bf16 v[88:91], a[132:135], a[176:179], v[88:91]\n"
    "ds_read_b128 a[232:235], v47 offset:36864\n"
    "v_bitop3_b32 v33, s8, v35, v33 bitop3:0xbe\n"
    "v_mfma_f32_16x16x32_bf16 v[52:55], a[136:139], a[180:183], v[52:55]\n"
    "ds_read_b128 a[236:239], v47 offset:38912\n"
    "s_waitcnt vmcnt(4)\n"
    "v_bitop3_b32 v33, s8, v37, v33 bitop3:0xbe\n"
    "v_mfma_f32_16x16x32_bf16 v[56:59], a[140:143], a[180:183], v[56:59]\n"
    "s_add_u32 s17, s13, 0\n"
    "s_xor_b32 s17, s17, 0x10000\n"
    "v_mfma_f32_16x16x32_bf16 v[60:63], a[136:139], a[184:187], v[60:63]\n"
    "s_and_b32 s17, s17, 0x7ffff\n"
    "s_add_u32 s17, s17, s12\n"
    "v_mfma_f32_16x16x32_bf16 v[64:67], a[140:143], a[184:187], v[64:67]\n"
    "s_add_u32 m0, s5, 16384\n"
    "v_bitop3_b32 v33, s8, v39, v33 bitop3:0xbe\n"
    "v_mfma_f32_16x16x32_bf16 v[68:71], a[136:139], a[188:191], v[68:71]\n"
    "v_cmp_ne_u32 vcc, 0, v33\n"
    "s_nop 0\n"
    "s_cbranch_vccnz R4_%=\n"
    "B5_%=:\n"
    "v_mfma_f32_16x16x32_bf16 v[72:75], a[140:143], a[188:191], v[72:75]\n"
    "buffer_load_dwordx4 v11, s[72:75], s17 offen offset:0 lds\n"
    "v_mfma_f32_16x16x32_bf16 v[76:79], a[136:139], a[192:195], v[76:79]\n"
    "v_mul_f32 v32, 1.562500000e-03, v32\n"
    "v_mul_f32 v36, 1.562500000e-03, v36\n"
    "v_mfma_f32_16x16x32_bf16 v[80:83], a[140:143], a[192:195], v[80:83]\n"
    "buffer_load_dwordx4 v12, s[72:75], s17 offen offset:1024 lds\n"
    "s_waitcnt lgkmcnt(15)\n"
    "v_mfma_f32_16x16x32_bf16 v[84:87], a[136:139], a[196:199], v[84:87]\n"
    "v_mul_f32 v34, 1.562500000e-03, v34\n"
    "v_mul_f32 v38, 1.562500000e-03, v38\n"
    "v_mfma_f32_16x16x32_bf16 v[88:91], a[140:143], a[196:199], v[88:91]\n"
    "v_fma_f32 v34, -v32, v32, v34\n"
    "v_fma_f32 v38, -v36, v36, v38\n"
    "v_mfma_f32_16x16x32_bf16 v[92:95], v[20:23], a[240:243], v[92:95]\n"
    "buffer_load_dwordx4 v13, s[72:75], s17 offen offset:2048 lds\n"
    "v_mfma_f32_16x16x32_bf16 v[96:99], v[20:23], a[244:247], v[96:99]\n"
    "v_max_f32 v34, 0, v34\n"
    "v_max_f32 v38, 0, v38\n"
    "v_mfma_f32_16x16x32_bf16 v[100:103], v[20:23], a[248:251], v[100:103]\n"
    "buffer_load_dwordx4 v14, s[72:75], s17 offen offset:3072 lds\n"
    "v_mfma_f32_16x16x32_bf16 v[104:107], v[20:23], a[252:255], v[104:107]\n"
    "v_add_f32 v34, 1.0e-5, v34\n"
    "v_add_f32 v38, 1.0e-5, v38\n"
    "s_waitcnt lgkmcnt(14)\n"
    "v_mfma_f32_16x16x32_bf16 v[108:111], v[20:23], v[252:255], v[108:111]\n"
    "v_rsq_f32 v34, v34\n"
    "v_rsq_f32 v38, v38\n"
    "s_waitcnt lgkmcnt(11)\n"
    "v_mfma_f32_16x16x32_bf16 v[52:55], a[144:147], a[200:203], v[52:55]\n"
    "v_xor_b32 v34, s27, v34\n"
    "v_xor_b32 v38, s27, v38\n"
    "s_waitcnt lgkmcnt(9)\n"
    "v_mfma_f32_16x16x32_bf16 v[56:59], a[148:151], a[200:203], v[56:59]\n"
    "v_mul_f32 v32, v32, v34\n"
    "v_mul_f32 v36, v36, v38\n"
    "v_mfma_f32_16x16x32_bf16 v[60:63], a[144:147], a[204:207], v[60:63]\n"
    "v_mov_b32_dpp v120, v34 quad_perm:[0,0,0,0] row_mask:0xf bank_mask:0xf\n"
    "v_mov_b32_dpp v121, v34 quad_perm:[1,1,1,1] row_mask:0xf bank_mask:0xf\n"
    "v_mfma_f32_16x16x32_bf16 v[64:67], a[148:151], a[204:207], v[64:67]\n"
    "v_mov_b32_dpp v122, v34 quad_perm:[2,2,2,2] row_mask:0xf bank_mask:0xf\n"
    "v_mov_b32_dpp v123, v34 quad_perm:[3,3,3,3] row_mask:0xf bank_mask:0xf\n"
    "v_mfma_f32_16x16x32_bf16 v[68:71], a[144:147], a[208:211], v[68:71]\n"
    "ds_write_b32 v49, v92 offset:0\n"
    "v_mfma_f32_16x16x32_bf16 v[72:75], a[148:151], a[208:211], v[72:75]\n"
    "ds_write_b32 v49, v96 offset:256\n"
    "s_waitcnt lgkmcnt(10)\n"
    "v_mfma_f32_16x16x32_bf16 v[76:79], a[144:147], a[212:215], v[76:79]\n"
    "ds_write_b32 v49, v100 offset:512\n"
    "v_mfma_f32_16x16x32_bf16 v[80:83], a[148:151], a[212:215], v[80:83]\n"
    "ds_write_b32 v49, v104 offset:768\n"
    "s_waitcnt lgkmcnt(10)\n"
    "v_mfma_f32_16x16x32_bf16 v[84:87], a[144:147], a[216:219], v[84:87]\n"
    "ds_write_b32 v49, v108 offset:1024\n"
    "v_mfma_f32_16x16x32_bf16 v[88:91], a[148:151], a[216:219], v[88:91]\n"
    "v_mov_b32_dpp v124, v38 quad_perm:[0,0,0,0] row_mask:0xf bank_mask:0xf\n"
    "v_mov_b32_dpp v125, v38 quad_perm:[1,1,1,1] row_mask:0xf bank_mask:0xf\n"
    "s_waitcnt lgkmcnt(9)\n"
    "v_mfma_f32_16x16x32_bf16 v[52:55], a[152:155], a[220:223], v[52:55]\n"
    "v_mov_b32_dpp v126, v38 quad_perm:[2,2,2,2] row_mask:0xf bank_mask:0xf\n"
    "v_mov_b32_dpp v127, v38 quad_perm:[3,3,3,3] row_mask:0xf bank_mask:0xf\n"
    "v_mfma_f32_16x16x32_bf16 v[56:59], a[156:159], a[220:223], v[56:59]\n"
    "s_waitcnt lgkmcnt(0)\n"
    "s_barrier\n"
    "v_mfma_f32_16x16x32_bf16 v[60:63], a[152:155], a[224:227], v[60:63]\n"
    "ds_read_b128 v[92:95], v50 offset:0\n"
    "ds_read_b128 v[96:99], v50 offset:256\n"
    "v_mfma_f32_16x16x32_bf16 v[64:67], a[156:159], a[224:227], v[64:67]\n"
    "ds_read_b128 v[100:103], v50 offset:512\n"
    "ds_read_b128 v[104:107], v50 offset:768\n"
    "v_mfma_f32_16x16x32_bf16 v[68:71], a[152:155], a[228:231], v[68:71]\n"
    "ds_read_b128 v[108:111], v50 offset:1024\n"
    "v_mov_b32_dpp v128, v32 quad_perm:[0,0,0,0] row_mask:0xf bank_mask:0xf\n"
    "v_mfma_f32_16x16x32_bf16 v[72:75], a[156:159], a[228:231], v[72:75]\n"
    "v_mov_b32_dpp v129, v32 quad_perm:[1,1,1,1] row_mask:0xf bank_mask:0xf\n"
    "v_mov_b32_dpp v130, v32 quad_perm:[2,2,2,2] row_mask:0xf bank_mask:0xf\n"
    "v_mfma_f32_16x16x32_bf16 v[76:79], a[152:155], a[232:235], v[76:79]\n"
    "v_mov_b32_dpp v131, v32 quad_perm:[3,3,3,3] row_mask:0xf bank_mask:0xf\n"
    "v_mov_b32_dpp v132, v36 quad_perm:[0,0,0,0] row_mask:0xf bank_mask:0xf\n"
    "v_mfma_f32_16x16x32_bf16 v[80:83], a[156:159], a[232:235], v[80:83]\n"
    "v_mov_b32_dpp v133, v36 quad_perm:[1,1,1,1] row_mask:0xf bank_mask:0xf\n"
    "v_mov_b32_dpp v134, v36 quad_perm:[2,2,2,2] row_mask:0xf bank_mask:0xf\n"
    "v_mfma_f32_16x16x32_bf16 v[84:87], a[152:155], a[236:239], v[84:87]\n"
    "v_mov_b32_dpp v135, v36 quad_perm:[3,3,3,3] row_mask:0xf bank_mask:0xf\n"
    "v_mfma_f32_16x16x32_bf16 v[88:91], a[156:159], a[236:239], v[88:91]\n"
    "s_waitcnt lgkmcnt(4)\n"
    "v_pk_add_f32 v[92:93], v[92:93], v[94:95]\n"
    "s_waitcnt lgkmcnt(3)\n"
    "v_pk_add_f32 v[96:97], v[96:97], v[98:99]\n"
    "s_waitcnt lgkmcnt(2)\n"
    "v_pk_add_f32 v[100:101], v[100:101], v[102:103]\n"
    "s_waitcnt lgkmcnt(1)\n"
    "v_pk_add_f32 v[104:105], v[104:105], v[106:107]\n"
    "s_waitcnt lgkmcnt(0)\n"
    "v_pk_add_f32 v[108:109], v[108:109], v[110:111]\n"
    "v_add_f32 v92, v92, v93\n"
    "v_add_f32 v96, v96, v97\n"
    "v_add_f32 v100, v100, v101\n"
    "v_add_f32 v104, v104, v105\n"
    "v_add_f32 v108, v108, v109\n"
    "v_pk_mul_f32 v[52:53], v[52:53], v[120:121]\n"
    "v_pk_mul_f32 v[54:55], v[54:55], v[122:123]\n"
    "v_pk_mul_f32 v[56:57], v[56:57], v[124:125]\n"
    "v_pk_mul_f32 v[58:59], v[58:59], v[126:127]\n"
    "v_pk_fma_f32 v[52:53], v[128:129], v[92:93], v[52:53] op_sel_hi:[1,0,1] neg_lo:[1,0,0] neg_hi:[1,0,0]\n"
    "v_pk_fma_f32 v[54:55], v[130:131], v[92:93], v[54:55] op_sel_hi:[1,0,1] neg_lo:[1,0,0] neg_hi:[1,0,0]\n"
    "v_pk_fma_f32 v[56:57], v[132:133], v[92:93], v[56:57] op_sel_hi:[1,0,1] neg_lo:[1,0,0] neg_hi:[1,0,0]\n"
    "v_pk_fma_f32 v[58:59], v[134:135], v[92:93], v[58:59] op_sel_hi:[1,0,1] neg_lo:[1,0,0] neg_hi:[1,0,0]\n"
    "v_cvt_pk_bf16_f32 v112, v52, v53\n"
    "v_cvt_pk_bf16_f32 v113, v54, v55\n"
    "v_cvt_pk_bf16_f32 v114, v56, v57\n"
    "v_cvt_pk_bf16_f32 v115, v58, v59\n"
    "buffer_store_dwordx4 v[112:115], v2, s[68:71], s24 offen offset:0 sc1\n"
    "v_pk_mul_f32 v[60:61], v[60:61], v[120:121]\n"
    "v_pk_mul_f32 v[62:63], v[62:63], v[122:123]\n"
    "v_pk_mul_f32 v[64:65], v[64:65], v[124:125]\n"
    "v_pk_mul_f32 v[66:67], v[66:67], v[126:127]\n"
    "v_pk_fma_f32 v[60:61], v[128:129], v[96:97], v[60:61] op_sel_hi:[1,0,1] neg_lo:[1,0,0] neg_hi:[1,0,0]\n"
    "v_pk_fma_f32 v[62:63], v[130:131], v[96:97], v[62:63] op_sel_hi:[1,0,1] neg_lo:[1,0,0] neg_hi:[1,0,0]\n"
    "v_pk_fma_f32 v[64:65], v[132:133], v[96:97], v[64:65] op_sel_hi:[1,0,1] neg_lo:[1,0,0] neg_hi:[1,0,0]\n"
    "v_pk_fma_f32 v[66:67], v[134:135], v[96:97], v[66:67] op_sel_hi:[1,0,1] neg_lo:[1,0,0] neg_hi:[1,0,0]\n"
    "v_cvt_pk_bf16_f32 v116, v60, v61\n"
    "v_cvt_pk_bf16_f32 v117, v62, v63\n"
    "v_cvt_pk_bf16_f32 v118, v64, v65\n"
    "v_cvt_pk_bf16_f32 v119, v66, v67\n"
    "buffer_store_dwordx4 v[116:119], v2, s[68:71], s24 offen offset:1024 sc1\n"
    "v_pk_mul_f32 v[68:69], v[68:69], v[120:121]\n"
    "v_pk_mul_f32 v[70:71], v[70:71], v[122:123]\n"
    "v_pk_mul_f32 v[72:73], v[72:73], v[124:125]\n"
    "v_pk_mul_f32 v[74:75], v[74:75], v[126:127]\n"
    "v_pk_fma_f32 v[68:69], v[128:129], v[100:101], v[68:69] op_sel_hi:[1,0,1] neg_lo:[1,0,0] neg_hi:[1,0,0]\n"
    "v_pk_fma_f32 v[70:71], v[130:131], v[100:101], v[70:71] op_sel_hi:[1,0,1] neg_lo:[1,0,0] neg_hi:[1,0,0]\n"
    "v_pk_fma_f32 v[72:73], v[132:133], v[100:101], v[72:73] op_sel_hi:[1,0,1] neg_lo:[1,0,0] neg_hi:[1,0,0]\n"
    "v_pk_fma_f32 v[74:75], v[134:135], v[100:101], v[74:75] op_sel_hi:[1,0,1] neg_lo:[1,0,0] neg_hi:[1,0,0]\n"
    "v_cvt_pk_bf16_f32 v112, v68, v69\n"
    "v_cvt_pk_bf16_f32 v113, v70, v71\n"
    "v_cvt_pk_bf16_f32 v114, v72, v73\n"
    "v_cvt_pk_bf16_f32 v115, v74, v75\n"
    "buffer_store_dwordx4 v[112:115], v2, s[68:71], s24 offen offset:2048 sc1\n"
    "v_pk_mul_f32 v[76:77], v[76:77], v[120:121]\n"
    "v_pk_mul_f32 v[78:79], v[78:79], v[122:123]\n"
    "v_pk_mul_f32 v[80:81], v[80:81], v[124:125]\n"
    "v_pk_mul_f32 v[82:83], v[82:83], v[126:127]\n"
    "v_pk_fma_f32 v[76:77], v[128:129], v[104:105], v[76:77] op_sel_hi:[1,0,1] neg_lo:[1,0,0] neg_hi:[1,0,0]\n"
    "v_pk_fma_f32 v[78:79], v[130:131], v[104:105], v[78:79] op_sel_hi:[1,0,1] neg_lo:[1,0,0] neg_hi:[1,0,0]\n"
    "v_pk_fma_f32 v[80:81], v[132:133], v[104:105], v[80:81] op_sel_hi:[1,0,1] neg_lo:[1,0,0] neg_hi:[1,0,0]\n"
    "v_pk_fma_f32 v[82:83], v[134:135], v[104:105], v[82:83] op_sel_hi:[1,0,1] neg_lo:[1,0,0] neg_hi:[1,0,0]\n"
    "v_cvt_pk_bf16_f32 v116, v76, v77\n"
    "v_cvt_pk_bf16_f32 v117, v78, v79\n"
    "v_cvt_pk_bf16_f32 v118, v80, v81\n"
    "v_cvt_pk_bf16_f32 v119, v82, v83\n"
    "buffer_store_dwordx4 v[116:119], v2, s[68:71], s24 offen offset:3072 sc1\n"
    "v_pk_mul_f32 v[84:85], v[84:85], v[120:121]\n"
    "v_pk_mul_f32 v[86:87], v[86:87], v[122:123]\n"
    "v_pk_mul_f32 v[88:89], v[88:89], v[124:125]\n"
    "v_pk_mul_f32 v[90:91], v[90:91], v[126:127]\n"
    "v_pk_fma_f32 v[84:85], v[128:129], v[108:109], v[84:85] op_sel_hi:[1,0,1] neg_lo:[1,0,0] neg_hi:[1,0,0]\n"
    "v_pk_fma_f32 v[86:87], v[130:131], v[108:109], v[86:87] op_sel_hi:[1,0,1] neg_lo:[1,0,0] neg_hi:[1,0,0]\n"
    "v_pk_fma_f32 v[88:89], v[132:133], v[108:109], v[88:89] op_sel_hi:[1,0,1] neg_lo:[1,0,0] neg_hi:[1,0,0]\n"
    "v_pk_fma_f32 v[90:91], v[134:135], v[108:109], v[90:91] op_sel_hi:[1,0,1] neg_lo:[1,0,0] neg_hi:[1,0,0]\n"
    "v_cvt_pk_bf16_f32 v112, v84, v85\n"
    "v_cvt_pk_bf16_f32 v113, v86, v87\n"
    "v_cvt_pk_bf16_f32 v114, v88, v89\n"
    "v_cvt_pk_bf16_f32 v115, v90, v91\n"
    "buffer_store_dwordx4 v[112:115], v10, s[68:71], s24 offen offset:0 sc1\n"
    "s_add_u32 s17, s13, 512\n"
    "s_and_b32 s17, s17, 0x7ffff\n"
    "s_add_u32 s17, s17, s12\n"
    "s_add_u32 m0, s5, 65536\n"
    "s_nop 0\n"
    "buffer_load_dwordx4 v11, s[72:75], s17 offen offset:0 lds\n"
    "buffer_load_dwordx4 v12, s[72:75], s17 offen offset:1024 lds\n"
    "buffer_load_dwordx4 v13, s[72:75], s17 offen offset:2048 lds\n"
    "buffer_load_dwordx4 v14, s[72:75], s17 offen offset:3072 lds\n"
    "v_lshrrev_b32 v0, 3, v3\n"
    "v_lshl_add_u32 v19, v0, 12, v7\n"
    "s_lshl_b32 s38, s37, 13\n"
    "v_add_u32 v19, s38, v19\n"
    "v_xor_b32 v24, 64, v19\n"
    "v_add_u32 v25, 65536, v19\n"
    "v_add_u32 v26, 65536, v24\n"
    "s_lshl1_add_u32 s4, s10, s36\n"
    "s_and_b32 s40, s7, 6\n"
    "s_add_u32 s40, s40, s37\n"
    "s_lshr_b32 s38, s7, 2\n"
    "s_lshl3_add_u32 s38, s38, s4\n"
    "s_and_b32 s39, s40, 3\n"
    "s_lshl2_add_u32 s38, s38, s39\n"
    "s_mul_i32 s38, s38, 81920\n"
    "s_add_u32 s24, s38, 1310720\n"
    "s_lshl3_add_u32 s38, s40, s4\n"
    "s_mul_i32 s38, s38, 1152\n"
    "s_add_u32 s26, s38, 4096\n"
    "s_lshr_b32 s38, s7, 1\n"
    "s_and_b32 s39, s10, 3\n"
    "s_add_u32 s38, s38, s39\n"
    "s_and_b32 s38, s38, 3\n"
    "s_lshl2_add_u32 s27, s11, s38\n"
    "s_lshl2_add_u32 s38, s3, s6\n"
    "s_mul_i32 s39, s38, 7680\n"
    "s_add_u32 s28, s39, 6553600\n"
    "s_lshl_b32 s38, s38, 7\n"
    "s_add_u32 s29, s38, 77824\n"
    "v_lshlrev_b32 v27, 3, v1\n"
    "s_mul_i32 s38, s6, 5120\n"
    "s_add_u32 s38, s38, 131072\n"
    "v_add_u32 v29, s38, v2\n"
    "s_xor_b32 s39, s6, 2\n"
    "s_mul_i32 s38, s39, 5120\n"
    "s_add_u32 s38, s38, 131072\n"
    "v_add_u32 v30, s38, v2\n"
    "s_lshr_b32 s31, s3, 4\n"
    "s_bfe_u32 s45, s3, 0x20002\n"
    "s_and_b32 s38, s3, 3\n"
    "s_lshl1_add_u32 s44, s38, s37\n"
    "s_lshl2_add_u32 s30, s44, s45\n"
    "s_mul_i32 s56, s6, 3072\n"
    "s_add_u32 s56, s56, 151552\n"
    "s_cmp_ge_u32 s31, 5\n"
    "s_cselect_b32 s38, 5, 0\n"
    "s_cselect_b32 s43, 2, 0\n"
    "s_cmp_lt_u32 s31, 10\n"
    "s_cselect_b32 s41, 1, 0\n"
    "s_cselect_b32 s38, s38, 10\n"
    "s_cselect_b32 s43, s43, 0\n"
    "s_cselect_b32 s39, 0, 20\n"
    "s_cselect_b32 s40, 5, 6\n"
    "s_mov_b32 s51, 1280\n"
    "s_cselect_b32 s51, 3072, s51\n"
    "s_sub_u32 s42, s31, s38\n"
    "s_add_u32 s43, s43, s36\n"
    "s_mul_i32 s38, s43, s40\n"
    "s_add_u32 s38, s38, s39\n"
    "s_add_u32 s38, s38, s42\n"
    "s_mul_i32 s39, s30, 1152\n"
    "s_lshl2_add_u32 s16, s38, s39\n"
    "s_lshl_b32 s38, s42, 5\n"
    "s_cmp_ge_u32 s42, 2\n"
    "s_cselect_b32 s39, 16, 0\n"
    "s_add_u32 s38, s38, s39\n"
    "s_cmp_eq_u32 s42, 4\n"
    "s_cselect_b32 s38, 64, s38\n"
    "s_cselect_b32 s50, 80, 16\n"
    "s_mul_i32 s39, s45, 160\n"
    "s_add_u32 s38, s38, s39\n"
    "s_mul_i32 s46, s38, 3072\n"
    "s_and_b32 s40, s43, 1\n"
    "s_lshl1_add_u32 s40, s44, s40\n"
    "s_mul_i32 s38, s40, 48\n"
    "s_cmp_lt_u32 s43, 2\n"
    "s_cselect_b32 s39, 768, 0\n"
    "s_add_u32 s39, s38, s39\n"
    "s_lshl1_add_u32 s46, s39, s46\n"
    "s_lshr_b32 s39, s43, 1\n"
    "s_lshl1_add_u32 s38, s40, s39\n"
    "s_mul_i32 s55, s38, 3840\n"
    "s_mul_i32 s38, s38, 20\n"
    "s_mul_i32 s39, s45, 5\n"
    "s_add_u32 s39, s39, s42\n"
    "s_add_u32 s38, s38, s39\n"
    "s_mul_i32 s54, s38, 3\n"
    "s_add_u32 s54, s54, 30976\n"
    "s_mul_i32 s39, s39, 192\n"
    "s_add_u32 s55, s55, s39\n"
    "s_add_u32 s55, s55, 372736\n"
    "s_cmp_ge_u32 s42, 3\n"
    "s_cselect_b32 s38, 3, 0\n"
    "s_cselect_b32 s39, 96, 0\n"
    "s_sub_u32 s38, s42, s38\n"
    "s_mul_i32 s40, s38, 48\n"
    "s_cmp_eq_u32 s38, 2\n"
    "s_cselect_b32 s40, 32, s40\n"
    "s_cselect_b32 s48, 4, 16\n"
    "s_cselect_b32 s49, 8, 4\n"
    "s_cselect_b32 s52, 48, 8\n"
    "s_add_u32 s39, s39, s40\n"
    "s_mul_i32 s38, s45, 192\n"
    "s_add_u32 s38, s38, s39\n"
    "s_mul_i32 s47, s38, 1280\n"
    "s_mul_i32 s38, s44, 160\n"
    "s_add_u32 s47, s47, s38\n"
    "s_lshl_b32 s38, s43, 6\n"
    "s_add_u32 s47, s47, s38\n"
    "s_lshl_b32 s38, s42, 2\n"
    "s_lshr_b32 s38, 0x532410, s38\n"
    "s_and_b32 s38, s38, 15\n"
    "s_mul_i32 s39, s30, 6\n"
    "s_add_u32 s38, s38, s39\n"
    "s_mul_i32 s38, s38, 5\n"
    "s_lshl1_add_u32 s38, s43, s38\n"
    "s_add_u32 s38, s38, 32896\n"
    "s_cmp_lg_u32 s41, 0\n"
    "s_cselect_b32 s53, s46, s47\n"
    "s_cselect_b32 s48, 4, s48\n"
    "s_cselect_b32 s49, 8, s49\n"
    "s_cselect_b32 s50, s50, s52\n"
    "s_cselect_b32 s38, s54, s38\n"
    "s_lshl_b32 s54, s38, 10\n"
    "s_cmp_eq_u32 s43, 0\n"
    "s_cselect_b32 s52, 64, -64\n"
    "s_mov_b32 s47, 0x7f000000\n"
    "s_cselect_b32 s47, 4096, s47\n"
    "s_cmp_lg_u32 s41, 0\n"
    "s_cselect_b32 s52, 0, s52\n"
    "s_cselect_b32 s47, 2048, s47\n"
    "v_add_u32 v33, 0, v1\n"
    "v_mul_u32_u24 v31, 171, v33\n"
    "v_lshrrev_b32 v31, 10, v31\n"
    "v_mul_u32_u24 v32, 6, v31\n"
    "v_sub_u32 v32, v33, v32\n"
    "v_and_b32 v34, 3, v31\n"
    "v_bfe_u32 v33, v31, 2, 1\n"
    "v_mad_u32_u24 v34, v33, s48, v34\n"
    "v_bfe_u32 v33, v31, 3, 1\n"
    "v_mad_u32_u24 v34, v33, s49, v34\n"
    "v_lshrrev_b32 v33, 4, v31\n"
    "v_mad_u32_u24 v34, v33, s50, v34\n"
    "v_mul_u32_u24 v35, s51, v34\n"
    "v_lshl_add_u32 v35, v32, 4, v35\n"
    "v_lshrrev_b32 v33, 2, v32\n"
    "v_mul_lo_u32 v33, v33, s52\n"
    "v_add_u32 v35, v35, v33\n"
    "v_add_u32 v33, 64, v1\n"
    "v_mul_u32_u24 v31, 171, v33\n"
    "v_lshrrev_b32 v31, 10, v31\n"
    "v_mul_u32_u24 v32, 6, v31\n"
    "v_sub_u32 v32, v33, v32\n"
    "v_and_b32 v34, 3, v31\n"
    "v_bfe_u32 v33, v31, 2, 1\n"
    "v_mad_u32_u24 v34, v33, s48, v34\n"
    "v_bfe_u32 v33, v31, 3, 1\n"
    "v_mad_u32_u24 v34, v33, s49, v34\n"
    "v_lshrrev_b32 v33, 4, v31\n"
    "v_mad_u32_u24 v34, v33, s50, v34\n"
    "v_mul_u32_u24 v36, s51, v34\n"
    "v_lshl_add_u32 v36, v32, 4, v36\n"
    "PAD1_%=:\n"
    "s_cbranch_execz PADRET0_%=\n"
    "v_lshrrev_b32 v33, 2, v32\n"
    "v_mul_lo_u32 v33, v33, s52\n"
    "v_add_u32 v36, v36, v33\n"
    "v_add_u32 v33, 128, v1\n"
    "v_mul_u32_u24 v31, 171, v33\n"
    "v_lshrrev_b32 v31, 10, v31\n"
    "v_mul_u32_u24 v32, 6, v31\n"
    "v_sub_u32 v32, v33, v32\n"
    "v_and_b32 v34, 3, v31\n"
    "v_bfe_u32 v33, v31, 2, 1\n"
    "v_mad_u32_u24 v34, v33, s48, v34\n"
    "v_bfe_u32 v33, v31, 3, 1\n"
    "v_mad_u32_u24 v34, v33, s49, v34\n"
    "v_lshrrev_b32 v33, 4, v31\n"
    "v_mad_u32_u24 v34, v33, s50, v34\n"
    "v_mul_u32_u24 v37, s51, v34\n"
    "v_lshl_add_u32 v37, v32, 4, v37\n"
    "v_lshrrev_b32 v33, 2, v32\n"
    "v_mul_lo_u32 v33, v33, s52\n"
    "v_add_u32 v37, v37, v33\n"
    "s_cmp_lg_u32 s41, 0\n"
    "s_cselect_b32 s48, 8, 16\n"
    "s_cselect_b32 s49, 32, 8\n"
    "s_mov_b32 s50, 1024\n"
    "s_cselect_b32 s46, 0, 0x7f000000\n"
    "s_add_u32 s38, s7, 2\n"
    "s_and_b32 s38, s38, 7\n"
    "s_sub_u32 s38, s38, s7\n"
    "s_mul_i32 s39, s38, 30720\n"
    "s_add_u32 s39, s39, s28\n"
    "s_add_u32 s30, s39, 0\n"
    "s_lshl_b32 s38, s38, 9\n"
    "s_add_u32 s42, s38, s29\n"
    "s_add_u32 s38, s7, 4\n"
    "s_and_b32 s38, s38, 7\n"
    "s_sub_u32 s38, s38, s7\n"
    "s_mul_i32 s39, s38, 30720\n"
    "s_add_u32 s39, s39, s28\n"
    "s_add_u32 s31, s39, 2560\n"
    "s_lshl_b32 s38, s38, 9\n"
    "s_add_u32 s43, s38, s29\n"
    "s_add_u32 s38, s7, 6\n"
    "s_and_b32 s38, s38, 7\n"
    "s_sub_u32 s38, s38, s7\n"
    "s_mul_i32 s39, s38, 30720\n"
    "s_add_u32 s39, s39, s28\n"
    "s_add_u32 s41, s39, 5120\n"
    "s_lshl_b32 s38, s38, 9\n"
    "s_add_u32 s44, s38, s29\n"
    "s_lshl2_add_u32 s65, s19, s6\n"
    "s_lshl_b32 s38, s65, 17\n"
    "s_lshl_b32 s39, s20, 11\n"
    "s_add_u32 s58, s38, s39\n"
    "s_lshl_b32 s59, s6, 13\n"
    "s_add_u32 s39, s27, 1\n"
    "s_and_b32 s39, s39, 3\n"
    "s_lshl2_add_u32 s39, s11, s39\n"
    "s_lshl1_add_u32 s39, s39, s37\n"
    "s_mul_i32 s39, s39, 20480\n"
    "s_mul_i32 s40, s4, 160\n"
    "s_add_u32 s51, s40, s39\n"
    "s_mul_i32 s45, s6, 5120\n"
    "s_add_u32 s45, s45, 131072\n"
    "v_add_u32 v32, 0, v1\n"
    "v_mul_u32_u24 v33, 205, v32\n"
    "v_lshrrev_b32 v33, 11, v33\n"
    "v_mul_u32_u24 v33, 1120, v33\n"
    "v_lshl_add_u32 v31, v32, 4, v33\n"
    "v_add_u32 v32, 64, v1\n"
    "v_mul_u32_u24 v33, 205, v32\n"
    "v_lshrrev_b32 v33, 11, v33\n"
    "v_mul_u32_u24 v33, 1120, v33\n"
    "v_lshl_add_u32 v38, v32, 4, v33\n"
    "v_add_u32 v32, 128, v1\n"
    "v_mul_u32_u24 v33, 205, v32\n"
    "v_lshrrev_b32 v33, 11, v33\n"
    "v_mul_u32_u24 v33, 1120, v33\n"
    "v_lshl_add_u32 v39, v32, 4, v33\n"
    "v_mul_u32_u24 v33, 160, v3\n"
    "v_add_u32 v33, s45, v33\n"
    "v_lshl_add_u32 v32, v4, 4, v33\n"
    "v_lshl_add_u32 v33, v4, 3, v33\n"
    "s_lshl2_add_u32 s52, s34, s6\n"
    "s_mul_i32 s52, s52, 1152\n"
    "s_add_u32 s52, s52, 4608\n"
    "s_lshl1_add_u32 s57, s21, s36\n"
    "s_mul_i32 s57, s57, 3840\n"
    "s_add_u32 s57, s57, 372736\n"
    "s_add_u32 s16, s16, 4608\n"
    "s_mov_b64 exec, 0\n"
    "s_cmp_eq_u32 s6, 0\n"
    "s_cbranch_scc1 PAD4_%=\n"
    "s_cmp_eq_u32 s6, 1\n"
    "s_cbranch_scc1 PAD5_%=\n"
    "s_cmp_eq_u32 s6, 2\n"
    "s_cbranch_scc1 PAD6_%=\n"
    "s_branch PAD7_%=\n"
    "PADRET1_%=:\n"
    "s_mov_b64 exec, -1\n"
    "s_waitcnt vmcnt(4)\n"
    "s_barrier\n"
    "s_cmp_eq_u32 s6, 0\n"
    "s_cbranch_scc0 F6_%=\n"
    "s_mov_b64 exec, 1\n"
    "buffer_atomic_swap v5, v9, s[68:71], s25 offen sc1\n"
    "s_mov_b64 exec, -1\n"
    "F6_%=:\n"
    "ds_read_b128 a[0:3], v19 offset:49152\n"
    "ds_read_b128 a[16:19], v24 offset:49152\n"
    "ds_read_b128 a[32:35], v19 offset:50176\n"
    "ds_read_b128 a[48:51], v24 offset:50176\n"
    "ds_read_b128 a[64:67], v19 offset:51200\n"
    "ds_read_b128 a[80:83], v24 offset:51200\n"
    "ds_read_b128 a[96:99], v19 offset:52224\n"
    "ds_read_b128 a[112:115], v24 offset:52224\n"
    "ds_read_b128 a[4:7], v19 offset:16384\n"
    "ds_read_b128 a[20:23], v24 offset:16384\n"
    "ds_read_b128 a[36:39], v19 offset:17408\n"
    "ds_read_b128 a[52:55], v24 offset:17408\n"
    "ds_read_b128 a[68:71], v19 offset:18432\n"
    "ds_read_b128 a[84:87], v24 offset:18432\n"
    "ds_read_b128 a[100:103], v19 offset:19456\n"
    "ds_read_b128 a[116:119], v24 offset:19456\n"
    "s_add_u32 s17, s13, 131072\n"
    "s_and_b32 s17, s17, 0x7ffff\n"
    "s_add_u32 s17, s17, s12\n"
    "s_add_u32 m0, s5, 32768\n"
    "s_nop 0\n"
    "buffer_load_dwordx4 v11, s[72:75], s17 offen offset:0 lds\n"
    "buffer_load_dwordx4 v12, s[72:75], s17 offen offset:1024 lds\n"
    "buffer_load_dwordx4 v13, s[72:75], s17 offen offset:2048 lds\n"
    "buffer_load_dwordx4 v14, s[72:75], s17 offen offset:3072 lds\n"
    "s_add_u32 s17, s13, 131072\n"
    "s_xor_b32 s17, s17, 0x10000\n"
    "s_and_b32 s17, s17, 0x7ffff\n"
    "s_add_u32 s17, s17, s12\n"
    "s_add_u32 m0, s5, 0\n"
    "s_nop 0\n"
    "buffer_load_dwordx4 v11, s[72:75], s17 offen offset:0 lds\n"
    "buffer_load_dwordx4 v12, s[72:75], s17 offen offset:1024 lds\n"
    "buffer_load_dwordx4 v13, s[72:75], s17 offen offset:2048 lds\n"
    "buffer_load_dwordx4 v14, s[72:75], s17 offen offset:3072 lds\n"
    "s_add_u32 s17, s13, 512\n"
    "s_xor_b32 s17, s17, 0x10000\n"
    "s_and_b32 s17, s17, 0x7ffff\n"
    "s_add_u32 s17, s17, s12\n"
    "s_add_u32 m0, s5, 81920\n"
    "s_nop 0\n"
    "buffer_load_dwordx4 v11, s[72:75], s17 offen offset:0 lds\n"
    "buffer_load_dwordx4 v12, s[72:75], s17 offen offset:1024 lds\n"
    "buffer_load_dwordx4 v13, s[72:75], s17 offen offset:2048 lds\n"
    "buffer_load_dwordx4 v14, s[72:75], s17 offen offset:3072 lds\n"
    "s_waitcnt lgkmcnt(0)\n"
    "s_waitcnt vmcnt(4)\n"
    "s_barrier\n"
    "ds_read_b128 a[8:11], v19 offset:32768\n"
    "ds_read_b128 a[24:27], v24 offset:32768\n"
    "ds_read_b128 a[40:43], v19 offset:33792\n"
    "ds_read_b128 a[56:59], v24 offset:33792\n"
    "ds_read_b128 a[72:75], v19 offset:34816\n"
    "ds_read_b128 a[88:91], v24 offset:34816\n"
    "ds_read_b128 a[104:107], v19 offset:35840\n"
    "ds_read_b128 a[120:123], v24 offset:35840\n"
    "ds_read_b128 a[12:15], v19 offset:0\n"
    "ds_read_b128 a[28:31], v24 offset:0\n"
    "ds_read_b128 a[44:47], v19 offset:1024\n"
    "ds_read_b128 a[60:63], v24 offset:1024\n"
    "ds_read_b128 a[76:79], v19 offset:2048\n"
    "ds_read_b128 a[92:95], v24 offset:2048\n"
    "ds_read_b128 a[108:111], v19 offset:3072\n"
    "ds_read_b128 a[124:127], v24 offset:3072\n"
    "s_add_u32 s17, s13, 262144\n"
    "s_and_b32 s17, s17, 0x7ffff\n"
    "s_add_u32 s17, s17, s12\n"
    "s_add_u32 m0, s5, 16384\n"
    "s_nop 0\n"
    "buffer_load_dwordx4 v11, s[72:75], s17 offen offset:0 lds\n"
    "buffer_load_dwordx4 v12, s[72:75], s17 offen offset:1024 lds\n"
    "buffer_load_dwordx4 v13, s[72:75], s17 offen offset:2048 lds\n"
    "buffer_load_dwordx4 v14, s[72:75], s17 offen offset:3072 lds\n"
    "s_add_u32 s17, s13, 262144\n"
    "s_xor_b32 s17, s17, 0x10000\n"
    "s_and_b32 s17, s17, 0x7ffff\n"
    "s_add_u32 s17, s17, s12\n"
    "s_add_u32 m0, s5, 49152\n"
    "s_nop 0\n"
    "buffer_load_dwordx4 v11, s[72:75], s17 offen offset:0 lds\n"
    "buffer_load_dwordx4 v12, s[72:75], s17 offen offset:1024 lds\n"
    "buffer_load_dwordx4 v13, s[72:75], s17 offen offset:2048 lds\n"
    "buffer_load_dwordx4 v14, s[72:75], s17 offen offset:3072 lds\n"
    "s_add_u32 m0, s56, 0\n"
    "s_nop 0\n"
    "buffer_load_dwordx4 v35, s[88:91], s53 offen lds\n"
    "s_add_u32 m0, s56, 1024\n"
    "s_nop 0\n"
    "buffer_load_dwordx4 v36, s[88:91], s53 offen lds\n"
    "s_add_u32 m0, s56, 2048\n"
    "s_nop 0\n"
    "buffer_load_dwordx4 v37, s[88:91], s53 offen lds\n"
    "s_add_u32 m0, s45, 0\n"
    "s_nop 0\n"
    "buffer_load_dwordx4 v31, s[60:63], s51 offen lds\n"
    "s_add_u32 m0, s45, 1024\n"
    "s_nop 0\n"
    "buffer_load_dwordx4 v38, s[60:63], s51 offen lds\n"
    "s_add_u32 m0, s45, 2048\n"
    "s_lshr_b64 exec, -1, 32\n"
    "buffer_load_dwordx4 v39, s[60:63], s51 offen lds\n"
    "s_mov_b64 exec, -1\n"
    "s_mov_b64 exec, 0\n"
    "s_cmp_eq_u32 s6, 0\n"
    "s_cbranch_scc1 PAD8_%=\n"
    "s_cmp_eq_u32 s6, 1\n"
    "s_cbranch_scc1 PAD9_%=\n"
    "s_cmp_eq_u32 s6, 2\n"
    "s_cbranch_scc1 PAD10_%=\n"
    "s_branch PADRET2_%=\n"
    "PADRET2_%=:\n"
    "s_mov_b64 exec, -1\n"
    "s_add_u32 s17, s13, 131584\n"
    "s_and_b32 s17, s17, 0x7ffff\n"
    "s_add_u32 s17, s17, s12\n"
    "s_add_u32 m0, s5, 98304\n"
    "s_nop 0\n"
    "buffer_load_dwordx4 v11, s[72:75], s17 offen offset:0 lds\n"
    "buffer_load_dwordx4 v12, s[72:75], s17 offen offset:1024 lds\n"
    "buffer_load_dwordx4 v13, s[72:75], s17 offen offset:2048 lds\n"
    "buffer_load_dwordx4 v14, s[72:75], s17 offen offset:3072 lds\n"
    "s_add_u32 s17, s13, 131584\n"
    "s_xor_b32 s17, s17, 0x10000\n"
    "s_and_b32 s17, s17, 0x7ffff\n"
    "s_add_u32 s17, s17, s12\n"
    "s_add_u32 m0, s5, 114688\n"
    "s_nop 0\n"
    "buffer_load_dwordx4 v11, s[72:75], s17 offen offset:0 lds\n"
    "buffer_load_dwordx4 v12, s[72:75], s17 offen offset:1024 lds\n"
    "buffer_load_dwordx4 v13, s[72:75], s17 offen offset:2048 lds\n"
    "buffer_load_dwordx4 v14, s[72:75], s17 offen offset:3072 lds\n"
    "s_waitcnt lgkmcnt(0)\n"
    "s_barrier\n"
    "v_mov_b32 v28, 0\n"
    "s_mov_b64 exec, 0xf\n"
    "buffer_atomic_or v28, v9, s[68:71], s26 offen offset:0 sc0 sc1\n"
    "s_mov_b64 exec, -1\n"
    "s_add_u32 s17, s13, 262656\n"
    "s_and_b32 s17, s17, 0x7ffff\n"
    "s_add_u32 s17, s17, s12\n"
    "s_add_u32 m0, s5, 0\n"
    "s_nop 0\n"
    "buffer_load_dwordx4 v11, s[72:75], s17 offen offset:0 lds\n"
    "buffer_load_dwordx4 v12, s[72:75], s17 offen offset:1024 lds\n"
    "buffer_load_dwordx4 v13, s[72:75], s17 offen offset:2048 lds\n"
    "buffer_load_dwordx4 v14, s[72:75], s17 offen offset:3072 lds\n"
    "s_add_u32 s17, s13, 262656\n"
    "s_xor_b32 s17, s17, 0x10000\n"
    "s_and_b32 s17, s17, 0x7ffff\n"
    "s_add_u32 s17, s17, s12\n"
    "s_add_u32 m0, s5, 32768\n"
    "s_nop 0\n"
    "buffer_load_dwordx4 v11, s[72:75], s17 offen offset:0 lds\n"
    "buffer_load_dwordx4 v12, s[72:75], s17 offen offset:1024 lds\n"
    "buffer_load_dwordx4 v13, s[72:75], s17 offen offset:2048 lds\n"
    "buffer_load_dwordx4 v14, s[72:75], s17 offen offset:3072 lds\n"
    "s_waitcnt vmcnt(8)\n"
    "v_cmp_ne_u32 vcc, s8, v28\n"
    "s_nop 0\n"
    "s_and_b64 vcc, vcc, 0xf\n"
    "s_cbranch_vccnz R7_%=\n"
    "B8_%=:\n"
    "buffer_load_dwordx4 a[128:131], v2, s[68:71], s24 offen offset:0 sc1\n"
    "buffer_load_dwordx4 a[132:135], v2, s[68:71], s24 offen offset:1024 sc1\n"
    "buffer_load_dwordx4 a[136:139], v2, s[68:71], s24 offen offset:2048 sc1\n"
    "buffer_load_dwordx4 a[140:143], v2, s[68:71], s24 offen offset:3072 sc1\n"
    "buffer_load_dwordx4 a[144:147], v10, s[68:71], s24 offen offset:0 sc1\n"
    "s_add_u32 s24, s24, 5120\n"
    "buffer_load_dwordx4 a[148:151], v2, s[68:71], s24 offen offset:0 sc1\n"
    "buffer_load_dwordx4 a[152:155], v2, s[68:71], s24 offen offset:1024 sc1\n"
    "buffer_load_dwordx4 a[156:159], v2, s[68:71], s24 offen offset:2048 sc1\n"
    "ds_read_b128 v[80:83], v32\n"
    "ds_read_b128 v[84:87], v32 offset:64\n"
    "ds_read_b64 v[88:89], v33 offset:128\n"
    "s_waitcnt lgkmcnt(2)\n"
    "v_lshlrev_b32 v92, 16, v80\n"
    "v_and_b32 v93, 0xffff0000, v80\n"
    "v_lshlrev_b32 v94, 16, v81\n"
    "v_and_b32 v95, 0xffff0000, v81\n"
    "v_lshlrev_b32 v96, 16, v82\n"
    "v_and_b32 v97, 0xffff0000, v82\n"
    "v_lshlrev_b32 v98, 16, v83\n"
    "v_and_b32 v99, 0xffff0000, v83\n"
    "s_waitcnt lgkmcnt(1)\n"
    "v_lshlrev_b32 v100, 16, v84\n"
    "v_and_b32 v101, 0xffff0000, v84\n"
    "v_lshlrev_b32 v102, 16, v85\n"
    "v_and_b32 v103, 0xffff0000, v85\n"
    "v_lshlrev_b32 v104, 16, v86\n"
    "v_and_b32 v105, 0xffff0000, v86\n"
    "v_lshlrev_b32 v106, 16, v87\n"
    "v_and_b32 v107, 0xffff0000, v87\n"
    "s_waitcnt lgkmcnt(0)\n"
    "v_lshlrev_b32 v108, 16, v88\n"
    "v_and_b32 v109, 0xffff0000, v88\n"
    "v_lshlrev_b32 v110, 16, v89\n"
    "v_and_b32 v111, 0xffff0000, v89\n"
    "s_waitcnt vmcnt(7)\n"
    "v_mfma_f32_16x16x32_bf16 v[40:43], a[128:131], a[0:3], 0\n"
    "buffer_load_dwordx4 a[160:163], v2, s[68:71], s24 offen offset:3072 sc1\n"
    "v_mfma_f32_16x16x32_bf16 v[92:95], a[128:131], a[4:7], v[92:95]\n"
    "v_mfma_f32_16x16x32_bf16 v[60:63], a[128:131], a[8:11], 0\n"
    "v_mfma_f32_16x16x32_bf16 v[112:115], a[128:131], a[12:15], 0\n"
    "buffer_load_dwordx4 a[164:167], v10, s[68:71], s24 offen offset:0 sc1\n"
    "s_add_u32 s24, s24, 5120\n"
    "s_waitcnt vmcnt(8)\n"
    "v_mfma_f32_16x16x32_bf16 v[44:47], a[132:135], a[0:3], 0\n"
    "v_mfma_f32_16x16x32_bf16 v[96:99], a[132:135], a[4:7], v[96:99]\n"
    "v_mfma_f32_16x16x32_bf16 v[64:67], a[132:135], a[8:11], 0\n"
    "v_mfma_f32_16x16x32_bf16 v[116:119], a[132:135], a[12:15], 0\n"
    "s_waitcnt vmcnt(7)\n"
    "v_mfma_f32_16x16x32_bf16 v[48:51], a[136:139], a[0:3], 0\n"
    "buffer_load_dwordx4 a[168:171], v2, s[68:71], s24 offen offset:0 sc1\n"
    "v_mfma_f32_16x16x32_bf16 v[100:103], a[136:139], a[4:7], v[100:103]\n"
    "v_mfma_f32_16x16x32_bf16 v[68:71], a[136:139], a[8:11], 0\n"
    "v_mfma_f32_16x16x32_bf16 v[120:123], a[136:139], a[12:15], 0\n"
    "s_waitcnt vmcnt(7)\n"
    "v_mfma_f32_16x16x32_bf16 v[52:55], a[140:143], a[0:3], 0\n"
    "buffer_load_dwordx4 a[172:175], v2, s[68:71], s24 offen offset:1024 sc1\n"
    "v_mfma_f32_16x16x32_bf16 v[104:107], a[140:143], a[4:7], v[104:107]\n"
    "v_mfma_f32_16x16x32_bf16 v[72:75], a[140:143], a[8:11], 0\n"
    "v_mfma_f32_16x16x32_bf16 v[124:127], a[140:143], a[12:15], 0\n"
    "s_waitcnt vmcnt(7)\n"
    "v_mfma_f32_16x16x32_bf16 v[56:59], a[144:147], a[0:3], 0\n"
    "buffer_load_dwordx4 a[176:179], v2, s[68:71], s24 offen offset:2048 sc1\n"
    "v_mfma_f32_16x16x32_bf16 v[108:111], a[144:147], a[4:7], v[108:111]\n"
    "v_mfma_f32_16x16x32_bf16 v[76:79], a[144:147], a[8:11], 0\n"
    "v_mfma_f32_16x16x32_bf16 v[128:131], a[144:147], a[12:15], 0\n"
    "s_waitcnt vmcnt(7)\n"
    "v_mfma_f32_16x16x32_bf16 v[40:43], a[148:151], a[16:19], v[40:43]\n"
    "buffer_load_dwordx4 a[180:183], v2, s[68:71], s24 offen offset:3072 sc1\n"
    "v_mfma_f32_16x16x32_bf16 v[92:95], a[148:151], a[20:23], v[92:95]\n"
    "v_mfma_f32_16x16x32_bf16 v[60:63], a[148:151], a[24:27], v[60:63]\n"
    "v_mfma_f32_16x16x32_bf16 v[112:115], a[148:151], a[28:31], v[112:115]\n"
    "s_waitcnt vmcnt(7)\n"
    "v_mfma_f32_16x16x32_bf16 v[44:47], a[152:155], a[16:19], v[44:47]\n"
    "buffer_load_dwordx4 a[184:187], v10, s[68:71], s24 offen offset:0 sc1\n"
    "s_add_u32 s24, s24, 5120\n"
    "v_mfma_f32_16x16x32_bf16 v[96:99], a[152:155], a[20:23], v[96:99]\n"
    "v_mfma_f32_16x16x32_bf16 v[64:67], a[152:155], a[24:27], v[64:67]\n"
    "v_mfma_f32_16x16x32_bf16 v[116:119], a[152:155], a[28:31], v[116:119]\n"
    "s_waitcnt vmcnt(7)\n"
    "v_mfma_f32_16x16x32_bf16 v[48:51], a[156:159], a[16:19], v[48:51]\n"
    "buffer_load_dwordx4 a[0:3], v2, s[68:71], s24 offen offset:0 sc1\n"
    "v_mfma_f32_16x16x32_bf16 v[100:103], a[156:159], a[20:23], v[100:103]\n"
    "v_mfma_f32_16x16x32_bf16 v[68:71], a[156:159], a[24:27], v[68:71]\n"
    "v_mfma_f32_16x16x32_bf16 v[120:123], a[156:159], a[28:31], v[120:123]\n"
    "s_waitcnt vmcnt(7)\n"
    "v_mfma_f32_16x16x32_bf16 v[52:55], a[160:163], a[16:19], v[52:55]\n"
    "buffer_load_dwordx4 a[4:7], v2, s[68:71], s24 offen offset:1024 sc1\n"
    "v_mfma_f32_16x16x32_bf16 v[104:107], a[160:163], a[20:23], v[104:107]\n"
    "v_mfma_f32_16x16x32_bf16 v[72:75], a[160:163], a[24:27], v[72:75]\n"
    "v_mfma_f32_16x16x32_bf16 v[124:127], a[160:163], a[28:31], v[124:127]\n"
    "s_waitcnt vmcnt(7)\n"
    "v_mfma_f32_16x16x32_bf16 v[56:59], a[164:167], a[16:19], v[56:59]\n"
    "buffer_load_dwordx4 a[8:11], v2, s[68:71], s24 offen offset:2048 sc1\n"
    "v_mfma_f32_16x16x32_bf16 v[108:111], a[164:167], a[20:23], v[108:111]\n"
    "v_mfma_f32_16x16x32_bf16 v[76:79], a[164:167], a[24:27], v[76:79]\n"
    "v_mfma_f32_16x16x32_bf16 v[128:131], a[164:167], a[28:31], v[128:131]\n"
    "s_waitcnt vmcnt(7)\n"
    "v_mfma_f32_16x16x32_bf16 v[40:43], a[168:171], a[32:35], v[40:43]\n"
    "buffer_load_dwordx4 a[12:15], v2, s[68:71], s24 offen offset:3072 sc1\n"
    "v_mfma_f32_16x16x32_bf16 v[92:95], a[168:171], a[36:39], v[92:95]\n"
    "v_mfma_f32_16x16x32_bf16 v[60:63], a[168:171], a[40:43], v[60:63]\n"
    "v_mfma_f32_16x16x32_bf16 v[112:115], a[168:171], a[44:47], v[112:115]\n"
    "s_waitcnt vmcnt(7)\n"
    "v_mfma_f32_16x16x32_bf16 v[44:47], a[172:175], a[32:35], v[44:47]\n"
    "buffer_load_dwordx4 a[188:191], v10, s[68:71], s24 offen offset:0 sc1\n"
    "s_add_u32 s24, s24, 5120\n"
    "v_mfma_f32_16x16x32_bf16 v[96:99], a[172:175], a[36:39], v[96:99]\n"
    "v_mfma_f32_16x16x32_bf16 v[64:67], a[172:175], a[40:43], v[64:67]\n"
    "v_mfma_f32_16x16x32_bf16 v[116:119], a[172:175], a[44:47], v[116:119]\n"
    "s_waitcnt vmcnt(7)\n"
    "v_mfma_f32_16x16x32_bf16 v[48:51], a[176:179], a[32:35], v[48:51]\n"
    "buffer_load_dwordx4 a[16:19], v2, s[68:71], s24 offen offset:0 sc1\n"
    "v_mfma_f32_16x16x32_bf16 v[100:103], a[176:179], a[36:39], v[100:103]\n"
    "v_mfma_f32_16x16x32_bf16 v[68:71], a[176:179], a[40:43], v[68:71]\n"
    "v_mfma_f32_16x16x32_bf16 v[120:123], a[176:179], a[44:47], v[120:123]\n"
    "s_waitcnt vmcnt(7)\n"
    "v_mfma_f32_16x16x32_bf16 v[52:55], a[180:183], a[32:35], v[52:55]\n"
    "buffer_load_dwordx4 a[20:23], v2, s[68:71], s24 offen offset:1024 sc1\n"
    "v_mfma_f32_16x16x32_bf16 v[104:107], a[180:183], a[36:39], v[104:107]\n"
    "v_mfma_f32_16x16x32_bf16 v[72:75], a[180:183], a[40:43], v[72:75]\n"
    "v_mfma_f32_16x16x32_bf16 v[124:127], a[180:183], a[44:47], v[124:127]\n"
    "s_waitcnt vmcnt(7)\n"
    "v_mfma_f32_16x16x32_bf16 v[56:59], a[184:187], a[32:35], v[56:59]\n"
    "buffer_load_dwordx4 a[24:27], v2, s[68:71], s24 offen offset:2048 sc1\n"
    "v_mfma_f32_16x16x32_bf16 v[108:111], a[184:187], a[36:39], v[108:111]\n"
    "v_mfma_f32_16x16x32_bf16 v[76:79], a[184:187], a[40:43], v[76:79]\n"
    "v_mfma_f32_16x16x32_bf16 v[128:131], a[184:187], a[44:47], v[128:131]\n"
    "s_waitcnt vmcnt(7)\n"
    "v_mfma_f32_16x16x32_bf16 v[40:43], a[0:3], a[48:51], v[40:43]\n"
    "buffer_load_dwordx4 a[28:31], v2, s[68:71], s24 offen offset:3072 sc1\n"
    "v_mfma_f32_16x16x32_bf16 v[92:95], a[0:3], a[52:55], v[92:95]\n"
    "v_mfma_f32_16x16x32_bf16 v[60:63], a[0:3], a[56:59], v[60:63]\n"
    "v_mfma_f32_16x16x32_bf16 v[112:115], a[0:3], a[60:63], v[112:115]\n"
    "s_waitcnt vmcnt(7)\n"
    "v_mfma_f32_16x16x32_bf16 v[44:47], a[4:7], a[48:51], v[44:47]\n"
    "buffer_load_dwordx4 a[192:195], v10, s[68:71], s24 offen offset:0 sc1\n"
    "s_add_u32 s24, s24, 5120\n"
    "v_mfma_f32_16x16x32_bf16 v[96:99], a[4:7], a[52:55], v[96:99]\n"
    "v_mfma_f32_16x16x32_bf16 v[64:67], a[4:7], a[56:59], v[64:67]\n"
    "v_mfma_f32_16x16x32_bf16 v[116:119], a[4:7], a[60:63], v[116:119]\n"
    "s_waitcnt vmcnt(7)\n"
    "v_mfma_f32_16x16x32_bf16 v[48:51], a[8:11], a[48:51], v[48:51]\n"
    "buffer_load_dwordx4 a[32:35], v2, s[68:71], s24 offen offset:0 sc1\n"
    "v_mfma_f32_16x16x32_bf16 v[100:103], a[8:11], a[52:55], v[100:103]\n"
    "v_mfma_f32_16x16x32_bf16 v[68:71], a[8:11], a[56:59], v[68:71]\n"
    "v_mfma_f32_16x16x32_bf16 v[120:123], a[8:11], a[60:63], v[120:123]\n"
    "s_waitcnt vmcnt(7)\n"
    "v_mfma_f32_16x16x32_bf16 v[52:55], a[12:15], a[48:51], v[52:55]\n"
    "buffer_load_dwordx4 a[36:39], v2, s[68:71], s24 offen offset:1024 sc1\n"
    "v_mfma_f32_16x16x32_bf16 v[104:107], a[12:15], a[52:55], v[104:107]\n"
    "v_mfma_f32_16x16x32_bf16 v[72:75], a[12:15], a[56:59], v[72:75]\n"
    "v_mfma_f32_16x16x32_bf16 v[124:127], a[12:15], a[60:63], v[124:127]\n"
    "s_waitcnt vmcnt(7)\n"
    "v_mfma_f32_16x16x32_bf16 v[56:59], a[188:191], a[48:51], v[56:59]\n"
    "buffer_load_dwordx4 a[40:43], v2, s[68:71], s24 offen offset:2048 sc1\n"
    "v_mfma_f32_16x16x32_bf16 v[108:111], a[188:191], a[52:55], v[108:111]\n"
    "v_mfma_f32_16x16x32_bf16 v[76:79], a[188:191], a[56:59], v[76:79]\n"
    "v_mfma_f32_16x16x32_bf16 v[128:131], a[188:191], a[60:63], v[128:131]\n"
    "s_waitcnt vmcnt(7)\n"
    "v_mfma_f32_16x16x32_bf16 v[40:43], a[16:19], a[64:67], v[40:43]\n"
    "buffer_load_dwordx4 a[44:47], v2, s[68:71], s24 offen offset:3072 sc1\n"
    "v_mfma_f32_16x16x32_bf16 v[92:95], a[16:19], a[68:71], v[92:95]\n"
    "v_mfma_f32_16x16x32_bf16 v[60:63], a[16:19], a[72:75], v[60:63]\n"
    "v_mfma_f32_16x16x32_bf16 v[112:115], a[16:19], a[76:79], v[112:115]\n"
    "s_waitcnt vmcnt(7)\n"
    "v_mfma_f32_16x16x32_bf16 v[44:47], a[20:23], a[64:67], v[44:47]\n"
    "buffer_load_dwordx4 a[196:199], v10, s[68:71], s24 offen offset:0 sc1\n"
    "s_add_u32 s24, s24, 5120\n"
    "v_mfma_f32_16x16x32_bf16 v[96:99], a[20:23], a[68:71], v[96:99]\n"
    "v_mfma_f32_16x16x32_bf16 v[64:67], a[20:23], a[72:75], v[64:67]\n"
    "v_mfma_f32_16x16x32_bf16 v[116:119], a[20:23], a[76:79], v[116:119]\n"
    "s_waitcnt vmcnt(7)\n"
    "v_mfma_f32_16x16x32_bf16 v[48:51], a[24:27], a[64:67], v[48:51]\n"
    "buffer_load_dwordx4 a[48:51], v2, s[68:71], s24 offen offset:0 sc1\n"
    "v_mfma_f32_16x16x32_bf16 v[100:103], a[24:27], a[68:71], v[100:103]\n"
    "v_mfma_f32_16x16x32_bf16 v[68:71], a[24:27], a[72:75], v[68:71]\n"
    "v_mfma_f32_16x16x32_bf16 v[120:123], a[24:27], a[76:79], v[120:123]\n"
    "s_waitcnt vmcnt(7)\n"
    "v_mfma_f32_16x16x32_bf16 v[52:55], a[28:31], a[64:67], v[52:55]\n"
    "buffer_load_dwordx4 a[52:55], v2, s[68:71], s24 offen offset:1024 sc1\n"
    "v_mfma_f32_16x16x32_bf16 v[104:107], a[28:31], a[68:71], v[104:107]\n"
    "v_mfma_f32_16x16x32_bf16 v[72:75], a[28:31], a[72:75], v[72:75]\n"
    "v_mfma_f32_16x16x32_bf16 v[124:127], a[28:31], a[76:79], v[124:127]\n"
    "s_waitcnt vmcnt(7)\n"
    "v_mfma_f32_16x16x32_bf16 v[56:59], a[192:195], a[64:67], v[56:59]\n"
    "buffer_load_dwordx4 a[56:59], v2, s[68:71], s24 offen offset:2048 sc1\n"
    "v_mfma_f32_16x16x32_bf16 v[108:111], a[192:195], a[68:71], v[108:111]\n"
    "v_mfma_f32_16x16x32_bf16 v[76:79], a[192:195], a[72:75], v[76:79]\n"
    "v_mfma_f32_16x16x32_bf16 v[128:131], a[192:195], a[76:79], v[128:131]\n"
    "s_waitcnt vmcnt(7)\n"
    "v_mfma_f32_16x16x32_bf16 v[40:43], a[32:35], a[80:83], v[40:43]\n"
    "buffer_load_dwordx4 a[60:63], v2, s[68:71], s24 offen offset:3072 sc1\n"
    "v_mfma_f32_16x16x32_bf16 v[92:95], a[32:35], a[84:87], v[92:95]\n"
    "v_mfma_f32_16x16x32_bf16 v[60:63], a[32:35], a[88:91], v[60:63]\n"
    "v_mfma_f32_16x16x32_bf16 v[112:115], a[32:35], a[92:95], v[112:115]\n"
    "s_waitcnt vmcnt(7)\n"
    "v_mfma_f32_16x16x32_bf16 v[44:47], a[36:39], a[80:83], v[44:47]\n"
    "buffer_load_dwordx4 a[200:203], v10, s[68:71], s24 offen offset:0 sc1\n"
    "s_add_u32 s24, s24, 5120\n"
    "v_mfma_f32_16x16x32_bf16 v[96:99], a[36:39], a[84:87], v[96:99]\n"
    "v_mfma_f32_16x16x32_bf16 v[64:67], a[36:39], a[88:91], v[64:67]\n"
    "v_mfma_f32_16x16x32_bf16 v[116:119], a[36:39], a[92:95], v[116:119]\n"
    "s_waitcnt vmcnt(7)\n"
    "v_mfma_f32_16x16x32_bf16 v[48:51], a[40:43], a[80:83], v[48:51]\n"
    "buffer_load_dwordx4 a[64:67], v2, s[68:71], s24 offen offset:0 sc1\n"
    "v_mfma_f32_16x16x32_bf16 v[100:103], a[40:43], a[84:87], v[100:103]\n"
    "v_mfma_f32_16x16x32_bf16 v[68:71], a[40:43], a[88:91], v[68:71]\n"
    "v_mfma_f32_16x16x32_bf16 v[120:123], a[40:43], a[92:95], v[120:123]\n"
    "s_waitcnt vmcnt(7)\n"
    "v_mfma_f32_16x16x32_bf16 v[52:55], a[44:47], a[80:83], v[52:55]\n"
    "buffer_load_dwordx4 a[68:71], v2, s[68:71], s24 offen offset:1024 sc1\n"
    "v_mfma_f32_16x16x32_bf16 v[104:107], a[44:47], a[84:87], v[104:107]\n"
    "v_mfma_f32_16x16x32_bf16 v[72:75], a[44:47], a[88:91], v[72:75]\n"
    "v_mfma_f32_16x16x32_bf16 v[124:127], a[44:47], a[92:95], v[124:127]\n"
    "s_waitcnt vmcnt(7)\n"
    "v_mfma_f32_16x16x32_bf16 v[56:59], a[196:199], a[80:83], v[56:59]\n"
    "buffer_load_dwordx4 a[72:75], v2, s[68:71], s24 offen offset:2048 sc1\n"
    "v_mfma_f32_16x16x32_bf16 v[108:111], a[196:199], a[84:87], v[108:111]\n"
    "v_mfma_f32_16x16x32_bf16 v[76:79], a[196:199], a[88:91], v[76:79]\n"
    "v_mfma_f32_16x16x32_bf16 v[128:131], a[196:199], a[92:95], v[128:131]\n"
    "s_waitcnt vmcnt(7)\n"
    "v_mfma_f32_16x16x32_bf16 v[40:43], a[48:51], a[96:99], v[40:43]\n"
    "s_barrier\n"
    "v_mfma_f32_16x16x32_bf16 v[92:95], a[48:51], a[100:103], v[92:95]\n"
    "buffer_load_dwordx4 a[76:79], v2, s[68:71], s24 offen offset:3072 sc1\n"
    "v_mfma_f32_16x16x32_bf16 v[60:63], a[48:51], a[104:107], v[60:63]\n"
    "v_mfma_f32_16x16x32_bf16 v[112:115], a[48:51], a[108:111], v[112:115]\n"
    "s_waitcnt vmcnt(7)\n"
    "v_mfma_f32_16x16x32_bf16 v[44:47], a[52:55], a[96:99], v[44:47]\n"
    "buffer_load_dwordx4 a[204:207], v10, s[68:71], s24 offen offset:0 sc1\n"
    "s_add_u32 s24, s24, 5120\n"
    "v_mfma_f32_16x16x32_bf16 v[96:99], a[52:55], a[100:103], v[96:99]\n"
    "v_mfma_f32_16x16x32_bf16 v[64:67], a[52:55], a[104:107], v[64:67]\n"
    "v_mfma_f32_16x16x32_bf16 v[116:119], a[52:55], a[108:111], v[116:119]\n"
    "s_waitcnt vmcnt(7)\n"
    "v_mfma_f32_16x16x32_bf16 v[48:51], a[56:59], a[96:99], v[48:51]\n"
    "buffer_load_dwordx4 a[80:83], v2, s[68:71], s24 offen offset:0 sc1\n"
    "v_mfma_f32_16x16x32_bf16 v[100:103], a[56:59], a[100:103], v[100:103]\n"
    "v_mfma_f32_16x16x32_bf16 v[68:71], a[56:59], a[104:107], v[68:71]\n"
    "v_mfma_f32_16x16x32_bf16 v[120:123], a[56:59], a[108:111], v[120:123]\n"
    "s_waitcnt vmcnt(7)\n"
    "v_mfma_f32_16x16x32_bf16 v[52:55], a[60:63], a[96:99], v[52:55]\n"
    "buffer_load_dwordx4 a[84:87], v2, s[68:71], s24 offen offset:1024 sc1\n"
    "v_mfma_f32_16x16x32_bf16 v[104:107], a[60:63], a[100:103], v[104:107]\n"
    "v_mfma_f32_16x16x32_bf16 v[72:75], a[60:63], a[104:107], v[72:75]\n"
    "v_mfma_f32_16x16x32_bf16 v[124:127], a[60:63], a[108:111], v[124:127]\n"
    "s_waitcnt vmcnt(7)\n"
    "v_mfma_f32_16x16x32_bf16 v[56:59], a[200:203], a[96:99], v[56:59]\n"
    "buffer_load_dwordx4 a[88:91], v2, s[68:71], s24 offen offset:2048 sc1\n"
    "v_mfma_f32_16x16x32_bf16 v[108:111], a[200:203], a[100:103], v[108:111]\n"
    "v_mfma_f32_16x16x32_bf16 v[76:79], a[200:203], a[104:107], v[76:79]\n"
    "v_mfma_f32_16x16x32_bf16 v[128:131], a[200:203], a[108:111], v[128:131]\n"
    "s_waitcnt vmcnt(7)\n"
    "v_mfma_f32_16x16x32_bf16 v[40:43], a[64:67], a[112:115], v[40:43]\n"
    "buffer_load_dwordx4 a[92:95], v2, s[68:71], s24 offen offset:3072 sc1\n"
    "v_mfma_f32_16x16x32_bf16 v[92:95], a[64:67], a[116:119], v[92:95]\n"
    "ds_read_b128 a[96:99], v25 offset:0\n"
    "ds_read_b128 a[100:103], v25 offset:16384\n"
    "v_mfma_f32_16x16x32_bf16 v[60:63], a[64:67], a[120:123], v[60:63]\n"
    "ds_read_b128 a[104:107], v25 offset:32768\n"
    "ds_read_b128 a[108:111], v25 offset:49152\n"
    "v_mfma_f32_16x16x32_bf16 v[112:115], a[64:67], a[124:127], v[112:115]\n"
    "s_waitcnt vmcnt(7)\n"
    "v_mfma_f32_16x16x32_bf16 v[44:47], a[68:71], a[112:115], v[44:47]\n"
    "buffer_load_dwordx4 a[208:211], v10, s[68:71], s24 offen offset:0 sc1\n"
    "s_add_u32 s24, s24, 5120\n"
    "v_mfma_f32_16x16x32_bf16 v[96:99], a[68:71], a[116:119], v[96:99]\n"
    "v_mfma_f32_16x16x32_bf16 v[64:67], a[68:71], a[120:123], v[64:67]\n"
    "v_mfma_f32_16x16x32_bf16 v[116:119], a[68:71], a[124:127], v[116:119]\n"
    "s_waitcnt vmcnt(7)\n"
    "v_mfma_f32_16x16x32_bf16 v[48:51], a[72:75], a[112:115], v[48:51]\n"
    "buffer_load_dwordx4 a[228:231], v2, s[68:71], s24 offen offset:0 sc1\n"
    "v_mfma_f32_16x16x32_bf16 v[100:103], a[72:75], a[116:119], v[100:103]\n"
    "v_mfma_f32_16x16x32_bf16 v[68:71], a[72:75], a[120:123], v[68:71]\n"
    "v_mfma_f32_16x16x32_bf16 v[120:123], a[72:75], a[124:127], v[120:123]\n"
    "s_waitcnt vmcnt(7)\n"
    "v_mfma_f32_16x16x32_bf16 v[52:55], a[76:79], a[112:115], v[52:55]\n"
    "buffer_load_dwordx4 a[232:235], v2, s[68:71], s24 offen offset:1024 sc1\n"
    "v_mfma_f32_16x16x32_bf16 v[104:107], a[76:79], a[116:119], v[104:107]\n"
    "v_mfma_f32_16x16x32_bf16 v[72:75], a[76:79], a[120:123], v[72:75]\n"
    "v_mfma_f32_16x16x32_bf16 v[124:127], a[76:79], a[124:127], v[124:127]\n"
    "s_waitcnt vmcnt(7)\n"
    "v_mfma_f32_16x16x32_bf16 v[56:59], a[204:207], a[112:115], v[56:59]\n"
    "buffer_load_dwordx4 a[236:239], v2, s[68:71], s24 offen offset:2048 sc1\n"
    "v_mfma_f32_16x16x32_bf16 v[108:111], a[204:207], a[116:119], v[108:111]\n"
    "v_mfma_f32_16x16x32_bf16 v[76:79], a[204:207], a[120:123], v[76:79]\n"
    "v_mfma_f32_16x16x32_bf16 v[128:131], a[204:207], a[124:127], v[128:131]\n"
    "s_waitcnt lgkmcnt(0)\n"
    "s_waitcnt vmcnt(7)\n"
    "v_mfma_f32_16x16x32_bf16 v[40:43], a[80:83], a[96:99], v[40:43]\n"
    "buffer_load_dwordx4 a[240:243], v2, s[68:71], s24 offen offset:3072 sc1\n"
    "v_mfma_f32_16x16x32_bf16 v[92:95], a[80:83], a[100:103], v[92:95]\n"
    "ds_read_b128 a[212:215], v26 offset:0\n"
    "ds_read_b128 a[216:219], v26 offset:16384\n"
    "v_mfma_f32_16x16x32_bf16 v[60:63], a[80:83], a[104:107], v[60:63]\n"
    "ds_read_b128 a[220:223], v26 offset:32768\n"
    "ds_read_b128 a[224:227], v26 offset:49152\n"
    "v_mfma_f32_16x16x32_bf16 v[112:115], a[80:83], a[108:111], v[112:115]\n"
    "s_waitcnt vmcnt(7)\n"
    "v_mfma_f32_16x16x32_bf16 v[44:47], a[84:87], a[96:99], v[44:47]\n"
    "buffer_load_dwordx4 a[244:247], v10, s[68:71], s24 offen offset:0 sc1\n"
    "s_add_u32 s24, s24, 5120\n"
    "v_mfma_f32_16x16x32_bf16 v[96:99], a[84:87], a[100:103], v[96:99]\n"
    "v_mfma_f32_16x16x32_bf16 v[64:67], a[84:87], a[104:107], v[64:67]\n"
    "v_mfma_f32_16x16x32_bf16 v[116:119], a[84:87], a[108:111], v[116:119]\n"
    "s_waitcnt vmcnt(7)\n"
    "v_mfma_f32_16x16x32_bf16 v[48:51], a[88:91], a[96:99], v[48:51]\n"
    "buffer_load_dwordx4 a[112:115], v2, s[68:71], s24 offen offset:0 sc1\n"
    "v_mfma_f32_16x16x32_bf16 v[100:103], a[88:91], a[100:103], v[100:103]\n"
    "v_mfma_f32_16x16x32_bf16 v[68:71], a[88:91], a[104:107], v[68:71]\n"
    "v_mfma_f32_16x16x32_bf16 v[120:123], a[88:91], a[108:111], v[120:123]\n"
    "s_waitcnt vmcnt(7)\n"
    "v_mfma_f32_16x16x32_bf16 v[52:55], a[92:95], a[96:99], v[52:55]\n"
    "buffer_load_dwordx4 a[116:119], v2, s[68:71], s24 offen offset:1024 sc1\n"
    "v_mfma_f32_16x16x32_bf16 v[104:107], a[92:95], a[100:103], v[104:107]\n"
    "v_mfma_f32_16x16x32_bf16 v[72:75], a[92:95], a[104:107], v[72:75]\n"
    "v_mfma_f32_16x16x32_bf16 v[124:127], a[92:95], a[108:111], v[124:127]\n"
    "s_waitcnt vmcnt(7)\n"
    "v_mfma_f32_16x16x32_bf16 v[56:59], a[208:211], a[96:99], v[56:59]\n"
    "buffer_load_dwordx4 a[120:123], v2, s[68:71], s24 offen offset:2048 sc1\n"
    "v_mfma_f32_16x16x32_bf16 v[108:111], a[208:211], a[100:103], v[108:111]\n"
    "v_mfma_f32_16x16x32_bf16 v[76:79], a[208:211], a[104:107], v[76:79]\n"
    "v_mfma_f32_16x16x32_bf16 v[128:131], a[208:211], a[108:111], v[128:131]\n"
    "s_waitcnt lgkmcnt(0)\n"
    "s_waitcnt vmcnt(7)\n"
    "v_mfma_f32_16x16x32_bf16 v[40:43], a[228:231], a[212:215], v[40:43]\n"
    "buffer_load_dwordx4 a[124:127], v2, s[68:71], s24 offen offset:3072 sc1\n"
    "v_mfma_f32_16x16x32_bf16 v[92:95], a[228:231], a[216:219], v[92:95]\n"
    "ds_read_b128 a[96:99], v25 offset:1024\n"
    "ds_read_b128 a[100:103], v25 offset:17408\n"
    "v_mfma_f32_16x16x32_bf16 v[60:63], a[228:231], a[220:223], v[60:63]\n"
    "ds_read_b128 a[104:107], v25 offset:33792\n"
    "ds_read_b128 a[108:111], v25 offset:50176\n"
    "v_mfma_f32_16x16x32_bf16 v[112:115], a[228:231], a[224:227], v[112:115]\n"
    "PAD2_%=:\n"
    "s_cbranch_execz PADRET0_%=\n"
    "s_waitcnt vmcnt(7)\n"
    "v_mfma_f32_16x16x32_bf16 v[44:47], a[232:235], a[212:215], v[44:47]\n"
    "buffer_load_dwordx4 a[248:251], v10, s[68:71], s24 offen offset:0 sc1\n"
    "s_add_u32 s24, s24, 5120\n"
    "v_mfma_f32_16x16x32_bf16 v[96:99], a[232:235], a[216:219], v[96:99]\n"
    "v_mfma_f32_16x16x32_bf16 v[64:67], a[232:235], a[220:223], v[64:67]\n"
    "v_mfma_f32_16x16x32_bf16 v[116:119], a[232:235], a[224:227], v[116:119]\n"
    "s_waitcnt vmcnt(7)\n"
    "v_mfma_f32_16x16x32_bf16 v[48:51], a[236:239], a[212:215], v[48:51]\n"
    "buffer_load_dwordx4 a[252:255], v2, s[68:71], s24 offen offset:0 sc1\n"
    "v_mfma_f32_16x16x32_bf16 v[100:103], a[236:239], a[216:219], v[100:103]\n"
    "v_mfma_f32_16x16x32_bf16 v[68:71], a[236:239], a[220:223], v[68:71]\n"
    "v_mfma_f32_16x16x32_bf16 v[120:123], a[236:239], a[224:227], v[120:123]\n"
    "s_waitcnt vmcnt(7)\n"
    "v_mfma_f32_16x16x32_bf16 v[52:55], a[240:243], a[212:215], v[52:55]\n"
    "buffer_load_dwordx4 v[252:255], v2, s[68:71], s24 offen offset:1024 sc1\n"
    "v_mfma_f32_16x16x32_bf16 v[104:107], a[240:243], a[216:219], v[104:107]\n"
    "v_mfma_f32_16x16x32_bf16 v[72:75], a[240:243], a[220:223], v[72:75]\n"
    "v_mfma_f32_16x16x32_bf16 v[124:127], a[240:243], a[224:227], v[124:127]\n"
    "s_waitcnt vmcnt(7)\n"
    "v_mfma_f32_16x16x32_bf16 v[56:59], a[244:247], a[212:215], v[56:59]\n"
    "buffer_load_dwordx4 v[248:251], v2, s[68:71], s24 offen offset:2048 sc1\n"
    "v_mfma_f32_16x16x32_bf16 v[108:111], a[244:247], a[216:219], v[108:111]\n"
    "v_mfma_f32_16x16x32_bf16 v[76:79], a[244:247], a[220:223], v[76:79]\n"
    "v_mfma_f32_16x16x32_bf16 v[128:131], a[244:247], a[224:227], v[128:131]\n"
    "s_waitcnt lgkmcnt(0)\n"
    "s_waitcnt vmcnt(7)\n"
    "v_mfma_f32_16x16x32_bf16 v[40:43], a[112:115], a[96:99], v[40:43]\n"
    "buffer_load_dwordx4 v[244:247], v2, s[68:71], s24 offen offset:3072 sc1\n"
    "v_mfma_f32_16x16x32_bf16 v[92:95], a[112:115], a[100:103], v[92:95]\n"
    "ds_read_b128 a[212:215], v26 offset:1024\n"
    "ds_read_b128 a[216:219], v26 offset:17408\n"
    "v_mfma_f32_16x16x32_bf16 v[60:63], a[112:115], a[104:107], v[60:63]\n"
    "ds_read_b128 a[220:223], v26 offset:33792\n"
    "ds_read_b128 a[224:227], v26 offset:50176\n"
    "v_mfma_f32_16x16x32_bf16 v[112:115], a[112:115], a[108:111], v[112:115]\n"
    "s_waitcnt vmcnt(7)\n"
    "v_mfma_f32_16x16x32_bf16 v[44:47], a[116:119], a[96:99], v[44:47]\n"
    "buffer_load_dwordx4 v[240:243], v10, s[68:71], s24 offen offset:0 sc1\n"
    "s_add_u32 s24, s24, 5120\n"
    "v_mfma_f32_16x16x32_bf16 v[96:99], a[116:119], a[100:103], v[96:99]\n"
    "v_mfma_f32_16x16x32_bf16 v[64:67], a[116:119], a[104:107], v[64:67]\n"
    "v_mfma_f32_16x16x32_bf16 v[116:119], a[116:119], a[108:111], v[116:119]\n"
    "s_waitcnt vmcnt(7)\n"
    "v_mfma_f32_16x16x32_bf16 v[48:51], a[120:123], a[96:99], v[48:51]\n"
    "buffer_load_dwordx4 v[236:239], v2, s[68:71], s24 offen offset:0 sc1\n"
    "v_mfma_f32_16x16x32_bf16 v[100:103], a[120:123], a[100:103], v[100:103]\n"
    "v_mfma_f32_16x16x32_bf16 v[68:71], a[120:123], a[104:107], v[68:71]\n"
    "v_mfma_f32_16x16x32_bf16 v[120:123], a[120:123], a[108:111], v[120:123]\n"
    "s_waitcnt vmcnt(7)\n"
    "v_mfma_f32_16x16x32_bf16 v[52:55], a[124:127], a[96:99], v[52:55]\n"
    "buffer_load_dwordx4 v[232:235], v2, s[68:71], s24 offen offset:1024 sc1\n"
    "v_mfma_f32_16x16x32_bf16 v[104:107], a[124:127], a[100:103], v[104:107]\n"
    "v_mfma_f32_16x16x32_bf16 v[72:75], a[124:127], a[104:107], v[72:75]\n"
    "v_mfma_f32_16x16x32_bf16 v[124:127], a[124:127], a[108:111], v[124:127]\n"
    "s_waitcnt vmcnt(7)\n"
    "v_mfma_f32_16x16x32_bf16 v[56:59], a[248:251], a[96:99], v[56:59]\n"
    "buffer_load_dwordx4 v[228:231], v2, s[68:71], s24 offen offset:2048 sc1\n"
    "v_mfma_f32_16x16x32_bf16 v[108:111], a[248:251], a[100:103], v[108:111]\n"
    "v_mfma_f32_16x16x32_bf16 v[76:79], a[248:251], a[104:107], v[76:79]\n"
    "v_mfma_f32_16x16x32_bf16 v[128:131], a[248:251], a[108:111], v[128:131]\n"
    "s_waitcnt lgkmcnt(0)\n"
    "s_waitcnt vmcnt(7)\n"
    "v_mfma_f32_16x16x32_bf16 v[40:43], a[252:255], a[212:215], v[40:43]\n"
    "buffer_load_dwordx4 v[224:227], v2, s[68:71], s24 offen offset:3072 sc1\n"
    "v_mfma_f32_16x16x32_bf16 v[92:95], a[252:255], a[216:219], v[92:95]\n"
    "ds_read_b128 a[96:99], v25 offset:2048\n"
    "ds_read_b128 a[100:103], v25 offset:18432\n"
    "v_mfma_f32_16x16x32_bf16 v[60:63], a[252:255], a[220:223], v[60:63]\n"
    "ds_read_b128 a[104:107], v25 offset:34816\n"
    "ds_read_b128 a[108:111], v25 offset:51200\n"
    "v_mfma_f32_16x16x32_bf16 v[112:115], a[252:255], a[224:227], v[112:115]\n"
    "s_waitcnt vmcnt(7)\n"
    "v_mfma_f32_16x16x32_bf16 v[44:47], v[252:255], a[212:215], v[44:47]\n"
    "buffer_load_dwordx4 v[220:223], v10, s[68:71], s24 offen offset:0 sc1\n"
    "s_add_u32 s24, s24, 5120\n"
    "v_mfma_f32_16x16x32_bf16 v[96:99], v[252:255], a[216:219], v[96:99]\n"
    "v_mfma_f32_16x16x32_bf16 v[64:67], v[252:255], a[220:223], v[64:67]\n"
    "v_mfma_f32_16x16x32_bf16 v[116:119], v[252:255], a[224:227], v[116:119]\n"
    "s_waitcnt vmcnt(7)\n"
    "v_mfma_f32_16x16x32_bf16 v[48:51], v[248:251], a[212:215], v[48:51]\n"
    "buffer_load_dwordx4 v[216:219], v2, s[68:71], s24 offen offset:0 sc1\n"
    "v_mfma_f32_16x16x32_bf16 v[100:103], v[248:251], a[216:219], v[100:103]\n"
    "v_mfma_f32_16x16x32_bf16 v[68:71], v[248:251], a[220:223], v[68:71]\n"
    "v_mfma_f32_16x16x32_bf16 v[120:123], v[248:251], a[224:227], v[120:123]\n"
    "s_waitcnt vmcnt(7)\n"
    "v_mfma_f32_16x16x32_bf16 v[52:55], v[244:247], a[212:215], v[52:55]\n"
    "buffer_load_dwordx4 v[212:215], v2, s[68:71], s24 offen offset:1024 sc1\n"
    "v_mfma_f32_16x16x32_bf16 v[104:107], v[244:247], a[216:219], v[104:107]\n"
    "v_mfma_f32_16x16x32_bf16 v[72:75], v[244:247], a[220:223], v[72:75]\n"
    "v_mfma_f32_16x16x32_bf16 v[124:127], v[244:247], a[224:227], v[124:127]\n"
    "s_waitcnt vmcnt(7)\n"
    "v_mfma_f32_16x16x32_bf16 v[56:59], v[240:243], a[212:215], v[56:59]\n"
    "buffer_load_dwordx4 v[208:211], v2, s[68:71], s24 offen offset:2048 sc1\n"
    "v_mfma_f32_16x16x32_bf16 v[108:111], v[240:243], a[216:219], v[108:111]\n"
    "v_mfma_f32_16x16x32_bf16 v[76:79], v[240:243], a[220:223], v[76:79]\n"
    "v_mfma_f32_16x16x32_bf16 v[128:131], v[240:243], a[224:227], v[128:131]\n"
    "s_waitcnt lgkmcnt(0)\n"
    "s_waitcnt vmcnt(7)\n"
    "v_mfma_f32_16x16x32_bf16 v[40:43], v[236:239], a[96:99], v[40:43]\n"
    "buffer_load_dwordx4 v[204:207], v2, s[68:71], s24 offen offset:3072 sc1\n"
    "v_mfma_f32_16x16x32_bf16 v[92:95], v[236:239], a[100:103], v[92:95]\n"
    "ds_read_b128 a[212:215], v26 offset:2048\n"
    "ds_read_b128 a[216:219], v26 offset:18432\n"
    "v_mfma_f32_16x16x32_bf16 v[60:63], v[236:239], a[104:107], v[60:63]\n"
    "ds_read_b128 a[220:223], v26 offset:34816\n"
    "ds_read_b128 a[224:227], v26 offset:51200\n"
    "v_mfma_f32_16x16x32_bf16 v[112:115], v[236:239], a[108:111], v[112:115]\n"
    "s_waitcnt vmcnt(7)\n"
    "v_mfma_f32_16x16x32_bf16 v[44:47], v[232:235], a[96:99], v[44:47]\n"
    "buffer_load_dwordx4 v[200:203], v10, s[68:71], s24 offen offset:0 sc1\n"
    "s_add_u32 s24, s24, 5120\n"
    "v_mfma_f32_16x16x32_bf16 v[96:99], v[232:235], a[100:103], v[96:99]\n"
    "v_mfma_f32_16x16x32_bf16 v[64:67], v[232:235], a[104:107], v[64:67]\n"
    "v_mfma_f32_16x16x32_bf16 v[116:119], v[232:235], a[108:111], v[116:119]\n"
    "s_waitcnt vmcnt(7)\n"
    "v_mfma_f32_16x16x32_bf16 v[48:51], v[228:231], a[96:99], v[48:51]\n"
    "buffer_load_dwordx4 v[196:199], v2, s[68:71], s24 offen offset:0 sc1\n"
    "v_mfma_f32_16x16x32_bf16 v[100:103], v[228:231], a[100:103], v[100:103]\n"
    "v_mfma_f32_16x16x32_bf16 v[68:71], v[228:231], a[104:107], v[68:71]\n"
    "v_mfma_f32_16x16x32_bf16 v[120:123], v[228:231], a[108:111], v[120:123]\n"
    "s_waitcnt vmcnt(7)\n"
    "v_mfma_f32_16x16x32_bf16 v[52:55], v[224:227], a[96:99], v[52:55]\n"
    "buffer_load_dwordx4 v[192:195], v2, s[68:71], s24 offen offset:1024 sc1\n"
    "v_mfma_f32_16x16x32_bf16 v[104:107], v[224:227], a[100:103], v[104:107]\n"
    "v_mfma_f32_16x16x32_bf16 v[72:75], v[224:227], a[104:107], v[72:75]\n"
    "v_mfma_f32_16x16x32_bf16 v[124:127], v[224:227], a[108:111], v[124:127]\n"
    "s_waitcnt vmcnt(7)\n"
    "v_mfma_f32_16x16x32_bf16 v[56:59], v[220:223], a[96:99], v[56:59]\n"
    "buffer_load_dwordx4 v[188:191], v2, s[68:71], s24 offen offset:2048 sc1\n"
    "v_mfma_f32_16x16x32_bf16 v[108:111], v[220:223], a[100:103], v[108:111]\n"
    "v_mfma_f32_16x16x32_bf16 v[76:79], v[220:223], a[104:107], v[76:79]\n"
    "v_mfma_f32_16x16x32_bf16 v[128:131], v[220:223], a[108:111], v[128:131]\n"
    "s_waitcnt lgkmcnt(0)\n"
    "s_waitcnt vmcnt(7)\n"
    "v_mfma_f32_16x16x32_bf16 v[40:43], v[216:219], a[212:215], v[40:43]\n"
    "buffer_load_dwordx4 v[184:187], v2, s[68:71], s24 offen offset:3072 sc1\n"
    "v_mfma_f32_16x16x32_bf16 v[92:95], v[216:219], a[216:219], v[92:95]\n"
    "ds_read_b128 a[96:99], v25 offset:3072\n"
    "ds_read_b128 a[100:103], v25 offset:19456\n"
    "v_mfma_f32_16x16x32_bf16 v[60:63], v[216:219], a[220:223], v[60:63]\n"
    "ds_read_b128 a[104:107], v25 offset:35840\n"
    "ds_read_b128 a[108:111], v25 offset:52224\n"
    "v_mfma_f32_16x16x32_bf16 v[112:115], v[216:219], a[224:227], v[112:115]\n"
    "s_waitcnt vmcnt(7)\n"
    "v_mfma_f32_16x16x32_bf16 v[44:47], v[212:215], a[212:215], v[44:47]\n"
    "v_lshrrev_b32 v35, 2, v3\n"
    "v_lshl_add_u32 v35, v4, 3, v35\n"
    "v_mfma_f32_16x16x32_bf16 v[96:99], v[212:215], a[216:219], v[96:99]\n"
    "v_mul_u32_u24 v35, 96, v35\n"
    "v_add_u32 v35, s56, v35\n"
    "v_mfma_f32_16x16x32_bf16 v[64:67], v[212:215], a[220:223], v[64:67]\n"
    "v_lshl_add_u32 v37, v8, 1, v35\n"
    "v_lshrrev_b32 v36, 2, v8\n"
    "v_mfma_f32_16x16x32_bf16 v[116:119], v[212:215], a[224:227], v[116:119]\n"
    "v_mad_u32_u24 v35, v36, s48, v35\n"
    "v_add_u32 v36, s49, v35\n"
    "s_waitcnt vmcnt(6)\n"
    "v_mfma_f32_16x16x32_bf16 v[48:51], v[208:211], a[212:215], v[48:51]\n"
    "buffer_load_dwordx4 v[180:183], v10, s[68:71], s24 offen offset:0 sc1\n"
    "s_add_u32 s24, s24, 5120\n"
    "v_mfma_f32_16x16x32_bf16 v[100:103], v[208:211], a[216:219], v[100:103]\n"
    "buffer_load_dwordx4 v[176:179], v2, s[68:71], s24 offen offset:0 sc1\n"
    "v_mfma_f32_16x16x32_bf16 v[68:71], v[208:211], a[220:223], v[68:71]\n"
    "v_mfma_f32_16x16x32_bf16 v[120:123], v[208:211], a[224:227], v[120:123]\n"
    "s_waitcnt vmcnt(7)\n"
    "v_mfma_f32_16x16x32_bf16 v[52:55], v[204:207], a[212:215], v[52:55]\n"
    "buffer_load_dwordx4 v[172:175], v2, s[68:71], s24 offen offset:1024 sc1\n"
    "v_mfma_f32_16x16x32_bf16 v[104:107], v[204:207], a[216:219], v[104:107]\n"
    "v_mfma_f32_16x16x32_bf16 v[72:75], v[204:207], a[220:223], v[72:75]\n"
    "v_mfma_f32_16x16x32_bf16 v[124:127], v[204:207], a[224:227], v[124:127]\n"
    "s_waitcnt vmcnt(7)\n"
    "v_mfma_f32_16x16x32_bf16 v[56:59], v[200:203], a[212:215], v[56:59]\n"
    "buffer_load_dwordx4 v[168:171], v2, s[68:71], s24 offen offset:2048 sc1\n"
    "v_mfma_f32_16x16x32_bf16 v[108:111], v[200:203], a[216:219], v[108:111]\n"
    "v_mfma_f32_16x16x32_bf16 v[76:79], v[200:203], a[220:223], v[76:79]\n"
    "v_mfma_f32_16x16x32_bf16 v[128:131], v[200:203], a[224:227], v[128:131]\n"
    "s_waitcnt lgkmcnt(0)\n"
    "s_waitcnt vmcnt(7)\n"
    "v_mfma_f32_16x16x32_bf16 v[40:43], v[196:199], a[96:99], v[40:43]\n"
    "buffer_load_dwordx4 v[164:167], v2, s[68:71], s24 offen offset:3072 sc1\n"
    "v_mfma_f32_16x16x32_bf16 v[92:95], v[196:199], a[100:103], v[92:95]\n"
    "ds_read_b128 a[212:215], v26 offset:3072\n"
    "ds_read_b128 a[216:219], v26 offset:19456\n"
    "v_mfma_f32_16x16x32_bf16 v[60:63], v[196:199], a[104:107], v[60:63]\n"
    "ds_read_b128 a[220:223], v26 offset:35840\n"
    "ds_read_b128 a[224:227], v26 offset:52224\n"
    "v_mfma_f32_16x16x32_bf16 v[112:115], v[196:199], a[108:111], v[112:115]\n"
    "s_waitcnt vmcnt(7)\n"
    "v_mfma_f32_16x16x32_bf16 v[44:47], v[192:195], a[96:99], v[44:47]\n"
    "buffer_load_dwordx4 v[160:163], v10, s[68:71], s24 offen offset:0 sc1\n"
    "v_mfma_f32_16x16x32_bf16 v[96:99], v[192:195], a[100:103], v[96:99]\n"
    "v_mfma_f32_16x16x32_bf16 v[64:67], v[192:195], a[104:107], v[64:67]\n"
    "ds_read_b64_tr_b16 v[132:133], v35 offset:0\n"
    "v_mfma_f32_16x16x32_bf16 v[116:119], v[192:195], a[108:111], v[116:119]\n"
    "ds_read_b64_tr_b16 v[134:135], v35 offset:384\n"
    "ds_read_b64_tr_b16 v[136:137], v36 offset:0\n"
    "s_waitcnt vmcnt(7)\n"
    "v_mfma_f32_16x16x32_bf16 v[48:51], v[188:191], a[96:99], v[48:51]\n"
    "ds_read_b64_tr_b16 v[138:139], v36 offset:384\n"
    "ds_read_b64_tr_b16 v[140:141], v37 offset:64\n"
    "v_mfma_f32_16x16x32_bf16 v[100:103], v[188:191], a[100:103], v[100:103]\n"
    "ds_read_b64_tr_b16 v[142:143], v37 offset:448\n"
    "v_add_u32 v35, s50, v2\n"
    "v_mfma_f32_16x16x32_bf16 v[68:71], v[188:191], a[104:107], v[68:71]\n"
    "v_add_u32 v37, s47, v2\n"
    "v_lshl_add_u32 v36, v3, 2, s46\n"
    "v_mfma_f32_16x16x32_bf16 v[120:123], v[188:191], a[108:111], v[120:123]\n"
    "s_waitcnt vmcnt(6)\n"
    "v_mfma_f32_16x16x32_bf16 v[52:55], v[184:187], a[96:99], v[52:55]\n"
    "v_mfma_f32_16x16x32_bf16 v[104:107], v[184:187], a[100:103], v[104:107]\n"
    "v_mfma_f32_16x16x32_bf16 v[72:75], v[184:187], a[104:107], v[72:75]\n"
    "v_mfma_f32_16x16x32_bf16 v[124:127], v[184:187], a[108:111], v[124:127]\n"
    "s_waitcnt vmcnt(5)\n"
    "v_mfma_f32_16x16x32_bf16 v[56:59], v[180:183], a[96:99], v[56:59]\n"
    "v_mfma_f32_16x16x32_bf16 v[108:111], v[180:183], a[100:103], v[108:111]\n"
    "v_mfma_f32_16x16x32_bf16 v[76:79], v[180:183], a[104:107], v[76:79]\n"
    "v_mfma_f32_16x16x32_bf16 v[128:131], v[180:183], a[108:111], v[128:131]\n"
    "s_waitcnt lgkmcnt(6)\n"
    "s_waitcnt vmcnt(4)\n"
    "v_mfma_f32_16x16x32_bf16 v[40:43], v[176:179], a[212:215], v[40:43]\n"
    "s_waitcnt lgkmcnt(4)\n"
    "v_mfma_f32_16x16x32_bf16 v[144:147], v[20:23], v[132:135], 0\n"
    "v_mfma_f32_16x16x32_bf16 v[92:95], v[176:179], a[216:219], v[92:95]\n"
    "buffer_store_dwordx4 v[132:135], v2, s[68:71], s54 offen sc1\n"
    "v_mfma_f32_16x16x32_bf16 v[60:63], v[176:179], a[220:223], v[60:63]\n"
    "v_mfma_f32_16x16x32_bf16 v[112:115], v[176:179], a[224:227], v[112:115]\n"
    "s_waitcnt vmcnt(4)\n"
    "v_mfma_f32_16x16x32_bf16 v[44:47], v[172:175], a[212:215], v[44:47]\n"
    "v_mfma_f32_16x16x32_bf16 v[96:99], v[172:175], a[216:219], v[96:99]\n"
    "v_mfma_f32_16x16x32_bf16 v[64:67], v[172:175], a[220:223], v[64:67]\n"
    "s_mov_b64 exec, 0xffff\n"
    "buffer_store_dword v144, v36, s[68:71], s55 offen offset:0 sc1\n"
    "s_mov_b64 exec, -1\n"
    "v_mfma_f32_16x16x32_bf16 v[116:119], v[172:175], a[224:227], v[116:119]\n"
    "s_waitcnt lgkmcnt(2)\n"
    "v_mfma_f32_16x16x32_bf16 v[144:147], v[20:23], v[136:139], 0\n"
    "s_waitcnt vmcnt(4)\n"
    "v_mfma_f32_16x16x32_bf16 v[48:51], v[168:171], a[212:215], v[48:51]\n"
    "s_waitcnt lgkmcnt(0)\n"
    "s_barrier\n"
    "v_mfma_f32_16x16x32_bf16 v[100:103], v[168:171], a[216:219], v[100:103]\n"
    "ds_read_b128 a[96:99], v19 offset:16384\n"
    "ds_read_b128 a[100:103], v24 offset:16384\n"
    "v_mfma_f32_16x16x32_bf16 v[68:71], v[168:171], a[220:223], v[68:71]\n"
    "buffer_store_dwordx4 v[136:139], v35, s[68:71], s54 offen sc1\n"
    "v_mfma_f32_16x16x32_bf16 v[120:123], v[168:171], a[224:227], v[120:123]\n"
    "s_waitcnt vmcnt(4)\n"
    "v_mfma_f32_16x16x32_bf16 v[52:55], v[164:167], a[212:215], v[52:55]\n"
    "v_mfma_f32_16x16x32_bf16 v[104:107], v[164:167], a[216:219], v[104:107]\n"
    "v_mfma_f32_16x16x32_bf16 v[72:75], v[164:167], a[220:223], v[72:75]\n"
    "v_mfma_f32_16x16x32_bf16 v[124:127], v[164:167], a[224:227], v[124:127]\n"
    "s_mov_b64 exec, 0xffff\n"
    "buffer_store_dword v144, v36, s[68:71], s55 offen offset:64 sc1\n"
    "s_mov_b64 exec, -1\n"
    "s_waitcnt vmcnt(4)\n"
    "v_mfma_f32_16x16x32_bf16 v[56:59], v[160:163], a[212:215], v[56:59]\n"
    "v_mfma_f32_16x16x32_bf16 v[144:147], v[20:23], v[140:143], 0\n"
    "v_mfma_f32_16x16x32_bf16 v[108:111], v[160:163], a[216:219], v[108:111]\n"
    "buffer_store_dwordx4 v[140:143], v37, s[68:71], s54 offen sc1\n"
    "v_mfma_f32_16x16x32_bf16 v[76:79], v[160:163], a[220:223], v[76:79]\n"
    "v_mfma_f32_16x16x32_bf16 v[128:131], v[160:163], a[224:227], v[128:131]\n"
    "s_mov_b64 exec, 0xffff\n"
    "s_nop 2\n"
    "buffer_store_dword v144, v36, s[68:71], s55 offen offset:128 sc1\n"
    "s_mov_b64 exec, -1\n"
    "s_waitcnt lgkmcnt(1)\n"
    "v_mfma_f32_16x16x32_bf16 a[212:215], a[128:131], a[96:99], 0\n"
    "ds_read_b128 a[104:107], v19 offset:17408\n"
    "ds_read_b128 a[108:111], v24 offset:17408\n"
    "v_mfma_f32_16x16x32_bf16 a[216:219], a[132:135], a[96:99], 0\n"
    "v_mfma_f32_16x16x32_bf16 a[220:223], a[136:139], a[96:99], 0\n"
    "v_mfma_f32_16x16x32_bf16 a[224:227], a[140:143], a[96:99], 0\n"
    "ds_write_b128 v29, v[40:43] offset:0\n"
    "v_mfma_f32_16x16x32_bf16 v[156:159], a[144:147], a[96:99], 0\n"
    "s_waitcnt lgkmcnt(3)\n"
    "v_mfma_f32_16x16x32_bf16 a[212:215], a[148:151], a[100:103], a[212:215]\n"
    "v_mfma_f32_16x16x32_bf16 a[216:219], a[152:155], a[100:103], a[216:219]\n"
    "ds_write_b128 v29, v[44:47] offset:1024\n"
    "v_mfma_f32_16x16x32_bf16 a[220:223], a[156:159], a[100:103], a[220:223]\n"
    "s_add_u32 s17, s13, 393216\n"
    "v_mfma_f32_16x16x32_bf16 a[224:227], a[160:163], a[100:103], a[224:227]\n"
    "s_and_b32 s17, s17, 0x7ffff\n"
    "s_add_u32 s17, s17, s12\n"
    "s_add_u32 m0, s5, 81920\n"
    "v_mfma_f32_16x16x32_bf16 v[156:159], a[164:167], a[100:103], v[156:159]\n"
    "s_waitcnt lgkmcnt(2)\n"
    "buffer_load_dwordx4 v11, s[72:75], s17 offen offset:0 lds\n"
    "v_mfma_f32_16x16x32_bf16 a[212:215], a[168:171], a[104:107], a[212:215]\n"
    "ds_read_b128 a[96:99], v19 offset:18432\n"
    "ds_read_b128 a[100:103], v24 offset:18432\n"
    "v_mfma_f32_16x16x32_bf16 a[216:219], a[172:175], a[104:107], a[216:219]\n"
    "ds_write_b128 v29, v[48:51] offset:2048\n"
    "v_mfma_f32_16x16x32_bf16 a[220:223], a[176:179], a[104:107], a[220:223]\n"
    "buffer_load_dwordx4 v12, s[72:75], s17 offen offset:1024 lds\n"
    "v_mfma_f32_16x16x32_bf16 a[224:227], a[180:183], a[104:107], a[224:227]\n"
    "v_mfma_f32_16x16x32_bf16 v[156:159], a[184:187], a[104:107], v[156:159]\n"
    "ds_write_b128 v29, v[52:55] offset:3072\n"
    "v_mfma_f32_16x16x32_bf16 a[212:215], a[0:3], a[108:111], a[212:215]\n"
    "buffer_load_dwordx4 v13, s[72:75], s17 offen offset:2048 lds\n"
    "v_mfma_f32_16x16x32_bf16 a[216:219], a[4:7], a[108:111], a[216:219]\n"
    "v_mfma_f32_16x16x32_bf16 a[220:223], a[8:11], a[108:111], a[220:223]\n"
    "ds_write_b128 v29, v[56:59] offset:4096\n"
    "v_mfma_f32_16x16x32_bf16 a[224:227], a[12:15], a[108:111], a[224:227]\n"
    "buffer_load_dwordx4 v14, s[72:75], s17 offen offset:3072 lds\n"
    "s_add_u32 s17, s13, 393728\n"
    "v_mfma_f32_16x16x32_bf16 v[156:159], a[188:191], a[108:111], v[156:159]\n"
    "s_waitcnt lgkmcnt(3)\n"
    "s_and_b32 s17, s17, 0x7ffff\n"
    "s_add_u32 s17, s17, s12\n"
    "v_mfma_f32_16x16x32_bf16 a[212:215], a[16:19], a[96:99], a[212:215]\n"
    "ds_read_b128 a[104:107], v19 offset:19456\n"
    "ds_read_b128 a[108:111], v24 offset:19456\n"
    "s_add_u32 m0, s5, 65536\n"
    "v_mfma_f32_16x16x32_bf16 a[216:219], a[20:23], a[96:99], a[216:219]\n"
    "buffer_load_dwordx4 v11, s[72:75], s17 offen offset:0 lds\n"
    "v_mfma_f32_16x16x32_bf16 a[220:223], a[24:27], a[96:99], a[220:223]\n"
    "v_mfma_f32_16x16x32_bf16 a[224:227], a[28:31], a[96:99], a[224:227]\n"
    "s_waitcnt lgkmcnt(2)\n"
    "s_barrier\n"
    "v_mfma_f32_16x16x32_bf16 v[156:159], a[192:195], a[96:99], v[156:159]\n"
    "ds_read_b128 v[20:23], v30 offset:0\n"
    "ds_read_b128 v[32:35], v30 offset:1024\n"
    "v_mfma_f32_16x16x32_bf16 a[212:215], a[32:35], a[100:103], a[212:215]\n"
    "ds_read_b128 v[36:39], v30 offset:2048\n"
    "ds_read_b128 v[132:135], v30 offset:3072\n"
    "v_mfma_f32_16x16x32_bf16 a[216:219], a[36:39], a[100:103], a[216:219]\n"
    "ds_read_b128 v[136:139], v30 offset:4096\n"
    "v_mfma_f32_16x16x32_bf16 a[220:223], a[40:43], a[100:103], a[220:223]\n"
    "buffer_load_dwordx4 v12, s[72:75], s17 offen offset:1024 lds\n"
    "v_mfma_f32_16x16x32_bf16 a[224:227], a[44:47], a[100:103], a[224:227]\n"
    "v_mfma_f32_16x16x32_bf16 v[156:159], a[196:199], a[100:103], v[156:159]\n"
    "s_waitcnt lgkmcnt(5)\n"
    "v_mfma_f32_16x16x32_bf16 a[212:215], a[48:51], a[104:107], a[212:215]\n"
    "ds_read_b128 a[96:99], v19 offset:0\n"
    "ds_read_b128 a[100:103], v24 offset:0\n"
    "v_mfma_f32_16x16x32_bf16 a[216:219], a[52:55], a[104:107], a[216:219]\n"
    "s_waitcnt lgkmcnt(6)\n"
    "v_add_f32 v92, v92, v20\n"
    "v_add_f32 v93, v93, v21\n"
    "v_mfma_f32_16x16x32_bf16 a[220:223], a[56:59], a[104:107], a[220:223]\n"
    "v_cvt_pk_bf16_f32 v80, v92, v93\n"
    "v_add_f32 v94, v94, v22\n"
    "v_mfma_f32_16x16x32_bf16 a[224:227], a[60:63], a[104:107], a[224:227]\n"
    "v_add_f32 v95, v95, v23\n"
    "v_cvt_pk_bf16_f32 v81, v94, v95\n"
    "v_mfma_f32_16x16x32_bf16 v[156:159], a[200:203], a[104:107], v[156:159]\n"
    "s_waitcnt lgkmcnt(5)\n"
    "v_add_f32 v96, v96, v32\n"
    "v_add_f32 v97, v97, v33\n"
    "v_mfma_f32_16x16x32_bf16 a[212:215], a[64:67], a[108:111], a[212:215]\n"
    "v_cvt_pk_bf16_f32 v82, v96, v97\n"
    "v_add_f32 v98, v98, v34\n"
    "v_mfma_f32_16x16x32_bf16 a[216:219], a[68:71], a[108:111], a[216:219]\n"
    "v_add_f32 v99, v99, v35\n"
    "v_cvt_pk_bf16_f32 v83, v98, v99\n"
    "v_mfma_f32_16x16x32_bf16 a[220:223], a[72:75], a[108:111], a[220:223]\n"
    "buffer_store_dwordx4 v[80:83], v2, s[68:71], s30 offen offset:0 sc1\n"
    "v_mfma_f32_16x16x32_bf16 a[224:227], a[76:79], a[108:111], a[224:227]\n"
    "s_waitcnt lgkmcnt(4)\n"
    "v_add_f32 v100, v100, v36\n"
    "v_add_f32 v101, v101, v37\n"
    "v_mfma_f32_16x16x32_bf16 v[156:159], a[204:207], a[108:111], v[156:159]\n"
    "s_waitcnt lgkmcnt(0)\n"
    "v_cvt_pk_bf16_f32 v84, v100, v101\n"
    "v_mfma_f32_16x16x32_bf16 a[212:215], a[80:83], a[96:99], a[212:215]\n"
    "ds_read_b128 a[104:107], v19 offset:1024\n"
    "ds_read_b128 a[108:111], v24 offset:1024\n"
    "v_mfma_f32_16x16x32_bf16 a[216:219], a[84:87], a[96:99], a[216:219]\n"
    "v_add_f32 v102, v102, v38\n"
    "v_add_f32 v103, v103, v39\n"
    "v_mfma_f32_16x16x32_bf16 a[220:223], a[88:91], a[96:99], a[220:223]\n"
    "v_cvt_pk_bf16_f32 v85, v102, v103\n"
    "v_add_f32 v104, v104, v132\n"
    "v_mfma_f32_16x16x32_bf16 a[224:227], a[92:95], a[96:99], a[224:227]\n"
    "v_add_f32 v105, v105, v133\n"
    "v_cvt_pk_bf16_f32 v86, v104, v105\n"
    "v_mfma_f32_16x16x32_bf16 v[156:159], a[208:211], a[96:99], v[156:159]\n"
    "v_add_f32 v106, v106, v134\n"
    "v_add_f32 v107, v107, v135\n"
    "v_mfma_f32_16x16x32_bf16 a[212:215], a[228:231], a[100:103], a[212:215]\n"
    "v_cvt_pk_bf16_f32 v87, v106, v107\n"
    "v_mfma_f32_16x16x32_bf16 a[216:219], a[232:235], a[100:103], a[216:219]\n"
    "s_barrier\n"
    "v_mfma_f32_16x16x32_bf16 a[220:223], a[236:239], a[100:103], a[220:223]\n"
    "ds_write_b128 v29, v[60:63] offset:0\n"
    "v_mfma_f32_16x16x32_bf16 a[224:227], a[240:243], a[100:103], a[224:227]\n"
    "buffer_store_dwordx4 v[84:87], v2, s[68:71], s30 offen offset:1024 sc1\n"
    "v_mfma_f32_16x16x32_bf16 v[156:159], a[244:247], a[100:103], v[156:159]\n"
    "s_waitcnt lgkmcnt(1)\n"
    "v_add_f32 v108, v108, v136\n"
    "v_mfma_f32_16x16x32_bf16 a[212:215], a[112:115], a[104:107], a[212:215]\n"
    "ds_read_b128 a[96:99], v19 offset:2048\n"
    "ds_read_b128 a[100:103], v24 offset:2048\n"
    "v_mfma_f32_16x16x32_bf16 a[216:219], a[116:119], a[104:107], a[216:219]\n"
    "ds_write_b128 v29, v[64:67] offset:1024\n"
    "v_mfma_f32_16x16x32_bf16 a[220:223], a[120:123], a[104:107], a[220:223]\n"
    "v_add_f32 v109, v109, v137\n"
    "v_cvt_pk_bf16_f32 v88, v108, v109\n"
    "v_mfma_f32_16x16x32_bf16 a[224:227], a[124:127], a[104:107], a[224:227]\n"
    "v_add_f32 v110, v110, v138\n"
    "v_add_f32 v111, v111, v139\n"
    "v_mfma_f32_16x16x32_bf16 v[156:159], a[248:251], a[104:107], v[156:159]\n"
    "ds_write_b128 v29, v[68:71] offset:2048\n"
    "v_mfma_f32_16x16x32_bf16 a[212:215], a[252:255], a[108:111], a[212:215]\n"
    "v_cvt_pk_bf16_f32 v89, v110, v111\n"
    "v_mfma_f32_16x16x32_bf16 a[216:219], v[252:255], a[108:111], a[216:219]\n"
    "buffer_store_dwordx2 v[88:89], v27, s[68:71], s30 offen offset:2048 sc1\n"
    "v_mfma_f32_16x16x32_bf16 a[220:223], v[248:251], a[108:111], a[220:223]\n"
    "ds_write_b128 v29, v[72:75] offset:3072\n"
    "v_mfma_f32_16x16x32_bf16 a[224:227], v[244:247], a[108:111], a[224:227]\n"
    "s_waitcnt vmcnt(9)\n"
    "s_mov_b64 exec, 1\n"
    "buffer_store_dword v5, v9, s[68:71], s16 offen offset:0 sc1\n"
    "s_mov_b64 exec, -1\n"
    "v_mfma_f32_16x16x32_bf16 v[156:159], v[240:243], a[108:111], v[156:159]\n"
    "s_waitcnt lgkmcnt(3)\n"
    "v_mfma_f32_16x16x32_bf16 a[212:215], v[236:239], a[96:99], a[212:215]\n"
    "ds_read_b128 a[104:107], v19 offset:3072\n"
    "ds_read_b128 a[108:111], v24 offset:3072\n"
    "v_mfma_f32_16x16x32_bf16 a[216:219], v[232:235], a[96:99], a[216:219]\n"
    "ds_write_b128 v29, v[76:79] offset:4096\n"
    "v_mfma_f32_16x16x32_bf16 a[220:223], v[228:231], a[96:99], a[220:223]\n"
    "buffer_load_dwordx4 v13, s[72:75], s17 offen offset:2048 lds\n"
    "v_mfma_f32_16x16x32_bf16 a[224:227], v[224:227], a[96:99], a[224:227]\n"
    "v_mfma_f32_16x16x32_bf16 v[156:159], v[220:223], a[96:99], v[156:159]\n"
    "v_mfma_f32_16x16x32_bf16 a[212:215], v[216:219], a[100:103], a[212:215]\n"
    "v_mfma_f32_16x16x32_bf16 a[216:219], v[212:215], a[100:103], a[216:219]\n"
    "buffer_load_dwordx4 v14, s[72:75], s17 offen offset:3072 lds\n"
    "s_add_u32 s17, s13, 393216\n"
    "s_xor_b32 s17, s17, 0x10000\n"
    "v_mfma_f32_16x16x32_bf16 a[220:223], v[208:211], a[100:103], a[220:223]\n"
    "s_waitcnt lgkmcnt(0)\n"
    "s_barrier\n"
    "s_and_b32 s17, s17, 0x7ffff\n"
    "s_add_u32 s17, s17, s12\n"
    "v_mfma_f32_16x16x32_bf16 a[224:227], v[204:207], a[100:103], a[224:227]\n"
    "ds_read_b128 v[20:23], v30 offset:0\n"
    "ds_read_b128 v[32:35], v30 offset:1024\n"
    "s_add_u32 m0, s5, 114688\n"
    "v_mfma_f32_16x16x32_bf16 v[156:159], v[200:203], a[100:103], v[156:159]\n"
    "ds_read_b128 v[36:39], v30 offset:2048\n"
    "buffer_load_dwordx4 v11, s[72:75], s17 offen offset:0 lds\n"
    "v_mfma_f32_16x16x32_bf16 a[212:215], v[196:199], a[104:107], a[212:215]\n"
    "ds_read_b128 a[96:99], v19 offset:49152\n"
    "ds_read_b128 a[100:103], v24 offset:49152\n"
    "v_mfma_f32_16x16x32_bf16 a[216:219], v[192:195], a[104:107], a[216:219]\n"
    "ds_read_b128 v[132:135], v30 offset:3072\n"
    "ds_read_b128 v[136:139], v30 offset:4096\n"
    "v_mfma_f32_16x16x32_bf16 a[220:223], v[188:191], a[104:107], a[220:223]\n"
    "v_mfma_f32_16x16x32_bf16 a[224:227], v[184:187], a[104:107], a[224:227]\n"
    "buffer_load_dwordx4 v12, s[72:75], s17 offen offset:1024 lds\n"
    "v_mfma_f32_16x16x32_bf16 v[156:159], v[180:183], a[104:107], v[156:159]\n"
    "v_mfma_f32_16x16x32_bf16 a[212:215], v[176:179], a[108:111], a[212:215]\n"
    "v_mfma_f32_16x16x32_bf16 a[216:219], v[172:175], a[108:111], a[216:219]\n"
    "v_mfma_f32_16x16x32_bf16 a[220:223], v[168:171], a[108:111], a[220:223]\n"
    "buffer_load_dwordx4 v13, s[72:75], s17 offen offset:2048 lds\n"
    "v_mfma_f32_16x16x32_bf16 a[224:227], v[164:167], a[108:111], a[224:227]\n"
    "v_mfma_f32_16x16x32_bf16 v[156:159], v[160:163], a[108:111], v[156:159]\n"
    "s_waitcnt lgkmcnt(2)\n"
    "v_mfma_f32_16x16x32_bf16 v[92:95], a[128:131], a[96:99], 0\n"
    "ds_read_b128 a[104:107], v19 offset:50176\n"
    "ds_read_b128 a[108:111], v24 offset:50176\n"
    "v_mfma_f32_16x16x32_bf16 v[96:99], a[132:135], a[96:99], 0\n"
    "buffer_load_dwordx4 v14, s[72:75], s17 offen offset:3072 lds\n"
    "v_mfma_f32_16x16x32_bf16 v[100:103], a[136:139], a[96:99], 0\n"
    "v_add_f32 v112, v112, v20\n"
    "v_add_f32 v113, v113, v21\n"
    "v_mfma_f32_16x16x32_bf16 v[104:107], a[140:143], a[96:99], 0\n"
    "v_cvt_pk_bf16_f32 v80, v112, v113\n"
    "v_add_f32 v114, v114, v22\n"
    "v_mfma_f32_16x16x32_bf16 v[108:111], a[144:147], a[96:99], 0\n"
    "v_add_f32 v115, v115, v23\n"
    "v_cvt_pk_bf16_f32 v81, v114, v115\n"
    "v_mfma_f32_16x16x32_bf16 v[92:95], a[148:151], a[100:103], v[92:95]\n"
    "v_add_f32 v116, v116, v32\n"
    "v_add_f32 v117, v117, v33\n"
    "v_mfma_f32_16x16x32_bf16 v[96:99], a[152:155], a[100:103], v[96:99]\n"
    "v_cvt_pk_bf16_f32 v82, v116, v117\n"
    "v_add_f32 v118, v118, v34\n"
    "v_mfma_f32_16x16x32_bf16 v[100:103], a[156:159], a[100:103], v[100:103]\n"
    "v_add_f32 v119, v119, v35\n"
    "v_cvt_pk_bf16_f32 v83, v118, v119\n"
    "v_mfma_f32_16x16x32_bf16 v[104:107], a[160:163], a[100:103], v[104:107]\n"
    "buffer_store_dwordx4 v[80:83], v2, s[68:71], s31 offen offset:0 sc1\n"
    "v_mfma_f32_16x16x32_bf16 v[108:111], a[164:167], a[100:103], v[108:111]\n"
    "s_waitcnt lgkmcnt(0)\n"
    "v_add_f32 v120, v120, v36\n"
    "v_mfma_f32_16x16x32_bf16 v[92:95], a[168:171], a[104:107], v[92:95]\n"
    "ds_read_b128 a[96:99], v19 offset:51200\n"
    "ds_read_b128 a[100:103], v24 offset:51200\n"
    "v_mfma_f32_16x16x32_bf16 v[96:99], a[172:175], a[104:107], v[96:99]\n"
    "v_add_f32 v121, v121, v37\n"
    "v_cvt_pk_bf16_f32 v84, v120, v121\n"
    "v_mfma_f32_16x16x32_bf16 v[100:103], a[176:179], a[104:107], v[100:103]\n"
    "v_add_f32 v122, v122, v38\n"
    "v_add_f32 v123, v123, v39\n"
    "v_mfma_f32_16x16x32_bf16 v[104:107], a[180:183], a[104:107], v[104:107]\n"
    "v_cvt_pk_bf16_f32 v85, v122, v123\n"
    "v_add_f32 v124, v124, v132\n"
    "v_mfma_f32_16x16x32_bf16 v[108:111], a[184:187], a[104:107], v[108:111]\n"
    "v_add_f32 v125, v125, v133\n"
    "v_cvt_pk_bf16_f32 v86, v124, v125\n"
    "v_mfma_f32_16x16x32_bf16 v[92:95], a[0:3], a[108:111], v[92:95]\n"
    "v_add_f32 v126, v126, v134\n"
    "v_add_f32 v127, v127, v135\n"
    "v_mfma_f32_16x16x32_bf16 v[96:99], a[4:7], a[108:111], v[96:99]\n"
    "v_cvt_pk_bf16_f32 v87, v126, v127\n"
    "v_mfma_f32_16x16x32_bf16 v[100:103], a[8:11], a[108:111], v[100:103]\n"
    "buffer_store_dwordx4 v[84:87], v2, s[68:71], s31 offen offset:1024 sc1\n"
    "v_mfma_f32_16x16x32_bf16 v[104:107], a[12:15], a[108:111], v[104:107]\n"
    "v_add_f32 v128, v128, v136\n"
    "v_add_f32 v129, v129, v137\n"
    "v_mfma_f32_16x16x32_bf16 v[108:111], a[188:191], a[108:111], v[108:111]\n"
    "s_waitcnt lgkmcnt(0)\n"
    "v_cvt_pk_bf16_f32 v88, v128, v129\n"
    "v_mfma_f32_16x16x32_bf16 v[92:95], a[16:19], a[96:99], v[92:95]\n"
    "ds_read_b128 a[104:107], v19 offset:52224\n"
    "ds_read_b128 a[108:111], v24 offset:52224\n"
    "v_mfma_f32_16x16x32_bf16 v[96:99], a[20:23], a[96:99], v[96:99]\n"
    "v_add_f32 v130, v130, v138\n"
    "v_add_f32 v131, v131, v139\n"
    "v_mfma_f32_16x16x32_bf16 v[100:103], a[24:27], a[96:99], v[100:103]\n"
    "v_cvt_pk_bf16_f32 v89, v130, v131\n"
    "v_mfma_f32_16x16x32_bf16 v[104:107], a[28:31], a[96:99], v[104:107]\n"
    "buffer_store_dwordx2 v[88:89], v27, s[68:71], s31 offen offset:2048 sc1\n"
    "v_mfma_f32_16x16x32_bf16 v[108:111], a[192:195], a[96:99], v[108:111]\n"
    "s_add_u32 s17, s13, 393728\n"
    "s_xor_b32 s17, s17, 0x10000\n"
    "v_mfma_f32_16x16x32_bf16 v[92:95], a[32:35], a[100:103], v[92:95]\n"
    "s_and_b32 s17, s17, 0x7ffff\n"
    "s_add_u32 s17, s17, s12\n"
    "v_mfma_f32_16x16x32_bf16 v[96:99], a[36:39], a[100:103], v[96:99]\n"
    "s_waitcnt vmcnt(10)\n"
    "s_mov_b64 exec, 1\n"
    "buffer_store_dword v5, v9, s[68:71], s42 offen offset:0 sc1\n"
    "s_mov_b64 exec, -1\n"
    "v_mfma_f32_16x16x32_bf16 v[100:103], a[40:43], a[100:103], v[100:103]\n"
    "s_add_u32 m0, s5, 98304\n"
    "v_mfma_f32_16x16x32_bf16 v[104:107], a[44:47], a[100:103], v[104:107]\n"
    "v_mfma_f32_16x16x32_bf16 v[108:111], a[196:199], a[100:103], v[108:111]\n"
    "s_waitcnt lgkmcnt(0)\n"
    "v_mfma_f32_16x16x32_bf16 v[92:95], a[48:51], a[104:107], v[92:95]\n"
    "ds_read_b128 a[96:99], v19 offset:32768\n"
    "ds_read_b128 a[100:103], v24 offset:32768\n"
    "v_mfma_f32_16x16x32_bf16 v[96:99], a[52:55], a[104:107], v[96:99]\n"
    "v_mfma_f32_16x16x32_bf16 v[100:103], a[56:59], a[104:107], v[100:103]\n"
    "v_mfma_f32_16x16x32_bf16 v[104:107], a[60:63], a[104:107], v[104:107]\n"
    "v_mfma_f32_16x16x32_bf16 v[108:111], a[200:203], a[104:107], v[108:111]\n"
    "v_mfma_f32_16x16x32_bf16 v[92:95], a[64:67], a[108:111], v[92:95]\n"
    "v_mfma_f32_16x16x32_bf16 v[96:99], a[68:71], a[108:111], v[96:99]\n"
    "v_mfma_f32_16x16x32_bf16 v[100:103], a[72:75], a[108:111], v[100:103]\n"
    "v_mfma_f32_16x16x32_bf16 v[104:107], a[76:79], a[108:111], v[104:107]\n"
    "v_mfma_f32_16x16x32_bf16 v[108:111], a[204:207], a[108:111], v[108:111]\n"
    "s_waitcnt lgkmcnt(0)\n"
    "v_mfma_f32_16x16x32_bf16 v[92:95], a[80:83], a[96:99], v[92:95]\n"
    "ds_read_b128 a[104:107], v19 offset:33792\n"
    "ds_read_b128 a[108:111], v24 offset:33792\n"
    "v_mfma_f32_16x16x32_bf16 v[96:99], a[84:87], a[96:99], v[96:99]\n"
    "v_mfma_f32_16x16x32_bf16 v[100:103], a[88:91], a[96:99], v[100:103]\n"
    "s_barrier\n"
    "v_mfma_f32_16x16x32_bf16 v[104:107], a[92:95], a[96:99], v[104:107]\n"
    "ds_write_b128 v29, a[212:215] offset:0\n"
    "v_mfma_f32_16x16x32_bf16 v[108:111], a[208:211], a[96:99], v[108:111]\n"
    "v_mfma_f32_16x16x32_bf16 v[92:95], a[228:231], a[100:103], v[92:95]\n"
    "v_mfma_f32_16x16x32_bf16 v[96:99], a[232:235], a[100:103], v[96:99]\n"
    "ds_write_b128 v29, a[216:219] offset:1024\n"
    "v_mfma_f32_16x16x32_bf16 v[100:103], a[236:239], a[100:103], v[100:103]\n"
    "v_mfma_f32_16x16x32_bf16 v[104:107], a[240:243], a[100:103], v[104:107]\n"
    "v_mfma_f32_16x16x32_bf16 v[108:111], a[244:247], a[100:103], v[108:111]\n"
    "s_waitcnt lgkmcnt(2)\n"
    "v_mfma_f32_16x16x32_bf16 v[92:95], a[112:115], a[104:107], v[92:95]\n"
    "ds_read_b128 a[96:99], v19 offset:34816\n"
    "ds_read_b128 a[100:103], v24 offset:34816\n"
    "v_mfma_f32_16x16x32_bf16 v[96:99], a[116:119], a[104:107], v[96:99]\n"
    "PAD3_%=:\n"
    "s_cbranch_execz PADRET0_%=\n"
    "ds_write_b128 v29, a[220:223] offset:2048\n"
    "v_mfma_f32_16x16x32_bf16 v[100:103], a[120:123], a[104:107], v[100:103]\n"
    "v_mfma_f32_16x16x32_bf16 v[104:107], a[124:127], a[104:107], v[104:107]\n"
    "v_mfma_f32_16x16x32_bf16 v[108:111], a[248:251], a[104:107], v[108:111]\n"
    "ds_write_b128 v29, a[224:227] offset:3072\n"
    "v_mfma_f32_16x16x32_bf16 v[92:95], a[252:255], a[108:111], v[92:95]\n"
    "v_mfma_f32_16x16x32_bf16 v[96:99], v[252:255], a[108:111], v[96:99]\n"
    "buffer_load_dwordx4 v11, s[72:75], s17 offen offset:0 lds\n"
    "v_mfma_f32_16x16x32_bf16 v[100:103], v[248:251], a[108:111], v[100:103]\n"
    "ds_write_b128 v29, v[156:159] offset:4096\n"
    "v_mfma_f32_16x16x32_bf16 v[104:107], v[244:247], a[108:111], v[104:107]\n"
    "v_mfma_f32_16x16x32_bf16 v[108:111], v[240:243], a[108:111], v[108:111]\n"
    "s_waitcnt lgkmcnt(3)\n"
    "buffer_load_dwordx4 v12, s[72:75], s17 offen offset:1024 lds\n"
    "v_mfma_f32_16x16x32_bf16 v[92:95], v[236:239], a[96:99], v[92:95]\n"
    "ds_read_b128 a[104:107], v19 offset:35840\n"
    "ds_read_b128 a[108:111], v24 offset:35840\n"
    "v_mfma_f32_16x16x32_bf16 v[96:99], v[232:235], a[96:99], v[96:99]\n"
    "v_mfma_f32_16x16x32_bf16 v[100:103], v[228:231], a[96:99], v[100:103]\n"
    "v_mfma_f32_16x16x32_bf16 v[104:107], v[224:227], a[96:99], v[104:107]\n"
    "s_waitcnt vmcnt(10) lgkmcnt(2)\n"
    "s_barrier\n"
    "buffer_load_dwordx4 v13, s[72:75], s17 offen offset:2048 lds\n"
    "v_mfma_f32_16x16x32_bf16 v[108:111], v[220:223], a[96:99], v[108:111]\n"
    "ds_read_b128 v[20:23], v30 offset:0\n"
    "ds_read_b128 v[32:35], v30 offset:1024\n"
    "v_mfma_f32_16x16x32_bf16 v[92:95], v[216:219], a[100:103], v[92:95]\n"
    "ds_read_b128 v[36:39], v30 offset:2048\n"
    "ds_read_b128 v[132:135], v30 offset:3072\n"
    "v_mfma_f32_16x16x32_bf16 v[96:99], v[212:215], a[100:103], v[96:99]\n"
    "ds_read_b128 v[136:139], v30 offset:4096\n"
    "v_mfma_f32_16x16x32_bf16 v[100:103], v[208:211], a[100:103], v[100:103]\n"
    "buffer_load_dwordx4 v14, s[72:75], s17 offen offset:3072 lds\n"
    "s_xor_b32 s64, s58, 0\n"
    "s_add_u32 m0, s59, 0\n"
    "v_mfma_f32_16x16x32_bf16 v[104:107], v[204:207], a[100:103], v[104:107]\n"
    "v_mfma_f32_16x16x32_bf16 v[108:111], v[200:203], a[100:103], v[108:111]\n"
    "s_waitcnt lgkmcnt(5)\n"
    "v_mfma_f32_16x16x32_bf16 v[92:95], v[196:199], a[104:107], v[92:95]\n"
    "ds_read_b128 a[96:99], v25 offset:16384\n"
    "ds_read_b128 a[100:103], v26 offset:16384\n"
    "buffer_load_dwordx4 v11, s[76:79], s64 offen offset:0 lds\n"
    "v_mfma_f32_16x16x32_bf16 v[96:99], v[192:195], a[104:107], v[96:99]\n"
    "v_mfma_f32_16x16x32_bf16 v[100:103], v[188:191], a[104:107], v[100:103]\n"
    "v_mfma_f32_16x16x32_bf16 v[104:107], v[184:187], a[104:107], v[104:107]\n"
    "v_mfma_f32_16x16x32_bf16 v[108:111], v[180:183], a[104:107], v[108:111]\n"
    "s_waitcnt vmcnt(6)\n"
    "s_mov_b64 exec, 1\n"
    "buffer_store_dword v5, v9, s[68:71], s43 offen offset:4 sc1\n"
    "s_mov_b64 exec, -1\n"
    "v_mfma_f32_16x16x32_bf16 v[92:95], v[176:179], a[108:111], v[92:95]\n"
    "buffer_load_dwordx4 v12, s[76:79], s64 offen offset:1024 lds\n"
    "v_mfma_f32_16x16x32_bf16 v[96:99], v[172:175], a[108:111], v[96:99]\n"
    "v_mfma_f32_16x16x32_bf16 v[100:103], v[168:171], a[108:111], v[100:103]\n"
    "v_mfma_f32_16x16x32_bf16 v[104:107], v[164:167], a[108:111], v[104:107]\n"
    "v_mfma_f32_16x16x32_bf16 v[108:111], v[160:163], a[108:111], v[108:111]\n"
    "s_waitcnt lgkmcnt(0)\n"
    "buffer_load_dwordx4 v13, s[76:79], s64 offen offset:2048 lds\n"
    "v_mfma_f32_16x16x32_bf16 a[212:215], a[128:131], a[96:99], 0\n"
    "ds_read_b128 a[104:107], v25 offset:17408\n"
    "ds_read_b128 a[108:111], v26 offset:17408\n"
    "v_mfma_f32_16x16x32_bf16 a[216:219], a[132:135], a[96:99], 0\n"
    "v_mfma_f32_16x16x32_bf16 a[220:223], a[136:139], a[96:99], 0\n"
    "v_mfma_f32_16x16x32_bf16 a[224:227], a[140:143], a[96:99], 0\n"
    "v_add_f32 v92, v92, v20\n"
    "v_add_f32 v93, v93, v21\n"
    "v_cvt_pk_bf16_f32 v80, v92, v93\n"
    "v_mfma_f32_16x16x32_bf16 v[156:159], a[144:147], a[96:99], 0\n"
    "v_add_f32 v94, v94, v22\n"
    "v_add_f32 v95, v95, v23\n"
    "v_cvt_pk_bf16_f32 v81, v94, v95\n"
    "v_mfma_f32_16x16x32_bf16 a[212:215], a[148:151], a[100:103], a[212:215]\n"
    "v_add_f32 v96, v96, v32\n"
    "v_add_f32 v97, v97, v33\n"
    "v_cvt_pk_bf16_f32 v82, v96, v97\n"
    "v_mfma_f32_16x16x32_bf16 a[216:219], a[152:155], a[100:103], a[216:219]\n"
    "v_add_f32 v98, v98, v34\n"
    "v_add_f32 v99, v99, v35\n"
    "v_cvt_pk_bf16_f32 v83, v98, v99\n"
    "v_mfma_f32_16x16x32_bf16 a[220:223], a[156:159], a[100:103], a[220:223]\n"
    "buffer_store_dwordx4 v[80:83], v2, s[68:71], s41 offen offset:0 sc1\n"
    "v_add_f32 v100, v100, v36\n"
    "v_add_f32 v101, v101, v37\n"
    "v_mfma_f32_16x16x32_bf16 a[224:227], a[160:163], a[100:103], a[224:227]\n"
    "v_cvt_pk_bf16_f32 v84, v100, v101\n"
    "v_add_f32 v102, v102, v38\n"
    "v_add_f32 v103, v103, v39\n"
    "v_mfma_f32_16x16x32_bf16 v[156:159], a[164:167], a[100:103], v[156:159]\n"
    "s_waitcnt lgkmcnt(0)\n"
    "v_cvt_pk_bf16_f32 v85, v102, v103\n"
    "v_add_f32 v104, v104, v132\n"
    "v_add_f32 v105, v105, v133\n"
    "v_mfma_f32_16x16x32_bf16 a[212:215], a[168:171], a[104:107], a[212:215]\n"
    "ds_read_b128 a[96:99], v25 offset:18432\n"
    "ds_read_b128 a[100:103], v26 offset:18432\n"
    "v_cvt_pk_bf16_f32 v86, v104, v105\n"
    "v_add_f32 v106, v106, v134\n"
    "v_add_f32 v107, v107, v135\n"
    "v_mfma_f32_16x16x32_bf16 a[216:219], a[172:175], a[104:107], a[216:219]\n"
    "v_cvt_pk_bf16_f32 v87, v106, v107\n"
    "buffer_store_dwordx4 v[84:87], v2, s[68:71], s41 offen offset:1024 sc1\n"
    "v_add_f32 v108, v108, v136\n"
    "v_mfma_f32_16x16x32_bf16 a[220:223], a[176:179], a[104:107], a[220:223]\n"
    "v_add_f32 v109, v109, v137\n"
    "v_cvt_pk_bf16_f32 v88, v108, v109\n"
    "v_add_f32 v110, v110, v138\n"
    "v_mfma_f32_16x16x32_bf16 a[224:227], a[180:183], a[104:107], a[224:227]\n"
    "v_add_f32 v111, v111, v139\n"
    "v_cvt_pk_bf16_f32 v89, v110, v111\n"
    "buffer_store_dwordx2 v[88:89], v27, s[68:71], s41 offen offset:2048 sc1\n"
    "v_mfma_f32_16x16x32_bf16 v[156:159], a[184:187], a[104:107], v[156:159]\n"
    "v_mfma_f32_16x16x32_bf16 a[212:215], a[0:3], a[108:111], a[212:215]\n"
    "v_mfma_f32_16x16x32_bf16 a[216:219], a[4:7], a[108:111], a[216:219]\n"
    "s_barrier\n"
    "buffer_load_dwordx4 v14, s[76:79], s64 offen offset:3072 lds\n"
    "v_mfma_f32_16x16x32_bf16 a[220:223], a[8:11], a[108:111], a[220:223]\n"
    "s_xor_b32 s64, s58, 32768\n"
    "s_add_u32 m0, s59, 4096\n"
    "v_mfma_f32_16x16x32_bf16 a[224:227], a[12:15], a[108:111], a[224:227]\n"
    "s_mov_b64 exec, 0x1\n"
    "buffer_load_dword v28, v9, s[68:71], s29 offen offset:0 sc1\n"
    "s_mov_b64 exec, -1\n"
    "v_mfma_f32_16x16x32_bf16 v[156:159], a[188:191], a[108:111], v[156:159]\n"
    "s_waitcnt lgkmcnt(0)\n"
    "buffer_load_dwordx4 v11, s[76:79], s64 offen offset:0 lds\n"
    "v_mfma_f32_16x16x32_bf16 a[212:215], a[16:19], a[96:99], a[212:215]\n"
    "ds_read_b128 a[104:107], v25 offset:19456\n"
    "ds_read_b128 a[108:111], v26 offset:19456\n"
    "v_mfma_f32_16x16x32_bf16 a[216:219], a[20:23], a[96:99], a[216:219]\n"
    "s_lshr_b64 exec, -1, 32\n"
    "buffer_load_dword v15, v9, s[68:71], s52 offen offset:0 sc1\n"
    "s_mov_b64 exec, -1\n"
    "v_mfma_f32_16x16x32_bf16 a[220:223], a[24:27], a[96:99], a[220:223]\n"
    "s_lshl2_add_u32 s38, s27, s10\n"
    "s_lshl1_add_u32 s38, s38, s37\n"
    "v_mfma_f32_16x16x32_bf16 a[224:227], a[28:31], a[96:99], a[224:227]\n"
    "s_mul_i32 s38, s38, 5120\n"
    "s_add_u32 s38, s38, 14417920\n"
    "v_mfma_f32_16x16x32_bf16 v[156:159], a[192:195], a[96:99], v[156:159]\n"
    "s_lshl_b32 s39, s36, 11\n"
    "s_add_u32 s16, s38, s39\n"
    "v_mfma_f32_16x16x32_bf16 a[212:215], a[32:35], a[100:103], a[212:215]\n"
    "s_lshl_b32 s39, s36, 9\n"
    "s_add_u32 s38, s38, s39\n"
    "v_mfma_f32_16x16x32_bf16 a[216:219], a[36:39], a[100:103], a[216:219]\n"
    "s_add_u32 s26, s38, 4096\n"
    "v_lshrrev_b32 v0, 1, v4\n"
    "v_mfma_f32_16x16x32_bf16 a[220:223], a[40:43], a[100:103], a[220:223]\n"
    "v_lshlrev_b32 v91, 4, v3\n"
    "v_lshl_add_u32 v90, v0, 8, v91\n"
    "v_mfma_f32_16x16x32_bf16 a[224:227], a[44:47], a[100:103], a[224:227]\n"
    "v_lshl_add_u32 v91, v0, 2, v91\n"
    "v_and_b32 v0, 1, v4\n"
    "v_mfma_f32_16x16x32_bf16 v[156:159], a[196:199], a[100:103], v[156:159]\n"
    "s_waitcnt lgkmcnt(0)\n"
    "v_lshl_add_u32 v90, v0, 3, v90\n"
    "v_mfma_f32_16x16x32_bf16 a[212:215], a[48:51], a[104:107], a[212:215]\n"
    "ds_read_b128 a[96:99], v25 offset:0\n"
    "ds_read_b128 a[100:103], v26 offset:0\n"
    "v_mfma_f32_16x16x32_bf16 a[216:219], a[52:55], a[104:107], a[216:219]\n"
    "s_lshl1_add_u32 s38, s27, s37\n"
    "s_lshl2_add_u32 s38, s38, s10\n"
    "v_mfma_f32_16x16x32_bf16 a[220:223], a[56:59], a[104:107], a[220:223]\n"
    "s_lshl_b32 s38, s38, 8\n"
    "s_lshl3_add_u32 s38, s36, s38\n"
    "v_mfma_f32_16x16x32_bf16 a[224:227], a[60:63], a[104:107], a[224:227]\n"
    "s_add_u32 s24, s38, 208896\n"
    "v_mfma_f32_16x16x32_bf16 v[156:159], a[200:203], a[104:107], v[156:159]\n"
    "s_mul_i32 s38, s27, 1152\n"
    "s_lshl2_add_u32 s38, s10, s38\n"
    "v_mfma_f32_16x16x32_bf16 a[212:215], a[64:67], a[108:111], a[212:215]\n"
    "s_add_u32 s25, s38, 4224\n"
    "v_mfma_f32_16x16x32_bf16 a[216:219], a[68:71], a[108:111], a[216:219]\n"
    "buffer_load_dwordx4 v12, s[76:79], s64 offen offset:1024 lds\n"
    "v_mfma_f32_16x16x32_bf16 a[220:223], a[72:75], a[108:111], a[220:223]\n"
    "v_mfma_f32_16x16x32_bf16 a[224:227], a[76:79], a[108:111], a[224:227]\n"
    "v_mfma_f32_16x16x32_bf16 v[156:159], a[204:207], a[108:111], v[156:159]\n"
    "s_waitcnt lgkmcnt(0)\n"
    "v_mfma_f32_16x16x32_bf16 a[212:215], a[80:83], a[96:99], a[212:215]\n"
    "ds_read_b128 a[104:107], v25 offset:1024\n"
    "ds_read_b128 a[108:111], v26 offset:1024\n"
    "v_mfma_f32_16x16x32_bf16 a[216:219], a[84:87], a[96:99], a[216:219]\n"
    "buffer_load_dwordx4 v13, s[76:79], s64 offen offset:2048 lds\n"
    "v_mfma_f32_16x16x32_bf16 a[220:223], a[88:91], a[96:99], a[220:223]\n"
    "v_mfma_f32_16x16x32_bf16 a[224:227], a[92:95], a[96:99], a[224:227]\n"
    "v_mfma_f32_16x16x32_bf16 v[156:159], a[208:211], a[96:99], v[156:159]\n"
    "v_mfma_f32_16x16x32_bf16 a[212:215], a[228:231], a[100:103], a[212:215]\n"
    "buffer_load_dwordx4 v14, s[76:79], s64 offen offset:3072 lds\n"
    "v_mfma_f32_16x16x32_bf16 a[216:219], a[232:235], a[100:103], a[216:219]\n"
    "s_xor_b32 s64, s58, 512\n"
    "s_add_u32 m0, s59, 32768\n"
    "v_mfma_f32_16x16x32_bf16 a[220:223], a[236:239], a[100:103], a[220:223]\n"
    "v_mfma_f32_16x16x32_bf16 a[224:227], a[240:243], a[100:103], a[224:227]\n"
    "v_mfma_f32_16x16x32_bf16 v[156:159], a[244:247], a[100:103], v[156:159]\n"
    "s_waitcnt lgkmcnt(0)\n"
    "v_mfma_f32_16x16x32_bf16 a[212:215], a[112:115], a[104:107], a[212:215]\n"
    "ds_read_b128 a[96:99], v25 offset:2048\n"
    "ds_read_b128 a[100:103], v26 offset:2048\n"
    "v_mfma_f32_16x16x32_bf16 a[216:219], a[116:119], a[104:107], a[216:219]\n"
    "buffer_load_dwordx4 v11, s[76:79], s64 offen offset:0 lds\n"
    "v_mfma_f32_16x16x32_bf16 a[220:223], a[120:123], a[104:107], a[220:223]\n"
    "v_mfma_f32_16x16x32_bf16 a[224:227], a[124:127], a[104:107], a[224:227]\n"
    "v_mfma_f32_16x16x32_bf16 v[156:159], a[248:251], a[104:107], v[156:159]\n"
    "v_mfma_f32_16x16x32_bf16 a[212:215], a[252:255], a[108:111], a[212:215]\n"
    "v_mfma_f32_16x16x32_bf16 a[216:219], v[252:255], a[108:111], a[216:219]\n"
    "v_mfma_f32_16x16x32_bf16 a[220:223], v[248:251], a[108:111], a[220:223]\n"
    "v_mfma_f32_16x16x32_bf16 a[224:227], v[244:247], a[108:111], a[224:227]\n"
    "buffer_load_dwordx4 v12, s[76:79], s64 offen offset:1024 lds\n"
    "v_mfma_f32_16x16x32_bf16 v[156:159], v[240:243], a[108:111], v[156:159]\n"
    "s_waitcnt lgkmcnt(0)\n"
    "v_mfma_f32_16x16x32_bf16 a[212:215], v[236:239], a[96:99], a[212:215]\n"
    "ds_read_b128 a[104:107], v25 offset:3072\n"
    "ds_read_b128 a[108:111], v26 offset:3072\n"
    "v_mfma_f32_16x16x32_bf16 a[216:219], v[232:235], a[96:99], a[216:219]\n"
    "s_waitcnt vmcnt(9)\n"
    "s_mov_b64 exec, 1\n"
    "buffer_store_dword v5, v9, s[68:71], s44 offen offset:8 sc1\n"
    "s_mov_b64 exec, -1\n"
    "v_mfma_f32_16x16x32_bf16 a[220:223], v[228:231], a[96:99], a[220:223]\n"
    "s_waitcnt vmcnt(6)\n"
    "v_cmp_ne_u32 vcc, s8, v15\n"
    "s_nop 0\n"
    "s_lshl_b64 vcc, vcc, 32\n"
    "s_cbranch_vccnz R9_%=\n"
    "B10_%=:\n"
    "v_mfma_f32_16x16x32_bf16 a[224:227], v[224:227], a[96:99], a[224:227]\n"
    "v_min_u32 v15, 944, v2\n"
    "v_mfma_f32_16x16x32_bf16 v[156:159], v[220:223], a[96:99], v[156:159]\n"
    "v_cmp_ne_u32 vcc, s8, v28\n"
    "s_nop 0\n"
    "s_and_b64 vcc, vcc, 0x1\n"
    "s_cbranch_vccnz R11_%=\n"
    "B12_%=:\n"
    "v_mfma_f32_16x16x32_bf16 a[212:215], v[216:219], a[100:103], a[212:215]\n"
    "s_add_u32 s40, s28, 0\n"
    "v_mfma_f32_16x16x32_bf16 a[216:219], v[212:215], a[100:103], a[216:219]\n"
    "v_mfma_f32_16x16x32_bf16 a[220:223], v[208:211], a[100:103], a[220:223]\n"
    "buffer_load_dwordx4 v[60:63], v2, s[68:71], s40 offen sc1\n"
    "v_mfma_f32_16x16x32_bf16 a[224:227], v[204:207], a[100:103], a[224:227]\n"
    "s_barrier\n"
    "v_mfma_f32_16x16x32_bf16 v[156:159], v[200:203], a[100:103], v[156:159]\n"
    "s_waitcnt lgkmcnt(0)\n"
    "v_mfma_f32_16x16x32_bf16 a[212:215], v[196:199], a[104:107], a[212:215]\n"
    "ds_read_b128 a[96:99], v25 offset:49152\n"
    "ds_read_b128 a[100:103], v26 offset:49152\n"
    "v_mfma_f32_16x16x32_bf16 a[216:219], v[192:195], a[104:107], a[216:219]\n"
    "buffer_load_dwordx4 v[64:67], v2, s[68:71], s40 offen offset:1024 sc1\n"
    "v_mfma_f32_16x16x32_bf16 a[220:223], v[188:191], a[104:107], a[220:223]\n"
    "v_mfma_f32_16x16x32_bf16 a[224:227], v[184:187], a[104:107], a[224:227]\n"
    "buffer_load_dwordx2 v[68:69], v27, s[68:71], s40 offen offset:2048 sc1\n"
    "v_mfma_f32_16x16x32_bf16 v[156:159], v[180:183], a[104:107], v[156:159]\n"
    "s_mov_b64 exec, 0x1\n"
    "buffer_load_dword v28, v9, s[68:71], s29 offen offset:4 sc1\n"
    "s_mov_b64 exec, -1\n"
    "v_mfma_f32_16x16x32_bf16 a[212:215], v[176:179], a[108:111], a[212:215]\n"
    "v_mfma_f32_16x16x32_bf16 a[216:219], v[172:175], a[108:111], a[216:219]\n"
    "v_mfma_f32_16x16x32_bf16 a[220:223], v[168:171], a[108:111], a[220:223]\n"
    "v_mfma_f32_16x16x32_bf16 a[224:227], v[164:167], a[108:111], a[224:227]\n"
    "v_mfma_f32_16x16x32_bf16 v[156:159], v[160:163], a[108:111], v[156:159]\n"
    "s_waitcnt lgkmcnt(0)\n"
    "v_mfma_f32_16x16x32_bf16 v[92:95], a[128:131], a[96:99], 0\n"
    "ds_read_b128 a[104:107], v25 offset:50176\n"
    "ds_read_b128 a[108:111], v26 offset:50176\n"
    "v_mfma_f32_16x16x32_bf16 v[96:99], a[132:135], a[96:99], 0\n"
    "v_mfma_f32_16x16x32_bf16 v[100:103], a[136:139], a[96:99], 0\n"
    "buffer_load_dwordx4 v13, s[76:79], s64 offen offset:2048 lds\n"
    "v_mfma_f32_16x16x32_bf16 v[104:107], a[140:143], a[96:99], 0\n"
    "ds_write_b128 v29, a[212:215] offset:0\n"
    "v_mfma_f32_16x16x32_bf16 v[108:111], a[144:147], a[96:99], 0\n"
    "v_mfma_f32_16x16x32_bf16 v[92:95], a[148:151], a[100:103], v[92:95]\n"
    "buffer_load_dwordx4 v14, s[76:79], s64 offen offset:3072 lds\n"
    "s_xor_b32 s64, s58, 33280\n"
    "s_add_u32 m0, s59, 36864\n"
    "v_mfma_f32_16x16x32_bf16 v[96:99], a[152:155], a[100:103], v[96:99]\n"
    "ds_write_b128 v29, a[216:219] offset:1024\n"
    "v_mfma_f32_16x16x32_bf16 v[100:103], a[156:159], a[100:103], v[100:103]\n"
    "v_mfma_f32_16x16x32_bf16 v[104:107], a[160:163], a[100:103], v[104:107]\n"
    "buffer_load_dwordx4 v11, s[76:79], s64 offen offset:0 lds\n"
    "v_mfma_f32_16x16x32_bf16 v[108:111], a[164:167], a[100:103], v[108:111]\n"
    "s_waitcnt lgkmcnt(2)\n"
    "v_mfma_f32_16x16x32_bf16 v[92:95], a[168:171], a[104:107], v[92:95]\n"
    "ds_read_b128 a[96:99], v25 offset:51200\n"
    "ds_read_b128 a[100:103], v26 offset:51200\n"
    "v_mfma_f32_16x16x32_bf16 v[96:99], a[172:175], a[104:107], v[96:99]\n"
    "ds_write_b128 v29, a[220:223] offset:2048\n"
    "v_mfma_f32_16x16x32_bf16 v[100:103], a[176:179], a[104:107], v[100:103]\n"
    "buffer_load_dwordx4 v12, s[76:79], s64 offen offset:1024 lds\n"
    "v_mfma_f32_16x16x32_bf16 v[104:107], a[180:183], a[104:107], v[104:107]\n"
    "v_mfma_f32_16x16x32_bf16 v[108:111], a[184:187], a[104:107], v[108:111]\n"
    "ds_write_b128 v29, a[224:227] offset:3072\n"
    "v_mfma_f32_16x16x32_bf16 v[92:95], a[0:3], a[108:111], v[92:95]\n"
    "buffer_load_dwordx4 v13, s[76:79], s64 offen offset:2048 lds\n"
    "v_mfma_f32_16x16x32_bf16 v[96:99], a[4:7], a[108:111], v[96:99]\n"
    "v_mfma_f32_16x16x32_bf16 v[100:103], a[8:11], a[108:111], v[100:103]\n"
    "ds_write_b128 v29, v[156:159] offset:4096\n"
    "v_mfma_f32_16x16x32_bf16 v[104:107], a[12:15], a[108:111], v[104:107]\n"
    "v_mfma_f32_16x16x32_bf16 v[108:111], a[188:191], a[108:111], v[108:111]\n"
    "s_waitcnt lgkmcnt(3)\n"
    "v_mfma_f32_16x16x32_bf16 v[92:95], a[16:19], a[96:99], v[92:95]\n"
    "ds_read_b128 a[104:107], v25 offset:52224\n"
    "ds_read_b128 a[108:111], v26 offset:52224\n"
    "v_mfma_f32_16x16x32_bf16 v[96:99], a[20:23], a[96:99], v[96:99]\n"
    "v_mfma_f32_16x16x32_bf16 v[100:103], a[24:27], a[96:99], v[100:103]\n"
    "v_mfma_f32_16x16x32_bf16 v[104:107], a[28:31], a[96:99], v[104:107]\n"
    "s_waitcnt lgkmcnt(2)\n"
    "s_barrier\n"
    "v_mfma_f32_16x16x32_bf16 v[108:111], a[192:195], a[96:99], v[108:111]\n"
    "ds_read_b128 v[20:23], v30 offset:0\n"
    "ds_read_b128 v[32:35], v30 offset:1024\n"
    "v_mfma_f32_16x16x32_bf16 v[92:95], a[32:35], a[100:103], v[92:95]\n"
    "ds_read_b128 v[36:39], v30 offset:2048\n"
    "ds_read_b128 v[132:135], v30 offset:3072\n"
    "v_mfma_f32_16x16x32_bf16 v[96:99], a[36:39], a[100:103], v[96:99]\n"
    "ds_read_b128 v[136:139], v30 offset:4096\n"
    "v_mfma_f32_16x16x32_bf16 v[100:103], a[40:43], a[100:103], v[100:103]\n"
    "buffer_load_dwordx4 v14, s[76:79], s64 offen offset:3072 lds\n"
    "v_mfma_f32_16x16x32_bf16 v[104:107], a[44:47], a[100:103], v[104:107]\n"
    "s_mov_b64 exec, 0x1\n"
    "buffer_load_dword v31, v9, s[68:71], s29 offen offset:8 sc1\n"
    "s_mov_b64 exec, -1\n"
    "v_mfma_f32_16x16x32_bf16 v[108:111], a[196:199], a[100:103], v[108:111]\n"
    "s_waitcnt lgkmcnt(5)\n"
    "v_mfma_f32_16x16x32_bf16 v[92:95], a[48:51], a[104:107], v[92:95]\n"
    "ds_read_b128 a[96:99], v25 offset:32768\n"
    "ds_read_b128 a[100:103], v26 offset:32768\n"
    "v_mfma_f32_16x16x32_bf16 v[96:99], a[52:55], a[104:107], v[96:99]\n"
    "s_waitcnt vmcnt(7)\n"
    "v_cmp_ne_u32 vcc, s8, v28\n"
    "s_nop 0\n"
    "s_and_b64 vcc, vcc, 0x1\n"
    "s_cbranch_vccnz R13_%=\n"
    "B14_%=:\n"
    "v_mfma_f32_16x16x32_bf16 v[100:103], a[56:59], a[104:107], v[100:103]\n"
    "v_lshlrev_b32 v40, 16, v60\n"
    "v_and_b32 v41, 0xffff0000, v60\n"
    "buffer_load_dwordx4 v[112:115], v15, s[68:71], s57 offen offset:0 sc1\n"
    "v_mfma_f32_16x16x32_bf16 v[104:107], a[60:63], a[104:107], v[104:107]\n"
    "s_add_u32 s40, s28, 2560\n"
    "v_lshlrev_b32 v42, 16, v61\n"
    "v_mfma_f32_16x16x32_bf16 v[108:111], a[200:203], a[104:107], v[108:111]\n"
    "v_and_b32 v43, 0xffff0000, v61\n"
    "v_lshlrev_b32 v44, 16, v62\n"
    "v_mfma_f32_16x16x32_bf16 v[92:95], a[64:67], a[108:111], v[92:95]\n"
    "buffer_load_dwordx4 v[70:73], v2, s[68:71], s40 offen sc1\n"
    "v_mfma_f32_16x16x32_bf16 v[96:99], a[68:71], a[108:111], v[96:99]\n"
    "v_and_b32 v45, 0xffff0000, v62\n"
    "v_lshlrev_b32 v46, 16, v63\n"
    "v_mfma_f32_16x16x32_bf16 v[100:103], a[72:75], a[108:111], v[100:103]\n"
    "buffer_load_dwordx4 v[74:77], v2, s[68:71], s40 offen offset:1024 sc1\n"
    "v_mfma_f32_16x16x32_bf16 v[104:107], a[76:79], a[108:111], v[104:107]\n"
    "v_and_b32 v47, 0xffff0000, v63\n"
    "v_lshlrev_b32 v48, 16, v64\n"
    "v_mfma_f32_16x16x32_bf16 v[108:111], a[204:207], a[108:111], v[108:111]\n"
    "s_waitcnt lgkmcnt(0)\n"
    "v_and_b32 v49, 0xffff0000, v64\n"
    "v_mfma_f32_16x16x32_bf16 v[92:95], a[80:83], a[96:99], v[92:95]\n"
    "ds_read_b128 a[104:107], v25 offset:33792\n"
    "ds_read_b128 a[108:111], v26 offset:33792\n"
    "v_mfma_f32_16x16x32_bf16 v[96:99], a[84:87], a[96:99], v[96:99]\n"
    "buffer_load_dwordx2 v[78:79], v27, s[68:71], s40 offen offset:2048 sc1\n"
    "v_mfma_f32_16x16x32_bf16 v[100:103], a[88:91], a[96:99], v[100:103]\n"
    "v_lshlrev_b32 v50, 16, v65\n"
    "v_and_b32 v51, 0xffff0000, v65\n"
    "v_mfma_f32_16x16x32_bf16 v[104:107], a[92:95], a[96:99], v[104:107]\n"
    "v_lshlrev_b32 v52, 16, v66\n"
    "v_and_b32 v53, 0xffff0000, v66\n"
    "v_mfma_f32_16x16x32_bf16 v[108:111], a[208:211], a[96:99], v[108:111]\n"
    "v_lshlrev_b32 v54, 16, v67\n"
    "v_and_b32 v55, 0xffff0000, v67\n"
    "v_mfma_f32_16x16x32_bf16 v[92:95], a[228:231], a[100:103], v[92:95]\n"
    "v_lshlrev_b32 v56, 16, v68\n"
    "v_and_b32 v57, 0xffff0000, v68\n"
    "v_mfma_f32_16x16x32_bf16 v[96:99], a[232:235], a[100:103], v[96:99]\n"
    "v_lshlrev_b32 v58, 16, v69\n"
    "v_and_b32 v59, 0xffff0000, v69\n"
    "v_mfma_f32_16x16x32_bf16 v[100:103], a[236:239], a[100:103], v[100:103]\n"
    "v_add_f32 v40, v40, v20\n"
    "v_add_f32 v41, v41, v21\n"
    "v_mfma_f32_16x16x32_bf16 v[104:107], a[240:243], a[100:103], v[104:107]\n"
    "v_add_f32 v42, v42, v22\n"
    "v_add_f32 v43, v43, v23\n"
    "v_mfma_f32_16x16x32_bf16 v[108:111], a[244:247], a[100:103], v[108:111]\n"
    "s_waitcnt lgkmcnt(0)\n"
    "v_add_f32 v44, v44, v32\n"
    "v_mfma_f32_16x16x32_bf16 v[92:95], a[112:115], a[104:107], v[92:95]\n"
    "ds_read_b128 a[96:99], v25 offset:34816\n"
    "ds_read_b128 a[100:103], v26 offset:34816\n"
    "buffer_load_dwordx4 v[116:119], v15, s[68:71], s57 offen offset:960 sc1\n"
    "v_mfma_f32_16x16x32_bf16 v[96:99], a[116:119], a[104:107], v[96:99]\n"
    "v_add_f32 v45, v45, v33\n"
    "v_add_f32 v46, v46, v34\n"
    "v_mfma_f32_16x16x32_bf16 v[100:103], a[120:123], a[104:107], v[100:103]\n"
    "v_add_f32 v47, v47, v35\n"
    "v_add_f32 v48, v48, v36\n"
    "v_mfma_f32_16x16x32_bf16 v[104:107], a[124:127], a[104:107], v[104:107]\n"
    "v_add_f32 v49, v49, v37\n"
    "v_add_f32 v50, v50, v38\n"
    "v_mfma_f32_16x16x32_bf16 v[108:111], a[248:251], a[104:107], v[108:111]\n"
    "v_add_f32 v51, v51, v39\n"
    "v_add_f32 v52, v52, v132\n"
    "buffer_load_dwordx4 v[120:123], v15, s[68:71], s57 offen offset:1920 sc1\n"
    "v_mfma_f32_16x16x32_bf16 v[92:95], a[252:255], a[108:111], v[92:95]\n"
    "v_add_f32 v53, v53, v133\n"
    "v_add_f32 v54, v54, v134\n"
    "v_mfma_f32_16x16x32_bf16 v[96:99], v[252:255], a[108:111], v[96:99]\n"
    "v_add_f32 v55, v55, v135\n"
    "v_add_f32 v56, v56, v136\n"
    "v_mfma_f32_16x16x32_bf16 v[100:103], v[248:251], a[108:111], v[100:103]\n"
    "v_add_f32 v57, v57, v137\n"
    "v_add_f32 v58, v58, v138\n"
    "v_mfma_f32_16x16x32_bf16 v[104:107], v[244:247], a[108:111], v[104:107]\n"
    "v_add_f32 v59, v59, v139\n"
    "v_mfma_f32_16x16x32_bf16 v[108:111], v[240:243], a[108:111], v[108:111]\n"
    "s_waitcnt lgkmcnt(0)\n"
    "buffer_load_dwordx4 v[124:127], v15, s[68:71], s57 offen offset:2880 sc1\n"
    "v_mfma_f32_16x16x32_bf16 v[92:95], v[236:239], a[96:99], v[92:95]\n"
    "ds_read_b128 a[104:107], v25 offset:35840\n"
    "ds_read_b128 a[108:111], v26 offset:35840\n"
    "v_mfma_f32_16x16x32_bf16 v[96:99], v[232:235], a[96:99], v[96:99]\n"
    "s_add_u32 m0, s23, 65536\n"
    "v_mfma_f32_16x16x32_bf16 v[100:103], v[228:231], a[96:99], v[100:103]\n"
    "v_mfma_f32_16x16x32_bf16 v[104:107], v[224:227], a[96:99], v[104:107]\n"
    "buffer_load_dwordx4 v2, s[68:71], s22 offen offset:0 sc1 lds\n"
    "v_mfma_f32_16x16x32_bf16 v[108:111], v[220:223], a[96:99], v[108:111]\n"
    "v_mfma_f32_16x16x32_bf16 v[92:95], v[216:219], a[100:103], v[92:95]\n"
    "v_mfma_f32_16x16x32_bf16 v[96:99], v[212:215], a[100:103], v[96:99]\n"
    "v_mfma_f32_16x16x32_bf16 v[100:103], v[208:211], a[100:103], v[100:103]\n"
    "v_mfma_f32_16x16x32_bf16 v[104:107], v[204:207], a[100:103], v[104:107]\n"
    "s_waitcnt vmcnt(8)\n"
    "v_cmp_ne_u32 vcc, s8, v31\n"
    "s_nop 0\n"
    "s_and_b64 vcc, vcc, 0x1\n"
    "s_cbranch_vccnz R15_%=\n"
    "B16_%=:\n"
    "v_mfma_f32_16x16x32_bf16 v[108:111], v[200:203], a[100:103], v[108:111]\n"
    "s_waitcnt lgkmcnt(0)\n"
    "s_add_u32 s40, s28, 5120\n"
    "v_mfma_f32_16x16x32_bf16 v[92:95], v[196:199], a[104:107], v[92:95]\n"
    "buffer_load_dwordx4 v[80:83], v2, s[68:71], s40 offen sc1\n"
    "v_mfma_f32_16x16x32_bf16 v[96:99], v[192:195], a[104:107], v[96:99]\n"
    "buffer_load_dwordx4 v[84:87], v2, s[68:71], s40 offen offset:1024 sc1\n"
    "v_mfma_f32_16x16x32_bf16 v[100:103], v[188:191], a[104:107], v[100:103]\n"
    "buffer_load_dwordx2 v[88:89], v27, s[68:71], s40 offen offset:2048 sc1\n"
    "v_mfma_f32_16x16x32_bf16 v[104:107], v[184:187], a[104:107], v[104:107]\n"
    "v_mfma_f32_16x16x32_bf16 v[108:111], v[180:183], a[104:107], v[108:111]\n"
    "v_mfma_f32_16x16x32_bf16 v[92:95], v[176:179], a[108:111], v[92:95]\n"
    "v_mfma_f32_16x16x32_bf16 v[96:99], v[172:175], a[108:111], v[96:99]\n"
    "s_barrier\n"
    "v_mfma_f32_16x16x32_bf16 v[100:103], v[168:171], a[108:111], v[100:103]\n"
    "v_mfma_f32_16x16x32_bf16 v[104:107], v[164:167], a[108:111], v[104:107]\n"
    "v_mfma_f32_16x16x32_bf16 v[108:111], v[160:163], a[108:111], v[108:111]\n"
    "buffer_load_dwordx4 v2, s[68:71], s22 offen offset:1024 sc1 lds\n"
    "s_nop 1\n"
    "v_pk_add_f32 v[40:41], v[40:41], v[92:93]\n"
    "v_pk_add_f32 v[42:43], v[42:43], v[94:95]\n"
    "v_pk_add_f32 v[44:45], v[44:45], v[96:97]\n"
    "v_pk_add_f32 v[46:47], v[46:47], v[98:99]\n"
    "v_pk_add_f32 v[48:49], v[48:49], v[100:101]\n"
    "v_pk_add_f32 v[50:51], v[50:51], v[102:103]\n"
    "v_pk_add_f32 v[52:53], v[52:53], v[104:105]\n"
    "v_pk_add_f32 v[54:55], v[54:55], v[106:107]\n"
    "v_pk_add_f32 v[56:57], v[56:57], v[108:109]\n"
    "v_pk_add_f32 v[58:59], v[58:59], v[110:111]\n"
    "s_waitcnt vmcnt(10)\n"
    "v_dot2c_f32_bf16 v40, s14, v70\n"
    "v_dot2c_f32_bf16 v41, s15, v70\n"
    "v_dot2c_f32_bf16 v42, s14, v71\n"
    "v_dot2c_f32_bf16 v43, s15, v71\n"
    "v_dot2c_f32_bf16 v44, s14, v72\n"
    "v_dot2c_f32_bf16 v45, s15, v72\n"
    "v_dot2c_f32_bf16 v46, s14, v73\n"
    "v_dot2c_f32_bf16 v47, s15, v73\n"
    "s_waitcnt vmcnt(9)\n"
    "v_dot2c_f32_bf16 v48, s14, v74\n"
    "v_dot2c_f32_bf16 v49, s15, v74\n"
    "v_dot2c_f32_bf16 v50, s14, v75\n"
    "v_dot2c_f32_bf16 v51, s15, v75\n"
    "v_dot2c_f32_bf16 v52, s14, v76\n"
    "v_dot2c_f32_bf16 v53, s15, v76\n"
    "v_dot2c_f32_bf16 v54, s14, v77\n"
    "v_dot2c_f32_bf16 v55, s15, v77\n"
    "s_waitcnt vmcnt(8)\n"
    "v_dot2c_f32_bf16 v56, s14, v78\n"
    "v_dot2c_f32_bf16 v57, s15, v78\n"
    "v_dot2c_f32_bf16 v58, s14, v79\n"
    "v_dot2c_f32_bf16 v59, s15, v79\n"
    "s_waitcnt vmcnt(3)\n"
    "v_dot2c_f32_bf16 v40, s14, v80\n"
    "v_dot2c_f32_bf16 v41, s15, v80\n"
    "v_dot2c_f32_bf16 v42, s14, v81\n"
    "v_dot2c_f32_bf16 v43, s15, v81\n"
    "v_dot2c_f32_bf16 v44, s14, v82\n"
    "v_dot2c_f32_bf16 v45, s15, v82\n"
    "v_dot2c_f32_bf16 v46, s14, v83\n"
    "v_dot2c_f32_bf16 v47, s15, v83\n"
    "s_waitcnt vmcnt(2)\n"
    "v_dot2c_f32_bf16 v48, s14, v84\n"
    "v_dot2c_f32_bf16 v49, s15, v84\n"
    "v_dot2c_f32_bf16 v50, s14, v85\n"
    "v_dot2c_f32_bf16 v51, s15, v85\n"
    "v_dot2c_f32_bf16 v52, s14, v86\n"
    "v_dot2c_f32_bf16 v53, s15, v86\n"
    "v_dot2c_f32_bf16 v54, s14, v87\n"
    "v_dot2c_f32_bf16 v55, s15, v87\n"
    "s_waitcnt vmcnt(1)\n"
    "v_dot2c_f32_bf16 v56, s14, v88\n"
    "v_dot2c_f32_bf16 v57, s15, v88\n"
    "v_dot2c_f32_bf16 v58, s14, v89\n"
    "v_dot2c_f32_bf16 v59, s15, v89\n"
    "v_cvt_pk_bf16_f32 v60, v40, v41\n"
    "v_cvt_pk_bf16_f32 v61, v42, v43\n"
    "v_cvt_pk_bf16_f32 v62, v44, v45\n"
    "v_cvt_pk_bf16_f32 v63, v46, v47\n"
    "buffer_store_dwordx4 v[60:63], v2, s[68:71], s16 offen offset:0 sc1\n"
    "v_cvt_pk_bf16_f32 v64, v48, v49\n"
    "v_cvt_pk_bf16_f32 v65, v50, v51\n"
    "v_cvt_pk_bf16_f32 v66, v52, v53\n"
    "v_cvt_pk_bf16_f32 v67, v54, v55\n"
    "buffer_store_dwordx4 v[64:67], v2, s[68:71], s16 offen offset:1024 sc1\n"
    "v_cvt_pk_bf16_f32 v68, v56, v57\n"
    "v_cvt_pk_bf16_f32 v69, v58, v59\n"
    "buffer_store_dwordx2 v[68:69], v90, s[68:71], s26 offen sc1\n"
    "v_mov_b32 v29, 0\n"
    "v_mov_b32 v30, 0\n"
    "s_mov_b32 s38, 0x3f803f80\n"
    "v_dot2c_f32_bf16 v29, s38, v60\n"
    "v_dot2c_f32_bf16 v30, v60, v60\n"
    "v_dot2c_f32_bf16 v29, s38, v61\n"
    "v_dot2c_f32_bf16 v30, v61, v61\n"
    "v_dot2c_f32_bf16 v29, s38, v62\n"
    "v_dot2c_f32_bf16 v30, v62, v62\n"
    "v_dot2c_f32_bf16 v29, s38, v63\n"
    "v_dot2c_f32_bf16 v30, v63, v63\n"
    "v_dot2c_f32_bf16 v29, s38, v64\n"
    "v_dot2c_f32_bf16 v30, v64, v64\n"
    "v_dot2c_f32_bf16 v29, s38, v65\n"
    "v_dot2c_f32_bf16 v30, v65, v65\n"
    "v_dot2c_f32_bf16 v29, s38, v66\n"
    "v_dot2c_f32_bf16 v30, v66, v66\n"
    "v_dot2c_f32_bf16 v29, s38, v67\n"
    "v_dot2c_f32_bf16 v30, v67, v67\n"
    "v_dot2c_f32_bf16 v29, s38, v68\n"
    "v_dot2c_f32_bf16 v30, v68, v68\n"
    "v_dot2c_f32_bf16 v29, s38, v69\n"
    "v_dot2c_f32_bf16 v30, v69, v69\n"
    "s_nop 2\n"
    "v_permlane32_swap_b32 v29, v30\n"
    "v_add_f32 v29, v29, v30\n"
    "v_mov_b32 v30, v29\n"
    "s_nop 1\n"
    "v_permlane16_swap_b32 v29, v30\n"
    "v_add_f32 v29, v29, v30\n"
    "buffer_store_dword v29, v91, s[68:71], s24 offen sc1\n"
    "buffer_load_dwordx4 v2, s[68:71], s22 offen offset:2048 sc1 lds\n"
    "s_add_u32 m0, s23, 77824\n"
    "s_add_u32 s22, s22, 12288\n"
    "buffer_load_dwordx4 v2, s[68:71], s22 offen offset:0 sc1 lds\n"
    "buffer_load_dwordx4 v2, s[68:71], s22 offen offset:1024 sc1 lds\n"
    "buffer_load_dwordx4 v2, s[68:71], s22 offen offset:2048 sc1 lds\n"
    "s_add_u32 m0, s23, 90112\n"
    "s_add_u32 s22, s22, 12288\n"
    "buffer_load_dwordx4 v2, s[68:71], s22 offen offset:0 sc1 lds\n"
    "buffer_load_dwordx4 v2, s[68:71], s22 offen offset:1024 sc1 lds\n"
    "v_pk_add_f32 v[112:113], v[112:113], v[120:121]\n"
    "v_pk_add_f32 v[114:115], v[114:115], v[122:123]\n"
    "v_pk_add_f32 v[116:117], v[116:117], v[124:125]\n"
    "v_pk_add_f32 v[118:119], v[118:119], v[126:127]\n"
    "v_pk_add_f32 v[112:113], v[112:113], v[116:117]\n"
    "v_pk_add_f32 v[114:115], v[114:115], v[118:119]\n"
    "s_lshl_b32 s39, s6, 11\n"
    "s_add_u32 s39, s39, 151552\n"
    "v_add_u32 v15, s39, v2\n"
    "ds_write_b128 v15, v[112:115]\n"
    "v_min_u32 v31, 188, v9\n"
    "v_add_u32 v15, s39, v31\n"
    "ds_read_b32 v116, v15 offset:0\n"
    "ds_read_b32 v117, v15 offset:192\n"
    "ds_read_b32 v118, v15 offset:384\n"
    "ds_read_b32 v119, v15 offset:576\n"
    "ds_read_b32 v120, v15 offset:768\n"
    "s_mul_i32 s38, s65, 1152\n"
    "s_add_u32 s4, s38, 4224\n"
    "v_lshrrev_b32 v89, 3, v3\n"
    "v_lshl_add_u32 v89, v89, 12, v7\n"
    "v_add_u32 v89, s59, v89\n"
    "v_xor_b32 v91, 64, v89\n"
    "v_add_u32 v16, 65536, v89\n"
    "v_add_u32 v17, 65536, v91\n"
    "v_add_u32 v18, 131072, v89\n"
    "v_add_u32 v19, 131072, v91\n"
    "s_lshl_b32 s38, s65, 11\n"
    "s_add_u32 s5, s38, 208896\n"
    "s_lshl_b32 s38, s65, 13\n"
    "s_add_u32 s10, s38, 524288\n"
    "v_and_b32 v29, 3, v3\n"
    "v_lshl_add_u32 v29, v4, 2, v29\n"
    "v_lshrrev_b32 v30, 2, v3\n"
    "v_lshlrev_b32 v0, 8, v30\n"
    "v_lshl_add_u32 v0, v29, 4, v0\n"
    "s_mov_b32 s12, -1.029432397e-01\n"
    "s_mov_b32 s13, -2.302208198e+00\n"
    "v_and_b32 v30, 1, v30\n"
    "v_lshlrev_b32 v88, 4, v29\n"
    "v_lshl_add_u32 v88, v30, 3, v88\n"
    "v_lshrrev_b32 v30, 3, v3\n"
    "v_lshl_add_u32 v88, v30, 12, v88\n"
    "s_waitcnt lgkmcnt(1)\n"
    "v_pk_add_f32 v[116:117], v[116:117], v[118:119]\n"
    "s_waitcnt lgkmcnt(0)\n"
    "v_add_f32 v116, v116, v120\n"
    "v_add_f32 v116, v116, v117\n"
    "s_mul_i32 s39, s36, 192\n"
    "s_add_u32 s39, s39, 163072\n"
    "v_add_u32 v31, s39, v31\n"
    "ds_write_b32 v31, v116\n"
    "ds_read_b128 a[0:3], v89 offset:0\n"
    "ds_read_b128 a[4:7], v91 offset:0\n"
    "ds_read_b128 a[8:11], v89 offset:1024\n"
    "ds_read_b128 a[12:15], v91 offset:1024\n"
    "ds_read_b128 a[16:19], v89 offset:2048\n"
    "ds_read_b128 a[20:23], v91 offset:2048\n"
    "ds_read_b128 a[24:27], v89 offset:3072\n"
    "ds_read_b128 a[28:31], v91 offset:3072\n"
    "ds_read_b128 a[32:35], v89 offset:32768\n"
    "ds_read_b128 a[36:39], v91 offset:32768\n"
    "ds_read_b128 a[40:43], v89 offset:33792\n"
    "PAD4_%=:\n"
    "s_cbranch_execz PADRET1_%=\n"
    "ds_read_b128 a[44:47], v91 offset:33792\n"
    "ds_read_b128 a[48:51], v89 offset:34816\n"
    "ds_read_b128 a[52:55], v91 offset:34816\n"
    "ds_read_b128 a[56:59], v89 offset:35840\n"
    "ds_read_b128 a[60:63], v91 offset:35840\n"
    "s_waitcnt lgkmcnt(0)\n"
    "s_waitcnt vmcnt(6)\n"
    "s_barrier\n"
    "s_cmp_eq_u32 s6, 0\n"
    "s_cbranch_scc0 F17_%=\n"
    "s_mov_b64 exec, 1\n"
    "buffer_atomic_swap v5, v9, s[68:71], s25 offen sc1\n"
    "s_mov_b64 exec, -1\n"
    "F17_%=:\n"
    "buffer_load_dwordx4 v2, s[68:71], s22 offen offset:2048 sc1 lds\n"
    "s_add_u32 m0, s23, 102400\n"
    "s_add_u32 s22, s22, 12288\n"
    "buffer_load_dwordx4 v2, s[68:71], s22 offen offset:0 sc1 lds\n"
    "buffer_load_dwordx4 v2, s[68:71], s22 offen offset:1024 sc1 lds\n"
    "buffer_load_dwordx4 v2, s[68:71], s22 offen offset:2048 sc1 lds\n"
    "s_add_u32 m0, s23, 114688\n"
    "s_add_u32 s22, s22, 12288\n"
    "buffer_load_dwordx4 v2, s[68:71], s22 offen offset:0 sc1 lds\n"
    "buffer_load_dwordx4 v2, s[68:71], s22 offen offset:1024 sc1 lds\n"
    "buffer_load_dwordx4 v2, s[68:71], s22 offen offset:2048 sc1 lds\n"
    "s_add_u32 m0, s23, 0\n"
    "s_add_u32 s22, s22, 12288\n"
    "buffer_load_dwordx4 v2, s[68:71], s22 offen offset:0 sc1 lds\n"
    "buffer_load_dwordx4 v2, s[68:71], s22 offen offset:1024 sc1 lds\n"
    "buffer_load_dwordx4 v2, s[68:71], s22 offen offset:2048 sc1 lds\n"
    "s_add_u32 m0, s23, 12288\n"
    "s_add_u32 s22, s22, 12288\n"
    "buffer_load_dwordx4 v2, s[68:71], s22 offen offset:0 sc1 lds\n"
    "buffer_load_dwordx4 v2, s[68:71], s22 offen offset:1024 sc1 lds\n"
    "buffer_load_dwordx4 v2, s[68:71], s22 offen offset:2048 sc1 lds\n"
    "s_add_u32 m0, s23, 24576\n"
    "s_add_u32 s22, s22, 12288\n"
    "buffer_load_dwordx4 v2, s[68:71], s22 offen offset:0 sc1 lds\n"
    "buffer_load_dwordx4 v2, s[68:71], s22 offen offset:1024 sc1 lds\n"
    "buffer_load_dwordx4 v2, s[68:71], s22 offen offset:2048 sc1 lds\n"
    "v_mov_b32 v28, 0\n"
    "s_mov_b64 exec, 0xf\n"
    "buffer_atomic_or v28, v9, s[68:71], s4 offen offset:0 sc0 sc1\n"
    "s_mov_b64 exec, -1\n"
    "v_lshlrev_b32 v29, 2, v3\n"
    "v_add_u32 v29, 163072, v29\n"
    "ds_read_b32 v20, v29 offset:0\n"
    "ds_read_b32 v21, v29 offset:64\n"
    "ds_read_b32 v22, v29 offset:128\n"
    "v_lshlrev_b32 v29, 4, v4\n"
    "v_add_u32 v29, 163264, v29\n"
    "ds_read_b128 v[24:27], v29 offset:0\n"
    "ds_read_b128 v[32:35], v29 offset:64\n"
    "ds_read_b128 v[36:39], v29 offset:128\n"
    "v_add_u32 v7, 65536, v2\n"
    "s_mul_i32 s38, s65, 40960\n"
    "s_add_u32 s11, s38, 14417920\n"
    "s_add_u32 s16, s38, 14423040\n"
    "s_add_u32 s17, s38, 14428160\n"
    "s_add_u32 s24, s38, 14433280\n"
    "s_add_u32 s25, s38, 14438400\n"
    "s_add_u32 s26, s38, 14443520\n"
    "s_add_u32 s27, s38, 14448640\n"
    "s_add_u32 s28, s38, 14453760\n"
    "v_and_b32 v30, 1, v3\n"
    "v_cmp_ne_u32 s[30:31], 0, v30\n"
    "v_and_b32 v30, 2, v3\n"
    "v_cmp_ne_u32 s[42:43], 0, v30\n"
    "v_and_b32 v30, 4, v3\n"
    "v_cmp_ne_u32 s[44:45], 0, v30\n"
    "v_and_b32 v30, 8, v3\n"
    "v_cmp_ne_u32 s[46:47], 0, v30\n"
    "v_lshrrev_b32 v29, 2, v3\n"
    "v_lshl_add_u32 v29, v29, 6, v8\n"
    "s_waitcnt vmcnt(10)\n"
    "s_waitcnt lgkmcnt(0)\n"
    "s_barrier\n"
    "ds_read_b128 a[224:227], v7 offset:0\n"
    "ds_read_b128 a[228:231], v7 offset:1024\n"
    "ds_read_b128 a[232:235], v7 offset:2048\n"
    "ds_read_b128 a[236:239], v7 offset:3072\n"
    "ds_read_b128 a[240:243], v7 offset:4096\n"
    "ds_read_b128 a[244:247], v7 offset:5120\n"
    "s_add_u32 m0, s23, 36864\n"
    "s_add_u32 s22, s22, 12288\n"
    "buffer_load_dwordx4 v2, s[68:71], s22 offen offset:0 sc1 lds\n"
    "buffer_load_dwordx4 v2, s[68:71], s22 offen offset:1024 sc1 lds\n"
    "buffer_load_dwordx4 v2, s[68:71], s22 offen offset:2048 sc1 lds\n"
    "s_add_u32 m0, s23, 49152\n"
    "s_add_u32 s22, s22, 12288\n"
    "buffer_load_dwordx4 v2, s[68:71], s22 offen offset:0 sc1 lds\n"
    "buffer_load_dwordx4 v2, s[68:71], s22 offen offset:1024 sc1 lds\n"
    "buffer_load_dwordx4 v2, s[68:71], s22 offen offset:2048 sc1 lds\n"
    "s_waitcnt vmcnt(6)\n"
    "v_cmp_ne_u32 vcc, s8, v28\n"
    "s_nop 0\n"
    "s_and_b64 vcc, vcc, 0xf\n"
    "s_cbranch_vccnz R18_%=\n"
    "B19_%=:\n"
    "buffer_load_dwordx4 a[64:67], v2, s[68:71], s11 offen offset:0 sc1\n"
    "buffer_load_dwordx4 a[68:71], v2, s[68:71], s16 offen offset:0 sc1\n"
    "buffer_load_dwordx4 a[72:75], v2, s[68:71], s11 offen offset:1024 sc1\n"
    "buffer_load_dwordx4 a[76:79], v2, s[68:71], s16 offen offset:1024 sc1\n"
    "buffer_load_dwordx4 a[80:83], v2, s[68:71], s11 offen offset:2048 sc1\n"
    "buffer_load_dwordx4 a[84:87], v2, s[68:71], s16 offen offset:2048 sc1\n"
    "buffer_load_dwordx4 a[88:91], v2, s[68:71], s11 offen offset:3072 sc1\n"
    "buffer_load_dwordx4 a[92:95], v2, s[68:71], s16 offen offset:3072 sc1\n"
    "s_waitcnt lgkmcnt(3)\n"
    "s_waitcnt vmcnt(7)\n"
    "v_mfma_f32_16x16x32_bf16 v[40:43], a[64:67], a[224:227], 0\n"
    "ds_read_b128 a[248:251], v7 offset:6144\n"
    "ds_read_b128 a[252:255], v7 offset:7168\n"
    "v_mfma_f32_16x16x32_bf16 v[44:47], a[64:67], a[228:231], 0\n"
    "ds_read_b128 v[252:255], v7 offset:8192\n"
    "v_mfma_f32_16x16x32_bf16 v[48:51], a[64:67], a[232:235], 0\n"
    "buffer_load_dwordx4 a[96:99], v10, s[68:71], s11 offen offset:0 sc1\n"
    "s_waitcnt vmcnt(7)\n"
    "v_mfma_f32_16x16x32_bf16 v[52:55], a[68:71], a[224:227], 0\n"
    "buffer_load_dwordx4 a[100:103], v10, s[68:71], s16 offen offset:0 sc1\n"
    "v_mfma_f32_16x16x32_bf16 v[56:59], a[68:71], a[228:231], 0\n"
    "v_mfma_f32_16x16x32_bf16 v[60:63], a[68:71], a[232:235], 0\n"
    "s_waitcnt lgkmcnt(3)\n"
    "s_waitcnt vmcnt(7)\n"
    "v_mfma_f32_16x16x32_bf16 v[40:43], a[72:75], a[236:239], v[40:43]\n"
    "ds_read_b128 a[224:227], v7 offset:9216\n"
    "ds_read_b128 a[228:231], v7 offset:10240\n"
    "v_mfma_f32_16x16x32_bf16 v[44:47], a[72:75], a[240:243], v[44:47]\n"
    "ds_read_b128 a[232:235], v7 offset:11264\n"
    "v_mfma_f32_16x16x32_bf16 v[48:51], a[72:75], a[244:247], v[48:51]\n"
    "buffer_load_dwordx4 a[104:107], v2, s[68:71], s17 offen offset:0 sc1\n"
    "s_waitcnt vmcnt(7)\n"
    "v_mfma_f32_16x16x32_bf16 v[52:55], a[76:79], a[236:239], v[52:55]\n"
    "buffer_load_dwordx4 a[108:111], v2, s[68:71], s24 offen offset:0 sc1\n"
    "v_mfma_f32_16x16x32_bf16 v[56:59], a[76:79], a[240:243], v[56:59]\n"
    "v_mfma_f32_16x16x32_bf16 v[60:63], a[76:79], a[244:247], v[60:63]\n"
    "s_waitcnt lgkmcnt(3)\n"
    "s_waitcnt vmcnt(7)\n"
    "v_mfma_f32_16x16x32_bf16 v[40:43], a[80:83], a[248:251], v[40:43]\n"
    "ds_read_b128 a[236:239], v7 offset:12288\n"
    "ds_read_b128 a[240:243], v7 offset:13312\n"
    "v_mfma_f32_16x16x32_bf16 v[44:47], a[80:83], a[252:255], v[44:47]\n"
    "ds_read_b128 a[244:247], v7 offset:14336\n"
    "v_mfma_f32_16x16x32_bf16 v[48:51], a[80:83], v[252:255], v[48:51]\n"
    "buffer_load_dwordx4 a[112:115], v2, s[68:71], s17 offen offset:1024 sc1\n"
    "s_waitcnt vmcnt(7)\n"
    "v_mfma_f32_16x16x32_bf16 v[52:55], a[84:87], a[248:251], v[52:55]\n"
    "buffer_load_dwordx4 a[116:119], v2, s[68:71], s24 offen offset:1024 sc1\n"
    "v_mfma_f32_16x16x32_bf16 v[56:59], a[84:87], a[252:255], v[56:59]\n"
    "v_mfma_f32_16x16x32_bf16 v[60:63], a[84:87], v[252:255], v[60:63]\n"
    "s_waitcnt lgkmcnt(3)\n"
    "s_waitcnt vmcnt(7)\n"
    "v_mfma_f32_16x16x32_bf16 v[40:43], a[88:91], a[224:227], v[40:43]\n"
    "ds_read_b128 a[248:251], v7 offset:15360\n"
    "ds_read_b128 a[252:255], v7 offset:16384\n"
    "v_mfma_f32_16x16x32_bf16 v[44:47], a[88:91], a[228:231], v[44:47]\n"
    "ds_read_b128 v[252:255], v7 offset:17408\n"
    "v_mfma_f32_16x16x32_bf16 v[48:51], a[88:91], a[232:235], v[48:51]\n"
    "buffer_load_dwordx4 a[120:123], v2, s[68:71], s17 offen offset:2048 sc1\n"
    "s_waitcnt vmcnt(7)\n"
    "v_mfma_f32_16x16x32_bf16 v[52:55], a[92:95], a[224:227], v[52:55]\n"
    "buffer_load_dwordx4 a[124:127], v2, s[68:71], s24 offen offset:2048 sc1\n"
    "v_mfma_f32_16x16x32_bf16 v[56:59], a[92:95], a[228:231], v[56:59]\n"
    "v_mfma_f32_16x16x32_bf16 v[60:63], a[92:95], a[232:235], v[60:63]\n"
    "s_waitcnt lgkmcnt(3)\n"
    "s_waitcnt vmcnt(7)\n"
    "v_mfma_f32_16x16x32_bf16 v[40:43], a[96:99], a[236:239], v[40:43]\n"
    "ds_read_b128 a[224:227], v7 offset:18432\n"
    "ds_read_b128 a[228:231], v7 offset:19456\n"
    "v_mfma_f32_16x16x32_bf16 v[44:47], a[96:99], a[240:243], v[44:47]\n"
    "ds_read_b128 a[232:235], v7 offset:20480\n"
    "v_mfma_f32_16x16x32_bf16 v[48:51], a[96:99], a[244:247], v[48:51]\n"
    "buffer_load_dwordx4 a[128:131], v2, s[68:71], s17 offen offset:3072 sc1\n"
    "s_waitcnt vmcnt(7)\n"
    "v_mfma_f32_16x16x32_bf16 v[52:55], a[100:103], a[236:239], v[52:55]\n"
    "buffer_load_dwordx4 a[132:135], v2, s[68:71], s24 offen offset:3072 sc1\n"
    "v_mfma_f32_16x16x32_bf16 v[56:59], a[100:103], a[240:243], v[56:59]\n"
    "v_mfma_f32_16x16x32_bf16 v[60:63], a[100:103], a[244:247], v[60:63]\n"
    "s_waitcnt lgkmcnt(3)\n"
    "s_waitcnt vmcnt(7)\n"
    "v_mfma_f32_16x16x32_bf16 v[40:43], a[104:107], a[248:251], v[40:43]\n"
    "ds_read_b128 a[236:239], v7 offset:21504\n"
    "ds_read_b128 a[240:243], v7 offset:22528\n"
    "v_mfma_f32_16x16x32_bf16 v[44:47], a[104:107], a[252:255], v[44:47]\n"
    "ds_read_b128 a[244:247], v7 offset:23552\n"
    "v_mfma_f32_16x16x32_bf16 v[48:51], a[104:107], v[252:255], v[48:51]\n"
    "buffer_load_dwordx4 a[136:139], v10, s[68:71], s17 offen offset:0 sc1\n"
    "s_waitcnt vmcnt(7)\n"
    "v_mfma_f32_16x16x32_bf16 v[52:55], a[108:111], a[248:251], v[52:55]\n"
    "buffer_load_dwordx4 a[140:143], v10, s[68:71], s24 offen offset:0 sc1\n"
    "v_mfma_f32_16x16x32_bf16 v[56:59], a[108:111], a[252:255], v[56:59]\n"
    "v_mfma_f32_16x16x32_bf16 v[60:63], a[108:111], v[252:255], v[60:63]\n"
    "s_waitcnt lgkmcnt(3)\n"
    "s_waitcnt vmcnt(7)\n"
    "v_mfma_f32_16x16x32_bf16 v[40:43], a[112:115], a[224:227], v[40:43]\n"
    "ds_read_b128 a[248:251], v7 offset:24576\n"
    "ds_read_b128 a[252:255], v7 offset:25600\n"
    "v_mfma_f32_16x16x32_bf16 v[44:47], a[112:115], a[228:231], v[44:47]\n"
    "ds_read_b128 v[252:255], v7 offset:26624\n"
    "v_mfma_f32_16x16x32_bf16 v[48:51], a[112:115], a[232:235], v[48:51]\n"
    "buffer_load_dwordx4 a[144:147], v2, s[68:71], s25 offen offset:0 sc1\n"
    "s_waitcnt vmcnt(7)\n"
    "v_mfma_f32_16x16x32_bf16 v[52:55], a[116:119], a[224:227], v[52:55]\n"
    "buffer_load_dwordx4 a[148:151], v2, s[68:71], s26 offen offset:0 sc1\n"
    "v_mfma_f32_16x16x32_bf16 v[56:59], a[116:119], a[228:231], v[56:59]\n"
    "v_mfma_f32_16x16x32_bf16 v[60:63], a[116:119], a[232:235], v[60:63]\n"
    "s_waitcnt lgkmcnt(3)\n"
    "s_waitcnt vmcnt(7)\n"
    "v_mfma_f32_16x16x32_bf16 v[40:43], a[120:123], a[236:239], v[40:43]\n"
    "ds_read_b128 a[224:227], v7 offset:27648\n"
    "ds_read_b128 a[228:231], v7 offset:28672\n"
    "v_mfma_f32_16x16x32_bf16 v[44:47], a[120:123], a[240:243], v[44:47]\n"
    "ds_read_b128 a[232:235], v7 offset:29696\n"
    "v_mfma_f32_16x16x32_bf16 v[48:51], a[120:123], a[244:247], v[48:51]\n"
    "buffer_load_dwordx4 a[152:155], v2, s[68:71], s25 offen offset:1024 sc1\n"
    "s_waitcnt vmcnt(7)\n"
    "v_mfma_f32_16x16x32_bf16 v[52:55], a[124:127], a[236:239], v[52:55]\n"
    "buffer_load_dwordx4 a[156:159], v2, s[68:71], s26 offen offset:1024 sc1\n"
    "v_mfma_f32_16x16x32_bf16 v[56:59], a[124:127], a[240:243], v[56:59]\n"
    "v_mfma_f32_16x16x32_bf16 v[60:63], a[124:127], a[244:247], v[60:63]\n"
    "s_waitcnt lgkmcnt(3)\n"
    "s_waitcnt vmcnt(7)\n"
    "v_mfma_f32_16x16x32_bf16 v[40:43], a[128:131], a[248:251], v[40:43]\n"
    "ds_read_b128 a[236:239], v7 offset:30720\n"
    "ds_read_b128 a[240:243], v7 offset:31744\n"
    "v_mfma_f32_16x16x32_bf16 v[44:47], a[128:131], a[252:255], v[44:47]\n"
    "ds_read_b128 a[244:247], v7 offset:32768\n"
    "v_mfma_f32_16x16x32_bf16 v[48:51], a[128:131], v[252:255], v[48:51]\n"
    "buffer_load_dwordx4 v[108:111], v0, s[68:71], s5 offen offset:0 sc1\n"
    "s_waitcnt vmcnt(7)\n"
    "v_mfma_f32_16x16x32_bf16 v[52:55], a[132:135], a[248:251], v[52:55]\n"
    "buffer_load_dwordx4 v[112:115], v0, s[68:71], s5 offen offset:1024 sc1\n"
    "v_mfma_f32_16x16x32_bf16 v[56:59], a[132:135], a[252:255], v[56:59]\n"
    "v_mfma_f32_16x16x32_bf16 v[60:63], a[132:135], v[252:255], v[60:63]\n"
    "s_waitcnt lgkmcnt(3)\n"
    "s_waitcnt vmcnt(7)\n"
    "v_mfma_f32_16x16x32_bf16 v[40:43], a[136:139], a[224:227], v[40:43]\n"
    "ds_read_b128 a[248:251], v7 offset:33792\n"
    "ds_read_b128 a[252:255], v7 offset:34816\n"
    "v_mfma_f32_16x16x32_bf16 v[44:47], a[136:139], a[228:231], v[44:47]\n"
    "ds_read_b128 v[252:255], v7 offset:35840\n"
    "v_mfma_f32_16x16x32_bf16 v[48:51], a[136:139], a[232:235], v[48:51]\n"
    "buffer_load_dwordx4 a[160:163], v2, s[68:71], s25 offen offset:2048 sc1\n"
    "s_waitcnt vmcnt(7)\n"
    "v_mfma_f32_16x16x32_bf16 v[52:55], a[140:143], a[224:227], v[52:55]\n"
    "buffer_load_dwordx4 a[164:167], v2, s[68:71], s26 offen offset:2048 sc1\n"
    "v_mfma_f32_16x16x32_bf16 v[56:59], a[140:143], a[228:231], v[56:59]\n"
    "v_mfma_f32_16x16x32_bf16 v[60:63], a[140:143], a[232:235], v[60:63]\n"
    "s_waitcnt lgkmcnt(3)\n"
    "s_waitcnt vmcnt(7)\n"
    "v_mfma_f32_16x16x32_bf16 v[40:43], a[144:147], a[236:239], v[40:43]\n"
    "ds_read_b128 a[224:227], v7 offset:36864\n"
    "ds_read_b128 a[228:231], v7 offset:37888\n"
    "v_mfma_f32_16x16x32_bf16 v[44:47], a[144:147], a[240:243], v[44:47]\n"
    "ds_read_b128 a[232:235], v7 offset:38912\n"
    "v_mfma_f32_16x16x32_bf16 v[48:51], a[144:147], a[244:247], v[48:51]\n"
    "buffer_load_dwordx4 a[168:171], v2, s[68:71], s25 offen offset:3072 sc1\n"
    "s_waitcnt vmcnt(7)\n"
    "v_mfma_f32_16x16x32_bf16 v[52:55], a[148:151], a[236:239], v[52:55]\n"
    "buffer_load_dwordx4 a[172:175], v2, s[68:71], s26 offen offset:3072 sc1\n"
    "v_mfma_f32_16x16x32_bf16 v[56:59], a[148:151], a[240:243], v[56:59]\n"
    "v_mfma_f32_16x16x32_bf16 v[60:63], a[148:151], a[244:247], v[60:63]\n"
    "s_waitcnt lgkmcnt(3)\n"
    "s_waitcnt vmcnt(7)\n"
    "v_mfma_f32_16x16x32_bf16 v[40:43], a[152:155], a[248:251], v[40:43]\n"
    "ds_read_b128 a[236:239], v7 offset:39936\n"
    "ds_read_b128 a[240:243], v7 offset:40960\n"
    "v_mfma_f32_16x16x32_bf16 v[44:47], a[152:155], a[252:255], v[44:47]\n"
    "ds_read_b128 a[244:247], v7 offset:41984\n"
    "v_mfma_f32_16x16x32_bf16 v[48:51], a[152:155], v[252:255], v[48:51]\n"
    "buffer_load_dwordx4 a[176:179], v10, s[68:71], s25 offen offset:0 sc1\n"
    "s_waitcnt vmcnt(7)\n"
    "v_mfma_f32_16x16x32_bf16 v[52:55], a[156:159], a[248:251], v[52:55]\n"
    "buffer_load_dwordx4 a[180:183], v10, s[68:71], s26 offen offset:0 sc1\n"
    "v_mfma_f32_16x16x32_bf16 v[56:59], a[156:159], a[252:255], v[56:59]\n"
    "v_mfma_f32_16x16x32_bf16 v[60:63], a[156:159], v[252:255], v[60:63]\n"
    "s_waitcnt lgkmcnt(3)\n"
    "s_waitcnt vmcnt(5)\n"
    "v_mfma_f32_16x16x32_bf16 v[40:43], a[160:163], a[224:227], v[40:43]\n"
    "ds_read_b128 a[248:251], v7 offset:43008\n"
    "ds_read_b128 a[252:255], v7 offset:44032\n"
    "buffer_load_dwordx4 a[184:187], v2, s[68:71], s27 offen offset:0 sc1\n"
    "buffer_load_dwordx4 a[188:191], v2, s[68:71], s28 offen offset:0 sc1\n"
    "v_mfma_f32_16x16x32_bf16 v[44:47], a[160:163], a[228:231], v[44:47]\n"
    "ds_read_b128 v[252:255], v7 offset:45056\n"
    "v_mfma_f32_16x16x32_bf16 v[48:51], a[160:163], a[232:235], v[48:51]\n"
    "buffer_load_dwordx4 a[192:195], v2, s[68:71], s27 offen offset:1024 sc1\n"
    "s_waitcnt vmcnt(7)\n"
    "v_mfma_f32_16x16x32_bf16 v[52:55], a[164:167], a[224:227], v[52:55]\n"
    "buffer_load_dwordx4 a[196:199], v2, s[68:71], s28 offen offset:1024 sc1\n"
    "v_mfma_f32_16x16x32_bf16 v[56:59], a[164:167], a[228:231], v[56:59]\n"
    "v_mfma_f32_16x16x32_bf16 v[60:63], a[164:167], a[232:235], v[60:63]\n"
    "s_waitcnt lgkmcnt(3)\n"
    "s_waitcnt vmcnt(7)\n"
    "v_mfma_f32_16x16x32_bf16 v[40:43], a[168:171], a[236:239], v[40:43]\n"
    "ds_read_b128 a[224:227], v7 offset:46080\n"
    "ds_read_b128 a[228:231], v7 offset:47104\n"
    "v_mfma_f32_16x16x32_bf16 v[44:47], a[168:171], a[240:243], v[44:47]\n"
    "ds_read_b128 a[232:235], v7 offset:48128\n"
    "v_mfma_f32_16x16x32_bf16 v[48:51], a[168:171], a[244:247], v[48:51]\n"
    "buffer_load_dwordx4 a[200:203], v2, s[68:71], s27 offen offset:2048 sc1\n"
    "s_waitcnt vmcnt(7)\n"
    "v_mfma_f32_16x16x32_bf16 v[52:55], a[172:175], a[236:239], v[52:55]\n"
    "buffer_load_dwordx4 a[204:207], v2, s[68:71], s28 offen offset:2048 sc1\n"
    "v_mfma_f32_16x16x32_bf16 v[56:59], a[172:175], a[240:243], v[56:59]\n"
    "v_mfma_f32_16x16x32_bf16 v[60:63], a[172:175], a[244:247], v[60:63]\n"
    "s_waitcnt lgkmcnt(3)\n"
    "s_waitcnt vmcnt(7)\n"
    "v_mfma_f32_16x16x32_bf16 v[40:43], a[176:179], a[248:251], v[40:43]\n"
    "ds_read_b128 a[236:239], v7 offset:49152\n"
    "ds_read_b128 a[240:243], v7 offset:50176\n"
    "v_mfma_f32_16x16x32_bf16 v[44:47], a[176:179], a[252:255], v[44:47]\n"
    "ds_read_b128 a[244:247], v7 offset:51200\n"
    "v_mfma_f32_16x16x32_bf16 v[48:51], a[176:179], v[252:255], v[48:51]\n"
    "v_add_f32 v108, v108, v110\n"
    "v_add_f32 v109, v109, v111\n"
    "s_waitcnt vmcnt(6)\n"
    "v_mfma_f32_16x16x32_bf16 v[52:55], a[180:183], a[248:251], v[52:55]\n"
    "v_add_f32 v112, v112, v114\n"
    "v_add_f32 v113, v113, v115\n"
    "buffer_load_dwordx4 a[208:211], v2, s[68:71], s27 offen offset:3072 sc1\n"
    "v_mfma_f32_16x16x32_bf16 v[56:59], a[180:183], a[252:255], v[56:59]\n"
    "v_add_f32_dpp v108, v108, v108 row_ror:4 row_mask:0xf bank_mask:0xf\n"
    "v_add_f32_dpp v112, v112, v112 row_ror:4 row_mask:0xf bank_mask:0xf\n"
    "v_mfma_f32_16x16x32_bf16 v[60:63], a[180:183], v[252:255], v[60:63]\n"
    "v_add_f32_dpp v109, v109, v109 row_ror:4 row_mask:0xf bank_mask:0xf\n"
    "s_waitcnt lgkmcnt(3)\n"
    "s_waitcnt vmcnt(6)\n"
    "v_mfma_f32_16x16x32_bf16 v[40:43], a[184:187], a[224:227], v[40:43]\n"
    "ds_read_b128 a[248:251], v7 offset:52224\n"
    "ds_read_b128 a[252:255], v7 offset:53248\n"
    "buffer_load_dwordx4 a[212:215], v2, s[68:71], s28 offen offset:3072 sc1\n"
    "v_mfma_f32_16x16x32_bf16 v[44:47], a[184:187], a[228:231], v[44:47]\n"
    "ds_read_b128 v[252:255], v7 offset:54272\n"
    "v_mfma_f32_16x16x32_bf16 v[48:51], a[184:187], a[232:235], v[48:51]\n"
    "v_add_f32_dpp v113, v113, v113 row_ror:4 row_mask:0xf bank_mask:0xf\n"
    "v_add_f32_dpp v108, v108, v108 row_ror:8 row_mask:0xf bank_mask:0xf\n"
    "s_waitcnt vmcnt(6)\n"
    "v_mfma_f32_16x16x32_bf16 v[52:55], a[188:191], a[224:227], v[52:55]\n"
    "v_add_f32_dpp v112, v112, v112 row_ror:8 row_mask:0xf bank_mask:0xf\n"
    "v_add_f32_dpp v109, v109, v109 row_ror:8 row_mask:0xf bank_mask:0xf\n"
    "buffer_load_dwordx4 a[216:219], v10, s[68:71], s27 offen offset:0 sc1\n"
    "v_mfma_f32_16x16x32_bf16 v[56:59], a[188:191], a[228:231], v[56:59]\n"
    "v_add_f32_dpp v113, v113, v113 row_ror:8 row_mask:0xf bank_mask:0xf\n"
    "v_mul_f32 v108, 1.562500000e-03, v108\n"
    "v_mfma_f32_16x16x32_bf16 v[60:63], a[188:191], a[232:235], v[60:63]\n"
    "v_mul_f32 v112, 1.562500000e-03, v112\n"
    "v_mul_f32 v109, 1.562500000e-03, v109\n"
    "s_waitcnt lgkmcnt(3)\n"
    "s_waitcnt vmcnt(6)\n"
    "v_mfma_f32_16x16x32_bf16 v[40:43], a[192:195], a[236:239], v[40:43]\n"
    "ds_read_b128 a[224:227], v7 offset:55296\n"
    "ds_read_b128 a[228:231], v7 offset:56320\n"
    "buffer_load_dwordx4 a[220:223], v10, s[68:71], s28 offen offset:0 sc1\n"
    "v_mfma_f32_16x16x32_bf16 v[44:47], a[192:195], a[240:243], v[44:47]\n"
    "ds_read_b128 a[232:235], v7 offset:57344\n"
    "v_mfma_f32_16x16x32_bf16 v[48:51], a[192:195], a[244:247], v[48:51]\n"
    "v_mul_f32 v113, 1.562500000e-03, v113\n"
    "v_fma_f32 v109, -v108, v108, v109\n"
    "s_waitcnt vmcnt(6)\n"
    "v_mfma_f32_16x16x32_bf16 v[52:55], a[196:199], a[236:239], v[52:55]\n"
    "v_fma_f32 v113, -v112, v112, v113\n"
    "v_max_f32 v109, 0, v109\n"
    "s_xor_b32 s64, s58, 1024\n"
    "s_add_u32 m0, s59, 131072\n"
    "v_mfma_f32_16x16x32_bf16 v[56:59], a[196:199], a[240:243], v[56:59]\n"
    "v_max_f32 v113, 0, v113\n"
    "v_add_f32 v109, 1.0e-5, v109\n"
    "buffer_load_dwordx4 v11, s[76:79], s64 offen offset:0 lds\n"
    "v_mfma_f32_16x16x32_bf16 v[60:63], a[196:199], a[244:247], v[60:63]\n"
    "v_add_f32 v113, 1.0e-5, v113\n"
    "v_rsq_f32 v109, v109\n"
    "s_waitcnt lgkmcnt(3)\n"
    "s_waitcnt vmcnt(6)\n"
    "v_mfma_f32_16x16x32_bf16 v[40:43], a[200:203], a[248:251], v[40:43]\n"
    "ds_read_b128 a[236:239], v7 offset:58368\n"
    "ds_read_b128 a[240:243], v7 offset:59392\n"
    "buffer_load_dwordx4 v12, s[76:79], s64 offen offset:1024 lds\n"
    "v_mfma_f32_16x16x32_bf16 v[44:47], a[200:203], a[252:255], v[44:47]\n"
    "ds_read_b128 a[244:247], v7 offset:60416\n"
    "v_mfma_f32_16x16x32_bf16 v[48:51], a[200:203], v[252:255], v[48:51]\n"
    "v_rsq_f32 v113, v113\n"
    "s_waitcnt vmcnt(6)\n"
    "v_mfma_f32_16x16x32_bf16 v[52:55], a[204:207], a[248:251], v[52:55]\n"
    "v_mul_f32 v108, v108, v109\n"
    "v_mul_f32 v112, v112, v113\n"
    "buffer_load_dwordx4 v13, s[76:79], s64 offen offset:2048 lds\n"
    "v_mfma_f32_16x16x32_bf16 v[56:59], a[204:207], a[252:255], v[56:59]\n"
    "v_mov_b32_dpp v92, v109 quad_perm:[0,0,0,0] row_mask:0xf bank_mask:0xf\n"
    "v_mov_b32_dpp v93, v109 quad_perm:[1,1,1,1] row_mask:0xf bank_mask:0xf\n"
    "v_mfma_f32_16x16x32_bf16 v[60:63], a[204:207], v[252:255], v[60:63]\n"
    "v_mov_b32_dpp v94, v109 quad_perm:[2,2,2,2] row_mask:0xf bank_mask:0xf\n"
    "v_mov_b32_dpp v95, v109 quad_perm:[3,3,3,3] row_mask:0xf bank_mask:0xf\n"
    "s_waitcnt lgkmcnt(3)\n"
    "s_waitcnt vmcnt(6)\n"
    "v_mfma_f32_16x16x32_bf16 v[40:43], a[208:211], a[224:227], v[40:43]\n"
    "buffer_load_dwordx4 v14, s[76:79], s64 offen offset:3072 lds\n"
    "v_mfma_f32_16x16x32_bf16 v[44:47], a[208:211], a[228:231], v[44:47]\n"
    "s_xor_b32 s64, s58, 33792\n"
    "s_add_u32 m0, s59, 135168\n"
    "v_mfma_f32_16x16x32_bf16 v[48:51], a[208:211], a[232:235], v[48:51]\n"
    "v_mov_b32_dpp v96, v113 quad_perm:[0,0,0,0] row_mask:0xf bank_mask:0xf\n"
    "v_mov_b32_dpp v97, v113 quad_perm:[1,1,1,1] row_mask:0xf bank_mask:0xf\n"
    "s_waitcnt vmcnt(6)\n"
    "v_mfma_f32_16x16x32_bf16 v[52:55], a[212:215], a[224:227], v[52:55]\n"
    "v_mov_b32_dpp v98, v113 quad_perm:[2,2,2,2] row_mask:0xf bank_mask:0xf\n"
    "v_mov_b32_dpp v99, v113 quad_perm:[3,3,3,3] row_mask:0xf bank_mask:0xf\n"
    "buffer_load_dwordx4 v11, s[76:79], s64 offen offset:0 lds\n"
    "v_mfma_f32_16x16x32_bf16 v[56:59], a[212:215], a[228:231], v[56:59]\n"
    "v_mov_b32_dpp v100, v108 quad_perm:[0,0,0,0] row_mask:0xf bank_mask:0xf\n"
    "v_mov_b32_dpp v101, v108 quad_perm:[1,1,1,1] row_mask:0xf bank_mask:0xf\n"
    "v_mfma_f32_16x16x32_bf16 v[60:63], a[212:215], a[232:235], v[60:63]\n"
    "v_mov_b32_dpp v102, v108 quad_perm:[2,2,2,2] row_mask:0xf bank_mask:0xf\n"
    "v_mov_b32_dpp v103, v108 quad_perm:[3,3,3,3] row_mask:0xf bank_mask:0xf\n"
    "s_waitcnt lgkmcnt(0)\n"
    "s_waitcnt vmcnt(6)\n"
    "v_mfma_f32_16x16x32_bf16 v[40:43], a[216:219], a[236:239], v[40:43]\n"
    "buffer_load_dwordx4 v12, s[76:79], s64 offen offset:1024 lds\n"
    "v_mfma_f32_16x16x32_bf16 v[44:47], a[216:219], a[240:243], v[44:47]\n"
    "buffer_load_dwordx4 v13, s[76:79], s64 offen offset:2048 lds\n"
    "v_mfma_f32_16x16x32_bf16 v[48:51], a[216:219], a[244:247], v[48:51]\n"
    "v_mov_b32_dpp v104, v112 quad_perm:[0,0,0,0] row_mask:0xf bank_mask:0xf\n"
    "v_mov_b32_dpp v105, v112 quad_perm:[1,1,1,1] row_mask:0xf bank_mask:0xf\n"
    "s_waitcnt vmcnt(7)\n"
    "v_mfma_f32_16x16x32_bf16 v[52:55], a[220:223], a[236:239], v[52:55]\n"
    "v_mov_b32_dpp v106, v112 quad_perm:[2,2,2,2] row_mask:0xf bank_mask:0xf\n"
    "v_mov_b32_dpp v107, v112 quad_perm:[3,3,3,3] row_mask:0xf bank_mask:0xf\n"
    "v_mfma_f32_16x16x32_bf16 v[56:59], a[220:223], a[240:243], v[56:59]\n"
    "ds_bpermute_b32 v116, v29, v109\n"
    "ds_bpermute_b32 v117, v29, v108\n"
    "v_mfma_f32_16x16x32_bf16 v[60:63], a[220:223], a[244:247], v[60:63]\n"
    "ds_bpermute_b32 v118, v29, v113\n"
    "ds_bpermute_b32 v119, v29, v112\n"
    "s_barrier\n"
    "v_pk_mul_f32 v[40:41], v[40:41], v[92:93]\n"
    "buffer_load_dwordx4 v14, s[76:79], s64 offen offset:3072 lds\n"
    "v_pk_mul_f32 v[42:43], v[42:43], v[94:95]\n"
    "s_xor_b32 s64, s58, 1536\n"
    "s_add_u32 m0, s59, 65536\n"
    "v_pk_mul_f32 v[44:45], v[44:45], v[92:93]\n"
    "v_pk_mul_f32 v[46:47], v[46:47], v[94:95]\n"
    "v_pk_mul_f32 v[48:49], v[48:49], v[92:93]\n"
    "v_pk_mul_f32 v[50:51], v[50:51], v[94:95]\n"
    "v_pk_mul_f32 v[52:53], v[52:53], v[96:97]\n"
    "v_pk_mul_f32 v[54:55], v[54:55], v[98:99]\n"
    "v_pk_mul_f32 v[56:57], v[56:57], v[96:97]\n"
    "v_pk_mul_f32 v[58:59], v[58:59], v[98:99]\n"
    "v_pk_mul_f32 v[60:61], v[60:61], v[96:97]\n"
    "buffer_load_dwordx4 v11, s[76:79], s64 offen offset:0 lds\n"
    "v_pk_mul_f32 v[62:63], v[62:63], v[98:99]\n"
    "v_pk_fma_f32 v[40:41], v[100:101], v[20:21], v[40:41] op_sel:[0,0,0] op_sel_hi:[1,0,1] neg_lo:[1,0,0] neg_hi:[1,0,0]\n"
    "v_pk_fma_f32 v[42:43], v[102:103], v[20:21], v[42:43] op_sel:[0,0,0] op_sel_hi:[1,0,1] neg_lo:[1,0,0] neg_hi:[1,0,0]\n"
    "v_pk_fma_f32 v[44:45], v[100:101], v[20:21], v[44:45] op_sel:[0,1,0] op_sel_hi:[1,1,1] neg_lo:[1,0,0] neg_hi:[1,0,0]\n"
    "v_pk_fma_f32 v[46:47], v[102:103], v[20:21], v[46:47] op_sel:[0,1,0] op_sel_hi:[1,1,1] neg_lo:[1,0,0] neg_hi:[1,0,0]\n"
    "v_pk_fma_f32 v[48:49], v[100:101], v[22:23], v[48:49] op_sel:[0,0,0] op_sel_hi:[1,0,1] neg_lo:[1,0,0] neg_hi:[1,0,0]\n"
    "v_pk_fma_f32 v[50:51], v[102:103], v[22:23], v[50:51] op_sel:[0,0,0] op_sel_hi:[1,0,1] neg_lo:[1,0,0] neg_hi:[1,0,0]\n"
    "v_pk_fma_f32 v[52:53], v[104:105], v[20:21], v[52:53] op_sel:[0,0,0] op_sel_hi:[1,0,1] neg_lo:[1,0,0] neg_hi:[1,0,0]\n"
    "v_pk_fma_f32 v[54:55], v[106:107], v[20:21], v[54:55] op_sel:[0,0,0] op_sel_hi:[1,0,1] neg_lo:[1,0,0] neg_hi:[1,0,0]\n"
    "v_pk_fma_f32 v[56:57], v[104:105], v[20:21], v[56:57] op_sel:[0,1,0] op_sel_hi:[1,1,1] neg_lo:[1,0,0] neg_hi:[1,0,0]\n"
    "v_pk_fma_f32 v[58:59], v[106:107], v[20:21], v[58:59] op_sel:[0,1,0] op_sel_hi:[1,1,1] neg_lo:[1,0,0] neg_hi:[1,0,0]\n"
    "v_pk_fma_f32 v[60:61], v[104:105], v[22:23], v[60:61] op_sel:[0,0,0] op_sel_hi:[1,0,1] neg_lo:[1,0,0] neg_hi:[1,0,0]\n"
    "buffer_load_dwordx4 v12, s[76:79], s64 offen offset:1024 lds\n"
    "v_pk_fma_f32 v[62:63], v[106:107], v[22:23], v[62:63] op_sel:[0,0,0] op_sel_hi:[1,0,1] neg_lo:[1,0,0] neg_hi:[1,0,0]\n"
    "v_pk_mul_f32 v[64:65], v[40:41], v[40:41]\n"
    "v_pk_mul_f32 v[66:67], v[42:43], v[42:43]\n"
    "v_pk_mul_f32 v[68:69], v[44:45], v[44:45]\n"
    "v_pk_mul_f32 v[70:71], v[46:47], v[46:47]\n"
    "v_pk_mul_f32 v[72:73], v[48:49], v[48:49]\n"
    "v_pk_mul_f32 v[74:75], v[50:51], v[50:51]\n"
    "v_pk_mul_f32 v[76:77], v[52:53], v[52:53]\n"
    "v_pk_mul_f32 v[78:79], v[54:55], v[54:55]\n"
    "v_pk_mul_f32 v[80:81], v[56:57], v[56:57]\n"
    "v_pk_mul_f32 v[82:83], v[58:59], v[58:59]\n"
    "v_pk_mul_f32 v[84:85], v[60:61], v[60:61]\n"
    "buffer_load_dwordx4 v13, s[76:79], s64 offen offset:2048 lds\n"
    "v_pk_mul_f32 v[86:87], v[62:63], v[62:63]\n"
    "v_pk_fma_f32 v[64:65], v[64:65], s[12:13], s[12:13] op_sel:[0,0,1] op_sel_hi:[1,0,1]\n"
    "v_pk_fma_f32 v[66:67], v[66:67], s[12:13], s[12:13] op_sel:[0,0,1] op_sel_hi:[1,0,1]\n"
    "v_pk_fma_f32 v[68:69], v[68:69], s[12:13], s[12:13] op_sel:[0,0,1] op_sel_hi:[1,0,1]\n"
    "v_pk_fma_f32 v[70:71], v[70:71], s[12:13], s[12:13] op_sel:[0,0,1] op_sel_hi:[1,0,1]\n"
    "v_pk_fma_f32 v[72:73], v[72:73], s[12:13], s[12:13] op_sel:[0,0,1] op_sel_hi:[1,0,1]\n"
    "v_pk_fma_f32 v[74:75], v[74:75], s[12:13], s[12:13] op_sel:[0,0,1] op_sel_hi:[1,0,1]\n"
    "v_pk_fma_f32 v[76:77], v[76:77], s[12:13], s[12:13] op_sel:[0,0,1] op_sel_hi:[1,0,1]\n"
    "v_pk_fma_f32 v[78:79], v[78:79], s[12:13], s[12:13] op_sel:[0,0,1] op_sel_hi:[1,0,1]\n"
    "v_pk_fma_f32 v[80:81], v[80:81], s[12:13], s[12:13] op_sel:[0,0,1] op_sel_hi:[1,0,1]\n"
    "v_pk_fma_f32 v[82:83], v[82:83], s[12:13], s[12:13] op_sel:[0,0,1] op_sel_hi:[1,0,1]\n"
    "v_pk_fma_f32 v[84:85], v[84:85], s[12:13], s[12:13] op_sel:[0,0,1] op_sel_hi:[1,0,1]\n"
    "v_pk_fma_f32 v[86:87], v[86:87], s[12:13], s[12:13] op_sel:[0,0,1] op_sel_hi:[1,0,1]\n"
    "v_pk_mul_f32 v[64:65], v[64:65], v[40:41]\n"
    "v_pk_mul_f32 v[66:67], v[66:67], v[42:43]\n"
    "v_exp_f32 v64, v64\n"
    "v_exp_f32 v65, v65\n"
    "buffer_load_dwordx4 v14, s[76:79], s64 offen offset:3072 lds\n"
    "v_pk_mul_f32 v[68:69], v[68:69], v[44:45]\n"
    "s_xor_b32 s64, s58, 34304\n"
    "s_add_u32 m0, s59, 69632\n"
    "v_exp_f32 v66, v66\n"
    "v_pk_add_f32 v[64:65], v[64:65], 1.0 op_sel_hi:[1,0]\n"
    "v_exp_f32 v67, v67\n"
    "v_pk_mul_f32 v[70:71], v[70:71], v[46:47]\n"
    "v_exp_f32 v68, v68\n"
    "v_pk_add_f32 v[66:67], v[66:67], 1.0 op_sel_hi:[1,0]\n"
    "v_exp_f32 v69, v69\n"
    "v_rcp_f32 v64, v64\n"
    "v_rcp_f32 v65, v65\n"
    "v_pk_mul_f32 v[72:73], v[72:73], v[48:49]\n"
    "v_exp_f32 v70, v70\n"
    "v_pk_add_f32 v[68:69], v[68:69], 1.0 op_sel_hi:[1,0]\n"
    "v_exp_f32 v71, v71\n"
    "buffer_load_dwordx4 v11, s[76:79], s64 offen offset:0 lds\n"
    "v_pk_mul_f32 v[40:41], v[40:41], v[64:65]\n"
    "v_rcp_f32 v66, v66\n"
    "v_rcp_f32 v67, v67\n"
    "v_pk_mul_f32 v[74:75], v[74:75], v[50:51]\n"
    "v_exp_f32 v72, v72\n"
    "v_pk_add_f32 v[70:71], v[70:71], 1.0 op_sel_hi:[1,0]\n"
    "v_exp_f32 v73, v73\n"
    "v_pk_mul_f32 v[42:43], v[42:43], v[66:67]\n"
    "v_rcp_f32 v68, v68\n"
    "v_rcp_f32 v69, v69\n"
    "v_pk_mul_f32 v[76:77], v[76:77], v[52:53]\n"
    "buffer_load_dwordx4 v12, s[76:79], s64 offen offset:1024 lds\n"
    "v_exp_f32 v74, v74\n"
    "v_pk_add_f32 v[72:73], v[72:73], 1.0 op_sel_hi:[1,0]\n"
    "v_exp_f32 v75, v75\n"
    "v_pk_mul_f32 v[44:45], v[44:45], v[68:69]\n"
    "v_rcp_f32 v70, v70\n"
    "v_rcp_f32 v71, v71\n"
    "v_pk_mul_f32 v[78:79], v[78:79], v[54:55]\n"
    "v_exp_f32 v76, v76\n"
    "v_pk_add_f32 v[74:75], v[74:75], 1.0 op_sel_hi:[1,0]\n"
    "v_exp_f32 v77, v77\n"
    "v_pk_mul_f32 v[46:47], v[46:47], v[70:71]\n"
    "v_rcp_f32 v72, v72\n"
    "buffer_load_dwordx4 v13, s[76:79], s64 offen offset:2048 lds\n"
    "v_rcp_f32 v73, v73\n"
    "v_pk_mul_f32 v[80:81], v[80:81], v[56:57]\n"
    "v_exp_f32 v78, v78\n"
    "v_pk_add_f32 v[76:77], v[76:77], 1.0 op_sel_hi:[1,0]\n"
    "v_exp_f32 v79, v79\n"
    "v_pk_mul_f32 v[48:49], v[48:49], v[72:73]\n"
    "v_rcp_f32 v74, v74\n"
    "v_rcp_f32 v75, v75\n"
    "v_pk_mul_f32 v[82:83], v[82:83], v[58:59]\n"
    "v_exp_f32 v80, v80\n"
    "v_pk_add_f32 v[78:79], v[78:79], 1.0 op_sel_hi:[1,0]\n"
    "buffer_load_dwordx4 v14, s[76:79], s64 offen offset:3072 lds\n"
    "v_exp_f32 v81, v81\n"
    "s_xor_b32 s64, s58, 65536\n"
    "s_add_u32 m0, s59, 98304\n"
    "v_pk_mul_f32 v[50:51], v[50:51], v[74:75]\n"
    "v_rcp_f32 v76, v76\n"
    "v_rcp_f32 v77, v77\n"
    "v_pk_mul_f32 v[84:85], v[84:85], v[60:61]\n"
    "v_exp_f32 v82, v82\n"
    "v_pk_add_f32 v[80:81], v[80:81], 1.0 op_sel_hi:[1,0]\n"
    "v_exp_f32 v83, v83\n"
    "v_pk_mul_f32 v[52:53], v[52:53], v[76:77]\n"
    "v_rcp_f32 v78, v78\n"
    "buffer_load_dwordx4 v11, s[76:79], s64 offen offset:0 lds\n"
    "v_rcp_f32 v79, v79\n"
    "v_pk_mul_f32 v[86:87], v[86:87], v[62:63]\n"
    "v_exp_f32 v84, v84\n"
    "v_pk_add_f32 v[82:83], v[82:83], 1.0 op_sel_hi:[1,0]\n"
    "v_exp_f32 v85, v85\n"
    "v_pk_mul_f32 v[54:55], v[54:55], v[78:79]\n"
    "v_rcp_f32 v80, v80\n"
    "v_rcp_f32 v81, v81\n"
    "v_exp_f32 v86, v86\n"
    "v_pk_add_f32 v[84:85], v[84:85], 1.0 op_sel_hi:[1,0]\n"
    "v_exp_f32 v87, v87\n"
    "buffer_load_dwordx4 v12, s[76:79], s64 offen offset:1024 lds\n"
    "v_pk_mul_f32 v[56:57], v[56:57], v[80:81]\n"
    "v_rcp_f32 v82, v82\n"
    "v_rcp_f32 v83, v83\n"
    "v_pk_add_f32 v[86:87], v[86:87], 1.0 op_sel_hi:[1,0]\n"
    "v_pk_mul_f32 v[58:59], v[58:59], v[82:83]\n"
    "v_rcp_f32 v84, v84\n"
    "v_rcp_f32 v85, v85\n"
    "s_nop 0\n"
    "v_pk_mul_f32 v[60:61], v[60:61], v[84:85]\n"
    "v_rcp_f32 v86, v86\n"
    "v_rcp_f32 v87, v87\n"
    "s_nop 0\n"
    "v_pk_mul_f32 v[62:63], v[62:63], v[86:87]\n"
    "buffer_load_dwordx4 v13, s[76:79], s64 offen offset:2048 lds\n"
    "v_pk_add_f32 v[64:65], v[40:41], v[44:45]\n"
    "v_pk_mul_f32 v[68:69], v[40:41], v[40:41]\n"
    "PAD5_%=:\n"
    "s_cbranch_execz PADRET1_%=\n"
    "v_pk_add_f32 v[64:65], v[64:65], v[48:49]\n"
    "v_pk_fma_f32 v[68:69], v[44:45], v[44:45], v[68:69]\n"
    "v_pk_fma_f32 v[68:69], v[48:49], v[48:49], v[68:69]\n"
    "v_pk_add_f32 v[66:67], v[42:43], v[46:47]\n"
    "v_pk_mul_f32 v[70:71], v[42:43], v[42:43]\n"
    "v_pk_add_f32 v[66:67], v[66:67], v[50:51]\n"
    "v_pk_fma_f32 v[70:71], v[46:47], v[46:47], v[70:71]\n"
    "v_pk_fma_f32 v[70:71], v[50:51], v[50:51], v[70:71]\n"
    "v_pk_add_f32 v[72:73], v[52:53], v[56:57]\n"
    "v_pk_mul_f32 v[76:77], v[52:53], v[52:53]\n"
    "v_pk_add_f32 v[72:73], v[72:73], v[60:61]\n"
    "v_pk_fma_f32 v[76:77], v[56:57], v[56:57], v[76:77]\n"
    "v_pk_fma_f32 v[76:77], v[60:61], v[60:61], v[76:77]\n"
    "v_pk_add_f32 v[74:75], v[54:55], v[58:59]\n"
    "v_pk_mul_f32 v[78:79], v[54:55], v[54:55]\n"
    "buffer_load_dwordx4 v14, s[76:79], s64 offen offset:3072 lds\n"
    "v_pk_add_f32 v[74:75], v[74:75], v[62:63]\n"
    "s_xor_b32 s64, s58, 98304\n"
    "s_add_u32 m0, s59, 102400\n"
    "v_pk_fma_f32 v[78:79], v[58:59], v[58:59], v[78:79]\n"
    "v_pk_fma_f32 v[78:79], v[62:63], v[62:63], v[78:79]\n"
    "v_cndmask_b32 v80, v72, v64, s[46:47]\n"
    "v_cndmask_b32 v64, v64, v72, s[46:47]\n"
    "v_cndmask_b32 v81, v73, v65, s[46:47]\n"
    "v_cndmask_b32 v65, v65, v73, s[46:47]\n"
    "v_cndmask_b32 v82, v74, v66, s[46:47]\n"
    "v_cndmask_b32 v66, v66, v74, s[46:47]\n"
    "v_cndmask_b32 v83, v75, v67, s[46:47]\n"
    "v_cndmask_b32 v67, v67, v75, s[46:47]\n"
    "v_cndmask_b32 v84, v76, v68, s[46:47]\n"
    "v_cndmask_b32 v68, v68, v76, s[46:47]\n"
    "v_cndmask_b32 v85, v77, v69, s[46:47]\n"
    "v_cndmask_b32 v69, v69, v77, s[46:47]\n"
    "v_cndmask_b32 v86, v78, v70, s[46:47]\n"
    "buffer_load_dwordx4 v11, s[76:79], s64 offen offset:0 lds\n"
    "v_cndmask_b32 v70, v70, v78, s[46:47]\n"
    "v_cndmask_b32 v87, v79, v71, s[46:47]\n"
    "v_cndmask_b32 v71, v71, v79, s[46:47]\n"
    "v_add_f32_dpp v64, v80, v64 row_ror:8 row_mask:0xf bank_mask:0xf\n"
    "v_add_f32_dpp v65, v81, v65 row_ror:8 row_mask:0xf bank_mask:0xf\n"
    "v_add_f32_dpp v66, v82, v66 row_ror:8 row_mask:0xf bank_mask:0xf\n"
    "v_add_f32_dpp v67, v83, v67 row_ror:8 row_mask:0xf bank_mask:0xf\n"
    "v_add_f32_dpp v68, v84, v68 row_ror:8 row_mask:0xf bank_mask:0xf\n"
    "v_add_f32_dpp v69, v85, v69 row_ror:8 row_mask:0xf bank_mask:0xf\n"
    "v_add_f32_dpp v70, v86, v70 row_ror:8 row_mask:0xf bank_mask:0xf\n"
    "v_add_f32_dpp v71, v87, v71 row_ror:8 row_mask:0xf bank_mask:0xf\n"
    "v_cndmask_b32 v80, v68, v64, s[44:45]\n"
    "v_cndmask_b32 v64, v64, v68, s[44:45]\n"
    "v_cndmask_b32 v81, v69, v65, s[44:45]\n"
    "v_cndmask_b32 v65, v65, v69, s[44:45]\n"
    "v_cndmask_b32 v82, v70, v66, s[44:45]\n"
    "v_cndmask_b32 v66, v66, v70, s[44:45]\n"
    "v_cndmask_b32 v83, v71, v67, s[44:45]\n"
    "buffer_load_dwordx4 v12, s[76:79], s64 offen offset:1024 lds\n"
    "v_cndmask_b32 v67, v67, v71, s[44:45]\n"
    "v_add_f32_dpp v64, v80, v64 row_half_mirror row_mask:0xf bank_mask:0xf\n"
    "v_add_f32_dpp v65, v81, v65 row_half_mirror row_mask:0xf bank_mask:0xf\n"
    "v_add_f32_dpp v66, v82, v66 row_half_mirror row_mask:0xf bank_mask:0xf\n"
    "v_add_f32_dpp v67, v83, v67 row_half_mirror row_mask:0xf bank_mask:0xf\n"
    "v_cndmask_b32 v80, v66, v64, s[42:43]\n"
    "v_cndmask_b32 v64, v64, v66, s[42:43]\n"
    "v_cndmask_b32 v81, v67, v65, s[42:43]\n"
    "v_cndmask_b32 v65, v65, v67, s[42:43]\n"
    "v_add_f32_dpp v64, v80, v64 quad_perm:[2,3,0,1] row_mask:0xf bank_mask:0xf\n"
    "s_nop 0\n"
    "v_add_f32_dpp v65, v81, v65 quad_perm:[2,3,0,1] row_mask:0xf bank_mask:0xf\n"
    "v_cndmask_b32 v80, v65, v64, s[30:31]\n"
    "v_cndmask_b32 v64, v64, v65, s[30:31]\n"
    "s_lshl_b32 s38, s21, 8\n"
    "s_add_u32 s38, s38, s10\n"
    "v_add_f32_dpp v64, v80, v64 quad_perm:[1,0,3,2] row_mask:0xf bank_mask:0xf\n"
    "v_mov_b32 v65, s8\n"
    "buffer_store_dwordx2 v[64:65], v88, s[68:71], s38 offen sc1\n"
    "s_barrier\n"
    "ds_read_b128 a[224:227], v2 offset:0\n"
    "ds_read_b128 a[228:231], v2 offset:1024\n"
    "ds_read_b128 a[232:235], v2 offset:2048\n"
    "ds_read_b128 a[236:239], v2 offset:3072\n"
    "ds_read_b128 a[240:243], v2 offset:4096\n"
    "ds_read_b128 a[244:247], v2 offset:5120\n"
    "v_and_b32 v29, 1, v4\n"
    "v_lshlrev_b32 v30, 4, v3\n"
    "v_lshl_add_u32 v30, v29, 9, v30\n"
    "v_lshrrev_b32 v29, 1, v4\n"
    "v_lshl_add_u32 v30, v29, 8, v30\n"
    "s_mul_i32 s38, s21, 196608\n"
    "s_lshl_b32 s39, s65, 10\n"
    "s_add_u32 s38, s38, s39\n"
    "s_add_u32 s11, s38, 17039360\n"
    "s_add_u32 s5, s10, 4096\n"
    "s_waitcnt lgkmcnt(3)\n"
    "v_mfma_f32_16x16x32_bf16 v[20:23], a[224:227], a[64:67], 0\n"
    "ds_read_b128 a[248:251], v2 offset:6144\n"
    "ds_read_b128 a[252:255], v2 offset:7168\n"
    "buffer_load_dwordx4 v13, s[76:79], s64 offen offset:2048 lds\n"
    "v_mfma_f32_16x16x32_bf16 v[108:111], a[228:231], a[64:67], 0\n"
    "ds_read_b128 v[252:255], v2 offset:8192\n"
    "v_mfma_f32_16x16x32_bf16 v[112:115], a[232:235], a[64:67], 0\n"
    "s_lshr_b32 s38, s18, 2\n"
    "s_lshl3_add_u32 s38, s34, s38\n"
    "v_mfma_f32_16x16x32_bf16 v[120:123], a[224:227], a[68:71], 0\n"
    "s_mul_i32 s38, s38, 1152\n"
    "s_and_b32 s39, s18, 3\n"
    "buffer_load_dwordx4 v14, s[76:79], s64 offen offset:3072 lds\n"
    "v_mfma_f32_16x16x32_bf16 v[124:127], a[228:231], a[68:71], 0\n"
    "s_lshl2_add_u32 s38, s39, s38\n"
    "s_add_u32 s16, s38, 4352\n"
    "v_mfma_f32_16x16x32_bf16 v[128:131], a[232:235], a[68:71], 0\n"
    "s_waitcnt vmcnt(17)\n"
    "ds_read_b128 v[248:251], v18 offset:0\n"
    "ds_read_b128 v[244:247], v19 offset:0\n"
    "ds_read_b128 v[240:243], v18 offset:1024\n"
    "ds_read_b128 v[236:239], v19 offset:1024\n"
    "ds_read_b128 v[232:235], v18 offset:2048\n"
    "ds_read_b128 v[228:231], v19 offset:2048\n"
    "ds_read_b128 v[224:227], v18 offset:3072\n"
    "ds_read_b128 v[220:223], v19 offset:3072\n"
    "s_waitcnt lgkmcnt(11)\n"
    "v_mfma_f32_16x16x32_bf16 v[20:23], a[236:239], a[72:75], v[20:23]\n"
    "ds_read_b128 a[224:227], v2 offset:9216\n"
    "ds_read_b128 a[228:231], v2 offset:10240\n"
    "s_waitcnt lgkmcnt(2)\n"
    "v_mfma_f32_16x16x32_bf16 v[108:111], a[240:243], a[72:75], v[108:111]\n"
    "ds_read_b128 a[232:235], v2 offset:11264\n"
    "s_xor_b32 s64, s58, 66048\n"
    "s_add_u32 m0, s59, 131072\n"
    "v_mfma_f32_16x16x32_bf16 v[112:115], a[244:247], a[72:75], v[112:115]\n"
    "buffer_load_dwordx4 v11, s[76:79], s64 offen offset:0 lds\n"
    "v_mfma_f32_16x16x32_bf16 v[120:123], a[236:239], a[76:79], v[120:123]\n"
    "v_mfma_f32_16x16x32_bf16 v[124:127], a[240:243], a[76:79], v[124:127]\n"
    "v_mfma_f32_16x16x32_bf16 v[128:131], a[244:247], a[76:79], v[128:131]\n"
    "v_mfma_f32_16x16x32_bf16 v[20:23], a[248:251], a[80:83], v[20:23]\n"
    "ds_read_b128 a[236:239], v2 offset:12288\n"
    "ds_read_b128 a[240:243], v2 offset:13312\n"
    "buffer_load_dwordx4 v12, s[76:79], s64 offen offset:1024 lds\n"
    "v_mfma_f32_16x16x32_bf16 v[108:111], a[252:255], a[80:83], v[108:111]\n"
    "ds_read_b128 a[244:247], v2 offset:14336\n"
    "v_mfma_f32_16x16x32_bf16 v[112:115], v[252:255], a[80:83], v[112:115]\n"
    "v_mfma_f32_16x16x32_bf16 v[120:123], a[248:251], a[84:87], v[120:123]\n"
    "v_mfma_f32_16x16x32_bf16 v[124:127], a[252:255], a[84:87], v[124:127]\n"
    "buffer_load_dwordx4 v13, s[76:79], s64 offen offset:2048 lds\n"
    "v_mfma_f32_16x16x32_bf16 v[128:131], v[252:255], a[84:87], v[128:131]\n"
    "s_waitcnt lgkmcnt(3)\n"
    "v_mfma_f32_16x16x32_bf16 v[20:23], a[224:227], a[88:91], v[20:23]\n"
    "ds_read_b128 a[248:251], v2 offset:15360\n"
    "ds_read_b128 a[252:255], v2 offset:16384\n"
    "v_mfma_f32_16x16x32_bf16 v[108:111], a[228:231], a[88:91], v[108:111]\n"
    "ds_read_b128 v[252:255], v2 offset:17408\n"
    "v_mfma_f32_16x16x32_bf16 v[112:115], a[232:235], a[88:91], v[112:115]\n"
    "buffer_load_dwordx4 v14, s[76:79], s64 offen offset:3072 lds\n"
    "s_xor_b32 s64, s58, 98816\n"
    "s_add_u32 m0, s59, 135168\n"
    "v_mfma_f32_16x16x32_bf16 v[120:123], a[224:227], a[92:95], v[120:123]\n"
    "v_mfma_f32_16x16x32_bf16 v[124:127], a[228:231], a[92:95], v[124:127]\n"
    "v_mfma_f32_16x16x32_bf16 v[128:131], a[232:235], a[92:95], v[128:131]\n"
    "buffer_load_dwordx4 v11, s[76:79], s64 offen offset:0 lds\n"
    "s_waitcnt lgkmcnt(3)\n"
    "v_mfma_f32_16x16x32_bf16 v[20:23], a[236:239], a[96:99], v[20:23]\n"
    "ds_read_b128 a[224:227], v2 offset:18432\n"
    "ds_read_b128 a[228:231], v2 offset:19456\n"
    "v_mfma_f32_16x16x32_bf16 v[108:111], a[240:243], a[96:99], v[108:111]\n"
    "ds_read_b128 a[232:235], v2 offset:20480\n"
    "v_mfma_f32_16x16x32_bf16 v[112:115], a[244:247], a[96:99], v[112:115]\n"
    "v_mfma_f32_16x16x32_bf16 v[120:123], a[236:239], a[100:103], v[120:123]\n"
    "buffer_load_dwordx4 v12, s[76:79], s64 offen offset:1024 lds\n"
    "v_mfma_f32_16x16x32_bf16 v[124:127], a[240:243], a[100:103], v[124:127]\n"
    "v_mfma_f32_16x16x32_bf16 v[128:131], a[244:247], a[100:103], v[128:131]\n"
    "s_waitcnt lgkmcnt(3)\n"
    "v_mfma_f32_16x16x32_bf16 v[20:23], a[248:251], a[104:107], v[20:23]\n"
    "ds_read_b128 a[236:239], v2 offset:21504\n"
    "ds_read_b128 a[240:243], v2 offset:22528\n"
    "v_mfma_f32_16x16x32_bf16 v[108:111], a[252:255], a[104:107], v[108:111]\n"
    "ds_read_b128 a[244:247], v2 offset:23552\n"
    "v_mfma_f32_16x16x32_bf16 v[112:115], v[252:255], a[104:107], v[112:115]\n"
    "s_waitcnt vmcnt(15)\n"
    "ds_read_b128 a[64:67], v16 offset:0\n"
    "v_mfma_f32_16x16x32_bf16 v[120:123], a[248:251], a[108:111], v[120:123]\n"
    "ds_read_b128 a[68:71], v17 offset:0\n"
    "ds_read_b128 a[72:75], v16 offset:1024\n"
    "v_mfma_f32_16x16x32_bf16 v[124:127], a[252:255], a[108:111], v[124:127]\n"
    "ds_read_b128 a[76:79], v17 offset:1024\n"
    "ds_read_b128 a[80:83], v16 offset:2048\n"
    "v_mfma_f32_16x16x32_bf16 v[128:131], v[252:255], a[108:111], v[128:131]\n"
    "ds_read_b128 a[84:87], v17 offset:2048\n"
    "ds_read_b128 a[88:91], v16 offset:3072\n"
    "ds_read_b128 a[92:95], v17 offset:3072\n"
    "s_waitcnt lgkmcnt(11)\n"
    "v_mfma_f32_16x16x32_bf16 v[20:23], a[224:227], a[112:115], v[20:23]\n"
    "ds_read_b128 a[248:251], v2 offset:24576\n"
    "ds_read_b128 a[252:255], v2 offset:25600\n"
    "buffer_load_dwordx4 v13, s[76:79], s64 offen offset:2048 lds\n"
    "v_mfma_f32_16x16x32_bf16 v[108:111], a[228:231], a[112:115], v[108:111]\n"
    "ds_read_b128 v[252:255], v2 offset:26624\n"
    "v_mfma_f32_16x16x32_bf16 v[112:115], a[232:235], a[112:115], v[112:115]\n"
    "v_mfma_f32_16x16x32_bf16 v[120:123], a[224:227], a[116:119], v[120:123]\n"
    "v_mfma_f32_16x16x32_bf16 v[124:127], a[228:231], a[116:119], v[124:127]\n"
    "buffer_load_dwordx4 v14, s[76:79], s64 offen offset:3072 lds\n"
    "v_mfma_f32_16x16x32_bf16 v[128:131], a[232:235], a[116:119], v[128:131]\n"
    "s_waitcnt lgkmcnt(3)\n"
    "v_mfma_f32_16x16x32_bf16 v[20:23], a[236:239], a[120:123], v[20:23]\n"
    "ds_read_b128 a[224:227], v2 offset:27648\n"
    "ds_read_b128 a[228:231], v2 offset:28672\n"
    "v_mfma_f32_16x16x32_bf16 v[108:111], a[240:243], a[120:123], v[108:111]\n"
    "ds_read_b128 a[232:235], v2 offset:29696\n"
    "v_mfma_f32_16x16x32_bf16 v[112:115], a[244:247], a[120:123], v[112:115]\n"
    "buffer_load_dwordx4 v[64:67], v0, s[68:71], s10 offen offset:0 sc1\n"
    "v_mfma_f32_16x16x32_bf16 v[120:123], a[236:239], a[124:127], v[120:123]\n"
    "v_mfma_f32_16x16x32_bf16 v[124:127], a[240:243], a[124:127], v[124:127]\n"
    "v_mfma_f32_16x16x32_bf16 v[128:131], a[244:247], a[124:127], v[128:131]\n"
    "s_waitcnt lgkmcnt(3)\n"
    "v_mfma_f32_16x16x32_bf16 v[20:23], a[248:251], a[128:131], v[20:23]\n"
    "ds_read_b128 a[236:239], v2 offset:30720\n"
    "ds_read_b128 a[240:243], v2 offset:31744\n"
    "v_mfma_f32_16x16x32_bf16 v[108:111], a[252:255], a[128:131], v[108:111]\n"
    "ds_read_b128 a[244:247], v2 offset:32768\n"
    "v_mfma_f32_16x16x32_bf16 v[112:115], v[252:255], a[128:131], v[112:115]\n"
    "buffer_load_dwordx4 v[68:71], v0, s[68:71], s10 offen offset:1024 sc1\n"
    "v_mfma_f32_16x16x32_bf16 v[120:123], a[248:251], a[132:135], v[120:123]\n"
    "v_mfma_f32_16x16x32_bf16 v[124:127], a[252:255], a[132:135], v[124:127]\n"
    "v_mfma_f32_16x16x32_bf16 v[128:131], v[252:255], a[132:135], v[128:131]\n"
    "s_waitcnt lgkmcnt(3)\n"
    "v_mfma_f32_16x16x32_bf16 v[20:23], a[224:227], a[136:139], v[20:23]\n"
    "ds_read_b128 a[248:251], v2 offset:33792\n"
    "ds_read_b128 a[252:255], v2 offset:34816\n"
    "v_mfma_f32_16x16x32_bf16 v[108:111], a[228:231], a[136:139], v[108:111]\n"
    "ds_read_b128 v[252:255], v2 offset:35840\n"
    "buffer_load_dwordx4 v[72:75], v0, s[68:71], s10 offen offset:2048 sc1\n"
    "v_mfma_f32_16x16x32_bf16 v[112:115], a[232:235], a[136:139], v[112:115]\n"
    "v_mfma_f32_16x16x32_bf16 v[120:123], a[224:227], a[140:143], v[120:123]\n"
    "v_mfma_f32_16x16x32_bf16 v[124:127], a[228:231], a[140:143], v[124:127]\n"
    "v_mfma_f32_16x16x32_bf16 v[128:131], a[232:235], a[140:143], v[128:131]\n"
    "buffer_load_dwordx4 v[76:79], v0, s[68:71], s10 offen offset:3072 sc1\n"
    "s_waitcnt lgkmcnt(3)\n"
    "v_mfma_f32_16x16x32_bf16 v[20:23], a[236:239], a[144:147], v[20:23]\n"
    "ds_read_b128 a[224:227], v2 offset:36864\n"
    "ds_read_b128 a[228:231], v2 offset:37888\n"
    "v_mfma_f32_16x16x32_bf16 v[108:111], a[240:243], a[144:147], v[108:111]\n"
    "ds_read_b128 a[232:235], v2 offset:38912\n"
    "v_mfma_f32_16x16x32_bf16 v[112:115], a[244:247], a[144:147], v[112:115]\n"
    "v_mfma_f32_16x16x32_bf16 v[120:123], a[236:239], a[148:151], v[120:123]\n"
    "buffer_load_dwordx4 v[80:83], v0, s[68:71], s5 offen offset:0 sc1\n"
    "v_mfma_f32_16x16x32_bf16 v[124:127], a[240:243], a[148:151], v[124:127]\n"
    "v_mfma_f32_16x16x32_bf16 v[128:131], a[244:247], a[148:151], v[128:131]\n"
    "s_waitcnt lgkmcnt(3)\n"
    "v_mfma_f32_16x16x32_bf16 v[20:23], a[248:251], a[152:155], v[20:23]\n"
    "ds_read_b128 a[236:239], v2 offset:39936\n"
    "ds_read_b128 a[240:243], v2 offset:40960\n"
    "v_mfma_f32_16x16x32_bf16 v[108:111], a[252:255], a[152:155], v[108:111]\n"
    "ds_read_b128 a[244:247], v2 offset:41984\n"
    "v_mfma_f32_16x16x32_bf16 v[112:115], v[252:255], a[152:155], v[112:115]\n"
    "s_waitcnt vmcnt(13)\n"
    "ds_read_b128 a[96:99], v16 offset:32768\n"
    "v_mfma_f32_16x16x32_bf16 v[120:123], a[248:251], a[156:159], v[120:123]\n"
    "ds_read_b128 a[100:103], v17 offset:32768\n"
    "ds_read_b128 a[104:107], v16 offset:33792\n"
    "v_mfma_f32_16x16x32_bf16 v[124:127], a[252:255], a[156:159], v[124:127]\n"
    "ds_read_b128 a[108:111], v17 offset:33792\n"
    "ds_read_b128 a[112:115], v16 offset:34816\n"
    "buffer_load_dwordx4 v[84:87], v0, s[68:71], s5 offen offset:1024 sc1\n"
    "v_mfma_f32_16x16x32_bf16 v[128:131], v[252:255], a[156:159], v[128:131]\n"
    "ds_read_b128 a[116:119], v17 offset:34816\n"
    "ds_read_b128 a[120:123], v16 offset:35840\n"
    "ds_read_b128 a[124:127], v17 offset:35840\n"
    "s_waitcnt lgkmcnt(11)\n"
    "v_mfma_f32_16x16x32_bf16 v[20:23], a[224:227], a[160:163], v[20:23]\n"
    "ds_read_b128 a[248:251], v2 offset:43008\n"
    "ds_read_b128 a[252:255], v2 offset:44032\n"
    "v_mfma_f32_16x16x32_bf16 v[108:111], a[228:231], a[160:163], v[108:111]\n"
    "ds_read_b128 v[252:255], v2 offset:45056\n"
    "v_mfma_f32_16x16x32_bf16 v[112:115], a[232:235], a[160:163], v[112:115]\n"
    "buffer_load_dwordx4 v[132:135], v0, s[68:71], s5 offen offset:2048 sc1\n"
    "v_mfma_f32_16x16x32_bf16 v[120:123], a[224:227], a[164:167], v[120:123]\n"
    "v_mfma_f32_16x16x32_bf16 v[124:127], a[228:231], a[164:167], v[124:127]\n"
    "v_mfma_f32_16x16x32_bf16 v[128:131], a[232:235], a[164:167], v[128:131]\n"
    "s_waitcnt lgkmcnt(11)\n"
    "v_mfma_f32_16x16x32_bf16 v[20:23], a[236:239], a[168:171], v[20:23]\n"
    "ds_read_b128 a[224:227], v2 offset:46080\n"
    "ds_read_b128 a[228:231], v2 offset:47104\n"
    "v_mfma_f32_16x16x32_bf16 v[108:111], a[240:243], a[168:171], v[108:111]\n"
    "ds_read_b128 a[232:235], v2 offset:48128\n"
    "v_mfma_f32_16x16x32_bf16 v[112:115], a[244:247], a[168:171], v[112:115]\n"
    "buffer_load_dwordx4 v[136:139], v0, s[68:71], s5 offen offset:3072 sc1\n"
    "v_mfma_f32_16x16x32_bf16 v[120:123], a[236:239], a[172:175], v[120:123]\n"
    "s_xor_b32 s64, s58, 66560\n"
    "s_add_u32 m0, s59, 65536\n"
    "v_mfma_f32_16x16x32_bf16 v[124:127], a[240:243], a[172:175], v[124:127]\n"
    "v_mfma_f32_16x16x32_bf16 v[128:131], a[244:247], a[172:175], v[128:131]\n"
    "s_waitcnt lgkmcnt(3)\n"
    "v_mfma_f32_16x16x32_bf16 v[20:23], a[248:251], a[176:179], v[20:23]\n"
    "ds_read_b128 a[236:239], v2 offset:49152\n"
    "ds_read_b128 a[240:243], v2 offset:50176\n"
    "v_mfma_f32_16x16x32_bf16 v[108:111], a[252:255], a[176:179], v[108:111]\n"
    "ds_read_b128 a[244:247], v2 offset:51200\n"
    "v_mfma_f32_16x16x32_bf16 v[112:115], v[252:255], a[176:179], v[112:115]\n"
    "buffer_load_dwordx4 v11, s[76:79], s64 offen offset:0 lds\n"
    "v_mfma_f32_16x16x32_bf16 v[120:123], a[248:251], a[180:183], v[120:123]\n"
    "v_mfma_f32_16x16x32_bf16 v[124:127], a[252:255], a[180:183], v[124:127]\n"
    "v_mfma_f32_16x16x32_bf16 v[128:131], v[252:255], a[180:183], v[128:131]\n"
    "s_waitcnt lgkmcnt(3)\n"
    "v_mfma_f32_16x16x32_bf16 v[20:23], a[224:227], a[184:187], v[20:23]\n"
    "ds_read_b128 a[248:251], v2 offset:52224\n"
    "ds_read_b128 a[252:255], v2 offset:53248\n"
    "v_mfma_f32_16x16x32_bf16 v[108:111], a[228:231], a[184:187], v[108:111]\n"
    "ds_read_b128 v[252:255], v2 offset:54272\n"
    "v_mfma_f32_16x16x32_bf16 v[112:115], a[232:235], a[184:187], v[112:115]\n"
    "buffer_load_dwordx4 v12, s[76:79], s64 offen offset:1024 lds\n"
    "v_mfma_f32_16x16x32_bf16 v[120:123], a[224:227], a[188:191], v[120:123]\n"
    "v_mfma_f32_16x16x32_bf16 v[124:127], a[228:231], a[188:191], v[124:127]\n"
    "v_mfma_f32_16x16x32_bf16 v[128:131], a[232:235], a[188:191], v[128:131]\n"
    "s_waitcnt vmcnt(2)\n"
    "v_xor_b32 v65, s8, v65\n"
    "v_bitop3_b32 v65, s8, v67, v65 bitop3:0xbe\n"
    "v_bitop3_b32 v65, s8, v69, v65 bitop3:0xbe\n"
    "v_bitop3_b32 v65, s8, v71, v65 bitop3:0xbe\n"
    "v_bitop3_b32 v65, s8, v73, v65 bitop3:0xbe\n"
    "v_bitop3_b32 v65, s8, v75, v65 bitop3:0xbe\n"
    "v_bitop3_b32 v65, s8, v77, v65 bitop3:0xbe\n"
    "v_bitop3_b32 v65, s8, v79, v65 bitop3:0xbe\n"
    "v_bitop3_b32 v65, s8, v81, v65 bitop3:0xbe\n"
    "v_bitop3_b32 v65, s8, v83, v65 bitop3:0xbe\n"
    "v_bitop3_b32 v65, s8, v85, v65 bitop3:0xbe\n"
    "v_bitop3_b32 v65, s8, v87, v65 bitop3:0xbe\n"
    "v_bitop3_b32 v65, s8, v133, v65 bitop3:0xbe\n"
    "v_bitop3_b32 v65, s8, v135, v65 bitop3:0xbe\n"
    "v_bitop3_b32 v65, s8, v137, v65 bitop3:0xbe\n"
    "v_bitop3_b32 v65, s8, v139, v65 bitop3:0xbe\n"
    "v_cmp_ne_u32 vcc, 0, v65\n"
    "s_nop 0\n"
    "s_cbranch_vccnz R20_%=\n"
    "B21_%=:\n"
    "v_add_f32 v64, v64, v68\n"
    "v_add_f32 v66, v66, v70\n"
    "v_add_f32 v80, v80, v84\n"
    "v_add_f32 v82, v82, v86\n"
    "v_add_f32 v72, v72, v76\n"
    "v_add_f32 v74, v74, v78\n"
    "v_add_f32 v132, v132, v136\n"
    "v_add_f32 v134, v134, v138\n"
    "v_add_f32 v64, v64, v72\n"
    "v_add_f32 v66, v66, v74\n"
    "v_add_f32 v80, v80, v132\n"
    "v_add_f32 v82, v82, v134\n"
    "v_add_f32_dpp v64, v64, v64 row_ror:4 row_mask:0xf bank_mask:0xf\n"
    "v_add_f32_dpp v80, v80, v80 row_ror:4 row_mask:0xf bank_mask:0xf\n"
    "v_add_f32_dpp v66, v66, v66 row_ror:4 row_mask:0xf bank_mask:0xf\n"
    "v_add_f32_dpp v82, v82, v82 row_ror:4 row_mask:0xf bank_mask:0xf\n"
    "v_add_f32_dpp v64, v64, v64 row_ror:8 row_mask:0xf bank_mask:0xf\n"
    "v_add_f32_dpp v80, v80, v80 row_ror:8 row_mask:0xf bank_mask:0xf\n"
    "v_add_f32_dpp v66, v66, v66 row_ror:8 row_mask:0xf bank_mask:0xf\n"
    "v_add_f32_dpp v82, v82, v82 row_ror:8 row_mask:0xf bank_mask:0xf\n"
    "v_mul_f32 v64, 1.302083333e-03, v64\n"
    "v_mul_f32 v80, 1.302083333e-03, v80\n"
    "v_mul_f32 v66, 1.302083333e-03, v66\n"
    "v_mul_f32 v82, 1.302083333e-03, v82\n"
    "v_fma_f32 v66, -v64, v64, v66\n"
    "v_fma_f32 v82, -v80, v80, v82\n"
    "v_max_f32 v66, 0, v66\n"
    "v_max_f32 v82, 0, v82\n"
    "v_add_f32 v66, 1.0e-5, v66\n"
    "v_add_f32 v82, 1.0e-5, v82\n"
    "v_rsq_f32 v66, v66\n"
    "v_rsq_f32 v82, v82\n"
    "v_mul_f32 v64, v64, v66\n"
    "v_mul_f32 v80, v80, v82\n"
    "v_mov_b32_dpp v92, v66 quad_perm:[0,0,0,0] row_mask:0xf bank_mask:0xf\n"
    "v_mov_b32_dpp v93, v66 quad_perm:[1,1,1,1] row_mask:0xf bank_mask:0xf\n"
    "v_mov_b32_dpp v94, v66 quad_perm:[2,2,2,2] row_mask:0xf bank_mask:0xf\n"
    "v_mov_b32_dpp v95, v66 quad_perm:[3,3,3,3] row_mask:0xf bank_mask:0xf\n"
    "v_mov_b32_dpp v96, v82 quad_perm:[0,0,0,0] row_mask:0xf bank_mask:0xf\n"
    "v_mov_b32_dpp v97, v82 quad_perm:[1,1,1,1] row_mask:0xf bank_mask:0xf\n"
    "v_mov_b32_dpp v98, v82 quad_perm:[2,2,2,2] row_mask:0xf bank_mask:0xf\n"
    "v_mov_b32_dpp v99, v82 quad_perm:[3,3,3,3] row_mask:0xf bank_mask:0xf\n"
    "v_mov_b32_dpp v100, v64 quad_perm:[0,0,0,0] row_mask:0xf bank_mask:0xf\n"
    "v_mov_b32_dpp v101, v64 quad_perm:[1,1,1,1] row_mask:0xf bank_mask:0xf\n"
    "v_mov_b32_dpp v102, v64 quad_perm:[2,2,2,2] row_mask:0xf bank_mask:0xf\n"
    "v_mov_b32_dpp v103, v64 quad_perm:[3,3,3,3] row_mask:0xf bank_mask:0xf\n"
    "v_mov_b32_dpp v104, v80 quad_perm:[0,0,0,0] row_mask:0xf bank_mask:0xf\n"
    "v_mov_b32_dpp v105, v80 quad_perm:[1,1,1,1] row_mask:0xf bank_mask:0xf\n"
    "v_mov_b32_dpp v106, v80 quad_perm:[2,2,2,2] row_mask:0xf bank_mask:0xf\n"
    "v_mov_b32_dpp v107, v80 quad_perm:[3,3,3,3] row_mask:0xf bank_mask:0xf\n"
    "v_pk_fma_f32 v[40:41], v[40:41], v[92:93], v[100:101] neg_lo:[0,0,1] neg_hi:[0,0,1]\n"
    "v_pk_fma_f32 v[42:43], v[42:43], v[94:95], v[102:103] neg_lo:[0,0,1] neg_hi:[0,0,1]\n"
    "v_pk_fma_f32 v[52:53], v[52:53], v[96:97], v[104:105] neg_lo:[0,0,1] neg_hi:[0,0,1]\n"
    "v_pk_fma_f32 v[54:55], v[54:55], v[98:99], v[106:107] neg_lo:[0,0,1] neg_hi:[0,0,1]\n"
    "v_cvt_pk_bf16_f32 v68, v40, v41\n"
    "v_cvt_pk_bf16_f32 v69, v42, v43\n"
    "v_cvt_pk_bf16_f32 v70, v52, v53\n"
    "v_cvt_pk_bf16_f32 v71, v54, v55\n"
    "s_nop 0\n"
    "v_permlane16_swap_b32 v68, v70\n"
    "v_permlane16_swap_b32 v69, v71\n"
    "buffer_store_dwordx4 v[68:71], v30, s[68:71], s11 offen sc1\n"
    "v_pk_fma_f32 v[44:45], v[44:45], v[92:93], v[100:101] neg_lo:[0,0,1] neg_hi:[0,0,1]\n"
    "v_pk_fma_f32 v[46:47], v[46:47], v[94:95], v[102:103] neg_lo:[0,0,1] neg_hi:[0,0,1]\n"
    "v_pk_fma_f32 v[56:57], v[56:57], v[96:97], v[104:105] neg_lo:[0,0,1] neg_hi:[0,0,1]\n"
    "v_pk_fma_f32 v[58:59], v[58:59], v[98:99], v[106:107] neg_lo:[0,0,1] neg_hi:[0,0,1]\n"
    "v_cvt_pk_bf16_f32 v72, v44, v45\n"
    "v_cvt_pk_bf16_f32 v73, v46, v47\n"
    "v_cvt_pk_bf16_f32 v74, v56, v57\n"
    "v_cvt_pk_bf16_f32 v75, v58, v59\n"
    "s_add_u32 s11, s11, 65536\n"
    "v_permlane16_swap_b32 v72, v74\n"
    "v_permlane16_swap_b32 v73, v75\n"
    "buffer_store_dwordx4 v[72:75], v30, s[68:71], s11 offen sc1\n"
    "v_pk_fma_f32 v[48:49], v[48:49], v[92:93], v[100:101] neg_lo:[0,0,1] neg_hi:[0,0,1]\n"
    "v_pk_fma_f32 v[50:51], v[50:51], v[94:95], v[102:103] neg_lo:[0,0,1] neg_hi:[0,0,1]\n"
    "v_pk_fma_f32 v[60:61], v[60:61], v[96:97], v[104:105] neg_lo:[0,0,1] neg_hi:[0,0,1]\n"
    "v_pk_fma_f32 v[62:63], v[62:63], v[98:99], v[106:107] neg_lo:[0,0,1] neg_hi:[0,0,1]\n"
    "v_cvt_pk_bf16_f32 v76, v48, v49\n"
    "v_cvt_pk_bf16_f32 v77, v50, v51\n"
    "v_cvt_pk_bf16_f32 v78, v60, v61\n"
    "v_cvt_pk_bf16_f32 v79, v62, v63\n"
    "s_add_u32 s11, s11, 65536\n"
    "v_permlane16_swap_b32 v76, v78\n"
    "v_permlane16_swap_b32 v77, v79\n"
    "buffer_store_dwordx4 v[76:79], v30, s[68:71], s11 offen sc1\n"
    "s_waitcnt lgkmcnt(3)\n"
    "v_mfma_f32_16x16x32_bf16 v[20:23], a[236:239], a[192:195], v[20:23]\n"
    "ds_read_b128 a[224:227], v2 offset:55296\n"
    "ds_read_b128 a[228:231], v2 offset:56320\n"
    "v_mfma_f32_16x16x32_bf16 v[108:111], a[240:243], a[192:195], v[108:111]\n"
    "ds_read_b128 a[232:235], v2 offset:57344\n"
    "v_mfma_f32_16x16x32_bf16 v[112:115], a[244:247], a[192:195], v[112:115]\n"
    "buffer_load_dwordx4 v13, s[76:79], s64 offen offset:2048 lds\n"
    "v_mfma_f32_16x16x32_bf16 v[120:123], a[236:239], a[196:199], v[120:123]\n"
    "v_mfma_f32_16x16x32_bf16 v[124:127], a[240:243], a[196:199], v[124:127]\n"
    "v_mfma_f32_16x16x32_bf16 v[128:131], a[244:247], a[196:199], v[128:131]\n"
    "s_waitcnt lgkmcnt(3)\n"
    "v_mfma_f32_16x16x32_bf16 v[20:23], a[248:251], a[200:203], v[20:23]\n"
    "ds_read_b128 a[236:239], v2 offset:58368\n"
    "ds_read_b128 a[240:243], v2 offset:59392\n"
    "buffer_load_dwordx4 v14, s[76:79], s64 offen offset:3072 lds\n"
    "s_xor_b32 s64, s58, 99328\n"
    "s_add_u32 m0, s59, 69632\n"
    "v_mfma_f32_16x16x32_bf16 v[108:111], a[252:255], a[200:203], v[108:111]\n"
    "ds_read_b128 a[244:247], v2 offset:60416\n"
    "v_mfma_f32_16x16x32_bf16 v[112:115], v[252:255], a[200:203], v[112:115]\n"
    "v_mfma_f32_16x16x32_bf16 v[120:123], a[248:251], a[204:207], v[120:123]\n"
    "buffer_load_dwordx4 v11, s[76:79], s64 offen offset:0 lds\n"
    "v_mfma_f32_16x16x32_bf16 v[124:127], a[252:255], a[204:207], v[124:127]\n"
    "v_mfma_f32_16x16x32_bf16 v[128:131], v[252:255], a[204:207], v[128:131]\n"
    "s_waitcnt lgkmcnt(3)\n"
    "v_mfma_f32_16x16x32_bf16 v[20:23], a[224:227], a[208:211], v[20:23]\n"
    "v_mfma_f32_16x16x32_bf16 v[108:111], a[228:231], a[208:211], v[108:111]\n"
    "buffer_load_dwordx4 v12, s[76:79], s64 offen offset:1024 lds\n"
    "v_mfma_f32_16x16x32_bf16 v[112:115], a[232:235], a[208:211], v[112:115]\n"
    "v_mfma_f32_16x16x32_bf16 v[120:123], a[224:227], a[212:215], v[120:123]\n"
    "v_mfma_f32_16x16x32_bf16 v[124:127], a[228:231], a[212:215], v[124:127]\n"
    "v_mfma_f32_16x16x32_bf16 v[128:131], a[232:235], a[212:215], v[128:131]\n"
    "buffer_load_dwordx4 v13, s[76:79], s64 offen offset:2048 lds\n"
    "s_waitcnt lgkmcnt(0)\n"
    "v_mfma_f32_16x16x32_bf16 v[20:23], a[236:239], a[216:219], v[20:23]\n"
    "v_mfma_f32_16x16x32_bf16 v[108:111], a[240:243], a[216:219], v[108:111]\n"
    "v_mfma_f32_16x16x32_bf16 v[112:115], a[244:247], a[216:219], v[112:115]\n"
    "v_mfma_f32_16x16x32_bf16 v[120:123], a[236:239], a[220:223], v[120:123]\n"
    "v_mfma_f32_16x16x32_bf16 v[124:127], a[240:243], a[220:223], v[124:127]\n"
    "v_mfma_f32_16x16x32_bf16 v[128:131], a[244:247], a[220:223], v[128:131]\n"
    "s_nop 2\n"
    "v_pk_mul_f32 v[20:21], v[20:21], v[116:117] op_sel_hi:[1,0]\n"
    "v_pk_mul_f32 v[22:23], v[22:23], v[116:117] op_sel_hi:[1,0]\n"
    "v_pk_mul_f32 v[108:109], v[108:109], v[116:117] op_sel_hi:[1,0]\n"
    "v_pk_mul_f32 v[110:111], v[110:111], v[116:117] op_sel_hi:[1,0]\n"
    "v_pk_mul_f32 v[112:113], v[112:113], v[116:117] op_sel_hi:[1,0]\n"
    "v_pk_mul_f32 v[114:115], v[114:115], v[116:117] op_sel_hi:[1,0]\n"
    "v_pk_mul_f32 v[120:121], v[120:121], v[118:119] op_sel_hi:[1,0]\n"
    "v_pk_mul_f32 v[122:123], v[122:123], v[118:119] op_sel_hi:[1,0]\n"
    "v_pk_mul_f32 v[124:125], v[124:125], v[118:119] op_sel_hi:[1,0]\n"
    "v_pk_mul_f32 v[126:127], v[126:127], v[118:119] op_sel_hi:[1,0]\n"
    "v_pk_mul_f32 v[128:129], v[128:129], v[118:119] op_sel_hi:[1,0]\n"
    "v_pk_mul_f32 v[130:131], v[130:131], v[118:119] op_sel_hi:[1,0]\n"
    "v_pk_fma_f32 v[20:21], v[24:25], v[116:117], v[20:21] op_sel:[0,1,0] neg_lo:[1,0,0] neg_hi:[1,0,0]\n"
    "v_pk_fma_f32 v[22:23], v[26:27], v[116:117], v[22:23] op_sel:[0,1,0] neg_lo:[1,0,0] neg_hi:[1,0,0]\n"
    "v_pk_fma_f32 v[108:109], v[32:33], v[116:117], v[108:109] op_sel:[0,1,0] neg_lo:[1,0,0] neg_hi:[1,0,0]\n"
    "v_pk_fma_f32 v[110:111], v[34:35], v[116:117], v[110:111] op_sel:[0,1,0] neg_lo:[1,0,0] neg_hi:[1,0,0]\n"
    "v_pk_fma_f32 v[112:113], v[36:37], v[116:117], v[112:113] op_sel:[0,1,0] neg_lo:[1,0,0] neg_hi:[1,0,0]\n"
    "v_pk_fma_f32 v[114:115], v[38:39], v[116:117], v[114:115] op_sel:[0,1,0] neg_lo:[1,0,0] neg_hi:[1,0,0]\n"
    "v_pk_fma_f32 v[120:121], v[24:25], v[118:119], v[120:121] op_sel:[0,1,0] neg_lo:[1,0,0] neg_hi:[1,0,0]\n"
    "v_pk_fma_f32 v[122:123], v[26:27], v[118:119], v[122:123] op_sel:[0,1,0] neg_lo:[1,0,0] neg_hi:[1,0,0]\n"
    "v_pk_fma_f32 v[124:125], v[32:33], v[118:119], v[124:125] op_sel:[0,1,0] neg_lo:[1,0,0] neg_hi:[1,0,0]\n"
    "v_pk_fma_f32 v[126:127], v[34:35], v[118:119], v[126:127] op_sel:[0,1,0] neg_lo:[1,0,0] neg_hi:[1,0,0]\n"
    "v_pk_fma_f32 v[128:129], v[36:37], v[118:119], v[128:129] op_sel:[0,1,0] neg_lo:[1,0,0] neg_hi:[1,0,0]\n"
    "v_pk_fma_f32 v[130:131], v[38:39], v[118:119], v[130:131] op_sel:[0,1,0] neg_lo:[1,0,0] neg_hi:[1,0,0]\n"
    "v_pk_mul_f32 v[64:65], v[20:21], v[20:21]\n"
    "v_pk_mul_f32 v[66:67], v[22:23], v[22:23]\n"
    "v_pk_mul_f32 v[68:69], v[108:109], v[108:109]\n"
    "v_pk_mul_f32 v[70:71], v[110:111], v[110:111]\n"
    "v_pk_mul_f32 v[72:73], v[112:113], v[112:113]\n"
    "v_pk_mul_f32 v[74:75], v[114:115], v[114:115]\n"
    "v_pk_mul_f32 v[76:77], v[120:121], v[120:121]\n"
    "v_pk_mul_f32 v[78:79], v[122:123], v[122:123]\n"
    "v_pk_mul_f32 v[80:81], v[124:125], v[124:125]\n"
    "v_pk_mul_f32 v[82:83], v[126:127], v[126:127]\n"
    "v_pk_mul_f32 v[84:85], v[128:129], v[128:129]\n"
    "v_pk_mul_f32 v[86:87], v[130:131], v[130:131]\n"
    "v_pk_fma_f32 v[64:65], v[64:65], s[12:13], s[12:13] op_sel:[0,0,1] op_sel_hi:[1,0,1]\n"
    "v_pk_fma_f32 v[66:67], v[66:67], s[12:13], s[12:13] op_sel:[0,0,1] op_sel_hi:[1,0,1]\n"
    "v_pk_fma_f32 v[68:69], v[68:69], s[12:13], s[12:13] op_sel:[0,0,1] op_sel_hi:[1,0,1]\n"
    "v_pk_fma_f32 v[70:71], v[70:71], s[12:13], s[12:13] op_sel:[0,0,1] op_sel_hi:[1,0,1]\n"
    "v_pk_fma_f32 v[72:73], v[72:73], s[12:13], s[12:13] op_sel:[0,0,1] op_sel_hi:[1,0,1]\n"
    "v_pk_fma_f32 v[74:75], v[74:75], s[12:13], s[12:13] op_sel:[0,0,1] op_sel_hi:[1,0,1]\n"
    "v_pk_fma_f32 v[76:77], v[76:77], s[12:13], s[12:13] op_sel:[0,0,1] op_sel_hi:[1,0,1]\n"
    "v_pk_fma_f32 v[78:79], v[78:79], s[12:13], s[12:13] op_sel:[0,0,1] op_sel_hi:[1,0,1]\n"
    "v_pk_fma_f32 v[80:81], v[80:81], s[12:13], s[12:13] op_sel:[0,0,1] op_sel_hi:[1,0,1]\n"
    "v_pk_fma_f32 v[82:83], v[82:83], s[12:13], s[12:13] op_sel:[0,0,1] op_sel_hi:[1,0,1]\n"
    "v_pk_fma_f32 v[84:85], v[84:85], s[12:13], s[12:13] op_sel:[0,0,1] op_sel_hi:[1,0,1]\n"
    "v_pk_fma_f32 v[86:87], v[86:87], s[12:13], s[12:13] op_sel:[0,0,1] op_sel_hi:[1,0,1]\n"
    "s_waitcnt vmcnt(5)\n"
    "s_barrier\n"
    "s_cmp_eq_u32 s6, 0\n"
    "s_cbranch_scc0 F22_%=\n"
    "s_mov_b64 exec, 1\n"
    "buffer_atomic_swap v5, v9, s[68:71], s16 offen sc1\n"
    "s_mov_b64 exec, -1\n"
    "F22_%=:\n"
    "v_pk_mul_f32 v[64:65], v[64:65], v[20:21]\n"
    "buffer_load_dwordx4 v14, s[76:79], s64 offen offset:3072 lds\n"
    "s_xor_b32 s64, s58, 67072\n"
    "s_add_u32 m0, s59, 98304\n"
    "v_pk_mul_f32 v[66:67], v[66:67], v[22:23]\n"
    "v_exp_f32 v64, v64\n"
    "v_exp_f32 v65, v65\n"
    "v_pk_mul_f32 v[68:69], v[68:69], v[108:109]\n"
    "v_exp_f32 v66, v66\n"
    "v_pk_add_f32 v[64:65], v[64:65], 1.0 op_sel_hi:[1,0]\n"
    "v_exp_f32 v67, v67\n"
    "buffer_load_dwordx4 v11, s[76:79], s64 offen offset:0 lds\n"
    "v_pk_mul_f32 v[70:71], v[70:71], v[110:111]\n"
    "v_exp_f32 v68, v68\n"
    "v_pk_add_f32 v[66:67], v[66:67], 1.0 op_sel_hi:[1,0]\n"
    "v_exp_f32 v69, v69\n"
    "v_rcp_f32 v64, v64\n"
    "v_rcp_f32 v65, v65\n"
    "v_pk_mul_f32 v[72:73], v[72:73], v[112:113]\n"
    "v_exp_f32 v70, v70\n"
    "buffer_load_dwordx4 v12, s[76:79], s64 offen offset:1024 lds\n"
    "v_pk_add_f32 v[68:69], v[68:69], 1.0 op_sel_hi:[1,0]\n"
    "v_exp_f32 v71, v71\n"
    "v_pk_mul_f32 v[20:21], v[20:21], v[64:65]\n"
    "v_rcp_f32 v66, v66\n"
    "v_rcp_f32 v67, v67\n"
    "v_pk_mul_f32 v[74:75], v[74:75], v[114:115]\n"
    "v_exp_f32 v72, v72\n"
    "v_pk_add_f32 v[70:71], v[70:71], 1.0 op_sel_hi:[1,0]\n"
    "buffer_load_dwordx4 v13, s[76:79], s64 offen offset:2048 lds\n"
    "v_exp_f32 v73, v73\n"
    "v_pk_mul_f32 v[22:23], v[22:23], v[66:67]\n"
    "v_rcp_f32 v68, v68\n"
    "v_rcp_f32 v69, v69\n"
    "v_pk_mul_f32 v[76:77], v[76:77], v[120:121]\n"
    "v_exp_f32 v74, v74\n"
    "v_pk_add_f32 v[72:73], v[72:73], 1.0 op_sel_hi:[1,0]\n"
    "v_exp_f32 v75, v75\n"
    "buffer_load_dwordx4 v14, s[76:79], s64 offen offset:3072 lds\n"
    "v_pk_mul_f32 v[108:109], v[108:109], v[68:69]\n"
    "s_xor_b32 s64, s58, 99840\n"
    "s_add_u32 m0, s59, 102400\n"
    "v_rcp_f32 v70, v70\n"
    "v_rcp_f32 v71, v71\n"
    "v_pk_mul_f32 v[78:79], v[78:79], v[122:123]\n"
    "v_exp_f32 v76, v76\n"
    "v_pk_add_f32 v[74:75], v[74:75], 1.0 op_sel_hi:[1,0]\n"
    "PAD6_%=:\n"
    "s_cbranch_execz PADRET1_%=\n"
    "v_exp_f32 v77, v77\n"
    "buffer_load_dwordx4 v11, s[76:79], s64 offen offset:0 lds\n"
    "v_pk_mul_f32 v[110:111], v[110:111], v[70:71]\n"
    "v_rcp_f32 v72, v72\n"
    "v_rcp_f32 v73, v73\n"
    "s_lshl1_add_u32 s38, s34, s20\n"
    "s_lshl2_add_u32 s38, s38, s6\n"
    "s_mul_i32 s38, s38, 1152\n"
    "s_add_u32 s4, s38, 4352\n"
    "s_lshl2_add_u32 s38, s34, s6\n"
    "s_mul_i32 s38, s38, 30720\n"
    "s_add_u32 s17, s38, 33685504\n"
    "s_lshl_b32 s38, s65, 6\n"
    "v_lshlrev_b32 v29, 1, v3\n"
    "buffer_load_ushort v92, v29, s[80:83], s38 offen\n"
    "buffer_load_ushort v96, v29, s[80:83], s38 offen offset:32\n"
    "buffer_load_dwordx4 v12, s[76:79], s64 offen offset:1024 lds\n"
    "buffer_load_dwordx4 v13, s[76:79], s64 offen offset:2048 lds\n"
    "buffer_load_dwordx4 v14, s[76:79], s64 offen offset:3072 lds\n"
    "s_add_u32 m0, s59, 0\n"
    "s_nop 0\n"
    "buffer_load_dwordx4 v2, s[68:71], s17 offen offset:0 sc1 lds\n"
    "buffer_load_dwordx4 v2, s[68:71], s17 offen offset:1024 sc1 lds\n"
    "v_mov_b32 v28, 0\n"
    "s_mov_b64 exec, 0xf\n"
    "buffer_atomic_or v28, v9, s[68:71], s4 offen offset:0 sc0 sc1\n"
    "s_mov_b64 exec, -1\n"
    "s_add_u32 m0, s59, 0\n"
    "s_nop 0\n"
    "buffer_load_dwordx4 v2, s[68:71], s17 offen offset:2048 sc1 lds\n"
    "buffer_load_dwordx4 v2, s[68:71], s17 offen offset:3072 sc1 lds\n"
    "s_add_u32 m0, s59, 4096\n"
    "s_add_u32 s17, s17, 4096\n"
    "buffer_load_dwordx4 v2, s[68:71], s17 offen offset:0 sc1 lds\n"
    "buffer_load_dwordx4 v2, s[68:71], s17 offen offset:1024 sc1 lds\n"
    "buffer_load_dwordx4 v2, s[68:71], s17 offen offset:2048 sc1 lds\n"
    "buffer_load_dwordx4 v2, s[68:71], s17 offen offset:3072 sc1 lds\n"
    "s_add_u32 s38, s59, 131072\n"
    "v_add_u32 v0, s38, v2\n"
    "v_add_u32 v7, 98304, v2\n"
    "s_lshl_b32 s25, s6, 12\n"
    "s_xor_b32 s38, s21, 1\n"
    "s_mul_i32 s38, s38, 196608\n"
    "s_lshl2_add_u32 s39, s20, s6\n"
    "s_lshl_b32 s39, s39, 13\n"
    "s_add_u32 s38, s38, s39\n"
    "s_add_u32 s5, s38, 17039360\n"
    "s_mul_i32 s10, s20, 393216\n"
    "s_sub_u32 s10, s10, 331776\n"
    "s_xor_b32 s38, s3, 1\n"
    "s_lshl2_add_u32 s38, s38, s6\n"
    "s_lshl_b32 s39, s38, 13\n"
    "s_add_u32 s11, s39, 20185088\n"
    "s_lshl_b32 s38, s38, 7\n"
    "s_add_u32 s16, s38, 77824\n"
    "s_lshl2_add_u32 s24, s18, s6\n"
    "ds_read_b128 a[128:131], v18 offset:0\n"
    "ds_read_b128 a[132:135], v19 offset:0\n"
    "ds_read_b128 a[136:139], v18 offset:1024\n"
    "ds_read_b128 a[140:143], v19 offset:1024\n"
    "ds_read_b128 a[144:147], v18 offset:2048\n"
    "ds_read_b128 a[148:151], v19 offset:2048\n"
    "ds_read_b128 a[152:155], v18 offset:3072\n"
    "ds_read_b128 a[156:159], v19 offset:3072\n"
    "s_waitcnt vmcnt(19)\n"
    "ds_read_b128 a[160:163], v16 offset:0\n"
    "ds_read_b128 a[164:167], v17 offset:0\n"
    "ds_read_b128 a[168:171], v16 offset:1024\n"
    "ds_read_b128 a[172:175], v17 offset:1024\n"
    "ds_read_b128 a[176:179], v16 offset:2048\n"
    "ds_read_b128 a[180:183], v17 offset:2048\n"
    "ds_read_b128 a[184:187], v16 offset:3072\n"
    "ds_read_b128 a[188:191], v17 offset:3072\n"
    "s_waitcnt vmcnt(9)\n"
    "ds_read_b128 a[192:195], v16 offset:32768\n"
    "ds_read_b128 a[196:199], v17 offset:32768\n"
    "ds_read_b128 a[200:203], v16 offset:33792\n"
    "ds_read_b128 a[204:207], v17 offset:33792\n"
    "ds_read_b128 a[208:211], v16 offset:34816\n"
    "ds_read_b128 a[212:215], v17 offset:34816\n"
    "ds_read_b128 a[216:219], v16 offset:35840\n"
    "ds_read_b128 a[220:223], v17 offset:35840\n"
    "s_waitcnt lgkmcnt(0)\n"
    "s_barrier\n"
    "s_waitcnt vmcnt(6)\n"
    "v_cmp_ne_u32 vcc, s8, v28\n"
    "s_nop 0\n"
    "s_and_b64 vcc, vcc, 0xf\n"
    "s_cbranch_vccnz R23_%=\n"
    "B24_%=:\n"
    "s_add_u32 m0, s25, 98304\n"
    "s_nop 0\n"
    "buffer_load_dwordx4 v2, s[68:71], s5 offen offset:0 sc1 lds\n"
    "buffer_load_dwordx4 v2, s[68:71], s5 offen offset:1024 sc1 lds\n"
    "buffer_load_dwordx4 v2, s[68:71], s5 offen offset:2048 sc1 lds\n"
    "buffer_load_dwordx4 v2, s[68:71], s5 offen offset:3072 sc1 lds\n"
    "s_add_u32 m0, s25, 114688\n"
    "s_add_u32 s5, s5, 4096\n"
    "buffer_load_dwordx4 v2, s[68:71], s5 offen offset:0 sc1 lds\n"
    "buffer_load_dwordx4 v2, s[68:71], s5 offen offset:1024 sc1 lds\n"
    "buffer_load_dwordx4 v2, s[68:71], s5 offen offset:2048 sc1 lds\n"
    "buffer_load_dwordx4 v2, s[68:71], s5 offen offset:3072 sc1 lds\n"
    "v_pk_mul_f32 v[80:81], v[80:81], v[124:125]\n"
    "v_exp_f32 v78, v78\n"
    "v_pk_add_f32 v[76:77], v[76:77], 1.0 op_sel_hi:[1,0]\n"
    "v_exp_f32 v79, v79\n"
    "v_pk_mul_f32 v[112:113], v[112:113], v[72:73]\n"
    "v_rcp_f32 v74, v74\n"
    "v_rcp_f32 v75, v75\n"
    "v_pk_mul_f32 v[82:83], v[82:83], v[126:127]\n"
    "v_exp_f32 v80, v80\n"
    "v_pk_add_f32 v[78:79], v[78:79], 1.0 op_sel_hi:[1,0]\n"
    "v_exp_f32 v81, v81\n"
    "v_pk_mul_f32 v[114:115], v[114:115], v[74:75]\n"
    "v_rcp_f32 v76, v76\n"
    "v_rcp_f32 v77, v77\n"
    "v_pk_mul_f32 v[84:85], v[84:85], v[128:129]\n"
    "v_exp_f32 v82, v82\n"
    "v_pk_add_f32 v[80:81], v[80:81], 1.0 op_sel_hi:[1,0]\n"
    "v_exp_f32 v83, v83\n"
    "v_pk_mul_f32 v[120:121], v[120:121], v[76:77]\n"
    "v_rcp_f32 v78, v78\n"
    "v_rcp_f32 v79, v79\n"
    "v_pk_mul_f32 v[86:87], v[86:87], v[130:131]\n"
    "v_exp_f32 v84, v84\n"
    "v_pk_add_f32 v[82:83], v[82:83], 1.0 op_sel_hi:[1,0]\n"
    "v_exp_f32 v85, v85\n"
    "v_pk_mul_f32 v[122:123], v[122:123], v[78:79]\n"
    "v_rcp_f32 v80, v80\n"
    "v_rcp_f32 v81, v81\n"
    "v_exp_f32 v86, v86\n"
    "v_pk_add_f32 v[84:85], v[84:85], 1.0 op_sel_hi:[1,0]\n"
    "v_exp_f32 v87, v87\n"
    "v_pk_mul_f32 v[124:125], v[124:125], v[80:81]\n"
    "v_rcp_f32 v82, v82\n"
    "v_rcp_f32 v83, v83\n"
    "v_pk_add_f32 v[86:87], v[86:87], 1.0 op_sel_hi:[1,0]\n"
    "v_pk_mul_f32 v[126:127], v[126:127], v[82:83]\n"
    "v_rcp_f32 v84, v84\n"
    "v_rcp_f32 v85, v85\n"
    "s_nop 0\n"
    "v_pk_mul_f32 v[128:129], v[128:129], v[84:85]\n"
    "v_rcp_f32 v86, v86\n"
    "v_rcp_f32 v87, v87\n"
    "s_nop 0\n"
    "v_pk_mul_f32 v[130:131], v[130:131], v[86:87]\n"
    "s_lshl2_add_u32 s38, s3, s6\n"
    "s_lshl_b32 s38, s38, 13\n"
    "s_add_u32 s28, s38, 20185088\n"
    "s_mul_i32 s38, s65, 48\n"
    "s_mul_i32 s39, s34, 3\n"
    "s_add_u32 s38, s38, s39\n"
    "s_lshl_b32 s38, s38, 10\n"
    "s_add_u32 s22, s38, 28573696\n"
    "s_lshl_b32 s38, s20, 10\n"
    "s_add_u32 s26, s38, s22\n"
    "s_add_u32 s27, s26, 24576\n"
    "s_lshr_b32 s38, s34, 1\n"
    "s_lshl2_add_u32 s38, s19, s38\n"
    "s_mul_i32 s38, s38, 1152\n"
    "s_and_b32 s39, s21, 3\n"
    "s_lshl2_add_u32 s38, s39, s38\n"
    "s_add_u32 s23, s38, 4480\n"
    "v_and_b32 v29, 1, v4\n"
    "v_mul_u32_u24 v15, 24576, v29\n"
    "v_lshrrev_b32 v29, 1, v4\n"
    "v_lshl_add_u32 v15, v29, 8, v15\n"
    "v_lshl_add_u32 v15, v3, 4, v15\n"
    "s_lshl_b32 s38, s20, 9\n"
    "v_add_u32 v15, s38, v15\n"
    "s_mul_i32 s38, s65, 1152\n"
    "s_add_u32 s12, s38, 4480\n"
    "s_mul_i32 s38, s24, 20480\n"
    "s_mul_i32 s39, s34, 160\n"
    "s_add_u32 s13, s38, s39\n"
    "s_mul_i32 s38, s24, 24576\n"
    "s_add_u32 s30, s38, 28573696\n"
    "s_add_u32 s31, s38, 28581888\n"
    "s_add_u32 s41, s38, 28590080\n"
    "s_lshl2_add_u32 s38, s3, s6\n"
    "s_cmp_eq_u32 s38, 0\n"
    "s_cselect_b32 s38, 0, 0x7f000000\n"
    "v_mov_b32 v30, s38\n"
    "s_lshr_b32 s38, s24, 1\n"
    "s_lshr_b32 s39, s34, 1\n"
    "s_lshl2_add_u32 s38, s38, s39\n"
    "s_lshl1_add_u32 s38, s38, s36\n"
    "s_mul_i32 s39, s38, 5120\n"
    "s_and_b32 s40, s34, 1\n"
    "s_add_u32 s39, s39, 14417920\n"
    "s_lshl_b32 s38, s40, 11\n"
    "s_add_u32 s42, s38, s39\n"
    "s_lshl_b32 s40, s40, 9\n"
    "s_add_u32 s40, s40, s39\n"
    "s_add_u32 s43, s40, 4096\n"
    "s_add_u32 m0, s25, 131072\n"
    "s_add_u32 s5, s5, 61440\n"
    "buffer_load_dwordx4 v2, s[68:71], s5 offen offset:0 sc1 lds\n"
    "buffer_load_dwordx4 v2, s[68:71], s5 offen offset:1024 sc1 lds\n"
    "buffer_load_dwordx4 v2, s[68:71], s5 offen offset:2048 sc1 lds\n"
    "s_waitcnt vmcnt(7)\n"
    "s_barrier\n"
    "ds_read_b128 a[224:227], v7 offset:0\n"
    "ds_read_b128 a[228:231], v7 offset:1024\n"
    "ds_read_b128 a[232:235], v7 offset:2048\n"
    "ds_read_b128 a[236:239], v7 offset:3072\n"
    "ds_read_b128 a[240:243], v7 offset:4096\n"
    "ds_read_b128 a[244:247], v7 offset:5120\n"
    "ds_read_b128 a[248:251], v7 offset:6144\n"
    "ds_read_b128 a[252:255], v7 offset:7168\n"
    "ds_read_b128 v[252:255], v7 offset:8192\n"
    "ds_read_b128 v[216:219], v7 offset:9216\n"
    "ds_read_b128 v[212:215], v7 offset:10240\n"
    "ds_read_b128 v[208:211], v7 offset:11264\n"
    "ds_read_b128 v[204:207], v7 offset:12288\n"
    "ds_read_b128 v[200:203], v7 offset:13312\n"
    "ds_read_b128 v[196:199], v7 offset:14336\n"
    "ds_read_b128 v[192:195], v7 offset:15360\n"
    "s_waitcnt lgkmcnt(15)\n"
    "v_mfma_f32_16x16x32_bf16 v[40:43], a[224:227], a[0:3], 0\n"
    "buffer_load_dwordx4 v2, s[68:71], s5 offen offset:3072 sc1 lds\n"
    "v_mfma_f32_16x16x32_bf16 v[44:47], a[224:227], a[96:99], 0\n"
    "s_add_u32 m0, s25, 147456\n"
    "s_add_u32 s5, s5, 4096\n"
    "s_waitcnt lgkmcnt(14)\n"
    "v_mfma_f32_16x16x32_bf16 v[40:43], a[228:231], a[4:7], v[40:43]\n"
    "v_mfma_f32_16x16x32_bf16 v[44:47], a[228:231], a[100:103], v[44:47]\n"
    "buffer_load_dwordx4 v2, s[68:71], s5 offen offset:0 sc1 lds\n"
    "s_waitcnt lgkmcnt(13)\n"
    "v_mfma_f32_16x16x32_bf16 v[40:43], a[232:235], a[8:11], v[40:43]\n"
    "v_mfma_f32_16x16x32_bf16 v[44:47], a[232:235], a[104:107], v[44:47]\n"
    "s_waitcnt lgkmcnt(12)\n"
    "v_mfma_f32_16x16x32_bf16 v[40:43], a[236:239], a[12:15], v[40:43]\n"
    "buffer_load_dwordx4 v2, s[68:71], s5 offen offset:1024 sc1 lds\n"
    "v_mfma_f32_16x16x32_bf16 v[44:47], a[236:239], a[108:111], v[44:47]\n"
    "s_waitcnt lgkmcnt(11)\n"
    "v_mfma_f32_16x16x32_bf16 v[40:43], a[240:243], a[32:35], v[40:43]\n"
    "v_mfma_f32_16x16x32_bf16 v[44:47], a[240:243], a[128:131], v[44:47]\n"
    "s_waitcnt lgkmcnt(10)\n"
    "v_mfma_f32_16x16x32_bf16 v[40:43], a[244:247], a[36:39], v[40:43]\n"
    "s_waitcnt vmcnt(6) lgkmcnt(0)\n"
    "s_barrier\n"
    "buffer_load_dwordx4 v2, s[68:71], s5 offen offset:2048 sc1 lds\n"
    "v_mfma_f32_16x16x32_bf16 v[44:47], a[244:247], a[132:135], v[44:47]\n"
    "ds_read_b128 v[188:191], v7 offset:16384\n"
    "ds_read_b128 v[184:187], v7 offset:17408\n"
    "v_mfma_f32_16x16x32_bf16 v[40:43], a[248:251], a[40:43], v[40:43]\n"
    "v_mfma_f32_16x16x32_bf16 v[44:47], a[248:251], a[136:139], v[44:47]\n"
    "ds_read_b128 v[180:183], v7 offset:18432\n"
    "ds_read_b128 v[176:179], v7 offset:19456\n"
    "v_mfma_f32_16x16x32_bf16 v[40:43], a[252:255], a[44:47], v[40:43]\n"
    "buffer_load_dwordx4 v2, s[68:71], s5 offen offset:3072 sc1 lds\n"
    "v_mfma_f32_16x16x32_bf16 v[44:47], a[252:255], a[140:143], v[44:47]\n"
    "ds_read_b128 v[172:175], v7 offset:20480\n"
    "ds_read_b128 v[168:171], v7 offset:21504\n"
    "v_mfma_f32_16x16x32_bf16 v[40:43], v[252:255], v[248:251], v[40:43]\n"
    "s_add_u32 m0, s25, 98304\n"
    "s_add_u32 s5, s5, 61440\n"
    "v_mfma_f32_16x16x32_bf16 v[44:47], v[252:255], a[160:163], v[44:47]\n"
    "ds_read_b128 v[164:167], v7 offset:22528\n"
    "ds_read_b128 v[160:163], v7 offset:23552\n"
    "v_mfma_f32_16x16x32_bf16 v[40:43], v[216:219], v[244:247], v[40:43]\n"
    "buffer_load_dwordx4 v2, s[68:71], s5 offen offset:0 sc1 lds\n"
    "v_mfma_f32_16x16x32_bf16 v[44:47], v[216:219], a[164:167], v[44:47]\n"
    "ds_read_b128 v[156:159], v7 offset:24576\n"
    "ds_read_b128 v[152:155], v7 offset:25600\n"
    "v_mfma_f32_16x16x32_bf16 v[40:43], v[212:215], v[240:243], v[40:43]\n"
    "v_mfma_f32_16x16x32_bf16 v[44:47], v[212:215], a[168:171], v[44:47]\n"
    "ds_read_b128 v[116:119], v7 offset:26624\n"
    "ds_read_b128 v[84:87], v7 offset:27648\n"
    "v_mfma_f32_16x16x32_bf16 v[40:43], v[208:211], v[236:239], v[40:43]\n"
    "buffer_load_dwordx4 v2, s[68:71], s5 offen offset:1024 sc1 lds\n"
    "v_mfma_f32_16x16x32_bf16 v[44:47], v[208:211], a[172:175], v[44:47]\n"
    "ds_read_b128 v[80:83], v7 offset:28672\n"
    "ds_read_b128 v[76:79], v7 offset:29696\n"
    "v_mfma_f32_16x16x32_bf16 v[40:43], v[204:207], a[64:67], v[40:43]\n"
    "v_mfma_f32_16x16x32_bf16 v[44:47], v[204:207], a[192:195], v[44:47]\n"
    "ds_read_b128 v[72:75], v7 offset:30720\n"
    "ds_read_b128 v[68:71], v7 offset:31744\n"
    "v_mfma_f32_16x16x32_bf16 v[40:43], v[200:203], a[68:71], v[40:43]\n"
    "buffer_load_dwordx4 v2, s[68:71], s5 offen offset:2048 sc1 lds\n"
    "v_mfma_f32_16x16x32_bf16 v[44:47], v[200:203], a[196:199], v[44:47]\n"
    "v_mfma_f32_16x16x32_bf16 v[40:43], v[196:199], a[72:75], v[40:43]\n"
    "v_mfma_f32_16x16x32_bf16 v[44:47], v[196:199], a[200:203], v[44:47]\n"
    "v_mfma_f32_16x16x32_bf16 v[40:43], v[192:195], a[76:79], v[40:43]\n"
    "v_mfma_f32_16x16x32_bf16 v[44:47], v[192:195], a[204:207], v[44:47]\n"
    "s_waitcnt lgkmcnt(0)\n"
    "v_mfma_f32_16x16x32_bf16 v[40:43], v[188:191], a[16:19], v[40:43]\n"
    "s_waitcnt vmcnt(7)\n"
    "s_barrier\n"
    "v_mfma_f32_16x16x32_bf16 v[44:47], v[188:191], a[112:115], v[44:47]\n"
    "ds_read_b128 a[224:227], v7 offset:32768\n"
    "ds_read_b128 a[228:231], v7 offset:33792\n"
    "v_mfma_f32_16x16x32_bf16 v[40:43], v[184:187], a[20:23], v[40:43]\n"
    "buffer_load_dwordx4 v2, s[68:71], s5 offen offset:3072 sc1 lds\n"
    "v_mfma_f32_16x16x32_bf16 v[44:47], v[184:187], a[116:119], v[44:47]\n"
    "ds_read_b128 a[232:235], v7 offset:34816\n"
    "ds_read_b128 a[236:239], v7 offset:35840\n"
    "v_mfma_f32_16x16x32_bf16 v[40:43], v[180:183], a[24:27], v[40:43]\n"
    "s_add_u32 m0, s25, 114688\n"
    "s_add_u32 s5, s5, 4096\n"
    "v_mfma_f32_16x16x32_bf16 v[44:47], v[180:183], a[120:123], v[44:47]\n"
    "ds_read_b128 a[240:243], v7 offset:36864\n"
    "ds_read_b128 a[244:247], v7 offset:37888\n"
    "v_mfma_f32_16x16x32_bf16 v[40:43], v[176:179], a[28:31], v[40:43]\n"
    "buffer_load_dwordx4 v2, s[68:71], s5 offen offset:0 sc1 lds\n"
    "v_mfma_f32_16x16x32_bf16 v[44:47], v[176:179], a[124:127], v[44:47]\n"
    "ds_read_b128 a[248:251], v7 offset:38912\n"
    "ds_read_b128 a[252:255], v7 offset:39936\n"
    "v_mfma_f32_16x16x32_bf16 v[40:43], v[172:175], a[48:51], v[40:43]\n"
    "v_mfma_f32_16x16x32_bf16 v[44:47], v[172:175], a[144:147], v[44:47]\n"
    "ds_read_b128 v[252:255], v7 offset:40960\n"
    "ds_read_b128 v[216:219], v7 offset:41984\n"
    "v_mfma_f32_16x16x32_bf16 v[40:43], v[168:171], a[52:55], v[40:43]\n"
    "buffer_load_dwordx4 v2, s[68:71], s5 offen offset:1024 sc1 lds\n"
    "v_mfma_f32_16x16x32_bf16 v[44:47], v[168:171], a[148:151], v[44:47]\n"
    "ds_read_b128 v[212:215], v7 offset:43008\n"
    "ds_read_b128 v[208:211], v7 offset:44032\n"
    "v_mfma_f32_16x16x32_bf16 v[40:43], v[164:167], a[56:59], v[40:43]\n"
    "v_mfma_f32_16x16x32_bf16 v[44:47], v[164:167], a[152:155], v[44:47]\n"
    "ds_read_b128 v[204:207], v7 offset:45056\n"
    "ds_read_b128 v[200:203], v7 offset:46080\n"
    "v_mfma_f32_16x16x32_bf16 v[40:43], v[160:163], a[60:63], v[40:43]\n"
    "v_mfma_f32_16x16x32_bf16 v[44:47], v[160:163], a[156:159], v[44:47]\n"
    "ds_read_b128 v[196:199], v7 offset:47104\n"
    "ds_read_b128 v[192:195], v7 offset:48128\n"
    "v_mfma_f32_16x16x32_bf16 v[40:43], v[156:159], v[232:235], v[40:43]\n"
    "buffer_load_dwordx4 v2, s[68:71], s5 offen offset:2048 sc1 lds\n"
    "v_mfma_f32_16x16x32_bf16 v[44:47], v[156:159], a[176:179], v[44:47]\n"
    "v_mfma_f32_16x16x32_bf16 v[40:43], v[152:155], v[228:231], v[40:43]\n"
    "v_mfma_f32_16x16x32_bf16 v[44:47], v[152:155], a[180:183], v[44:47]\n"
    "v_mfma_f32_16x16x32_bf16 v[40:43], v[116:119], v[224:227], v[40:43]\n"
    "v_mfma_f32_16x16x32_bf16 v[44:47], v[116:119], a[184:187], v[44:47]\n"
    "buffer_load_dwordx4 v2, s[68:71], s5 offen offset:3072 sc1 lds\n"
    "v_mfma_f32_16x16x32_bf16 v[40:43], v[84:87], v[220:223], v[40:43]\n"
    "v_mfma_f32_16x16x32_bf16 v[44:47], v[84:87], a[188:191], v[44:47]\n"
    "v_mfma_f32_16x16x32_bf16 v[40:43], v[80:83], a[80:83], v[40:43]\n"
    "v_mfma_f32_16x16x32_bf16 v[44:47], v[80:83], a[208:211], v[44:47]\n"
    "v_mfma_f32_16x16x32_bf16 v[40:43], v[76:79], a[84:87], v[40:43]\n"
    "buffer_load_dwordx4 v[140:143], v2, s[68:71], s42 offen sc1\n"
    "v_mfma_f32_16x16x32_bf16 v[44:47], v[76:79], a[212:215], v[44:47]\n"
    "v_mfma_f32_16x16x32_bf16 v[40:43], v[72:75], a[88:91], v[40:43]\n"
    "v_mfma_f32_16x16x32_bf16 v[44:47], v[72:75], a[216:219], v[44:47]\n"
    "v_mfma_f32_16x16x32_bf16 v[40:43], v[68:71], a[92:95], v[40:43]\n"
    "v_mfma_f32_16x16x32_bf16 v[44:47], v[68:71], a[220:223], v[44:47]\n"
    "s_waitcnt lgkmcnt(0)\n"
    "v_mfma_f32_16x16x32_bf16 v[48:51], a[224:227], a[0:3], 0\n"
    "s_waitcnt vmcnt(9)\n"
    "s_barrier\n"
    "v_mfma_f32_16x16x32_bf16 v[52:55], a[224:227], a[96:99], 0\n"
    "ds_read_b128 v[188:191], v7 offset:49152\n"
    "ds_read_b128 v[184:187], v7 offset:50176\n"
    "v_mfma_f32_16x16x32_bf16 v[48:51], a[228:231], a[4:7], v[48:51]\n"
    "buffer_load_dwordx4 v[144:147], v2, s[68:71], s42 offen offset:1024 sc1\n"
    "v_mfma_f32_16x16x32_bf16 v[52:55], a[228:231], a[100:103], v[52:55]\n"
    "ds_read_b128 v[180:183], v7 offset:51200\n"
    "ds_read_b128 v[176:179], v7 offset:52224\n"
    "v_mfma_f32_16x16x32_bf16 v[48:51], a[232:235], a[8:11], v[48:51]\n"
    "v_cvt_pk_bf16_f32 v100, v40, v41\n"
    "v_cvt_pk_bf16_f32 v101, v42, v43\n"
    "v_mfma_f32_16x16x32_bf16 v[52:55], a[232:235], a[104:107], v[52:55]\n"
    "ds_read_b128 v[172:175], v7 offset:53248\n"
    "ds_read_b128 v[168:171], v7 offset:54272\n"
    "v_mfma_f32_16x16x32_bf16 v[48:51], a[236:239], a[12:15], v[48:51]\n"
    "v_cvt_pk_bf16_f32 v102, v44, v45\n"
    "v_cvt_pk_bf16_f32 v103, v46, v47\n"
    "v_mfma_f32_16x16x32_bf16 v[52:55], a[236:239], a[108:111], v[52:55]\n"
    "ds_read_b128 v[164:167], v7 offset:55296\n"
    "ds_read_b128 v[160:163], v7 offset:56320\n"
    "v_mfma_f32_16x16x32_bf16 v[48:51], a[240:243], a[32:35], v[48:51]\n"
    "buffer_store_dwordx4 v[100:103], v2, s[68:71], s11 offen sc1\n"
    "v_mfma_f32_16x16x32_bf16 v[52:55], a[240:243], a[128:131], v[52:55]\n"
    "ds_read_b128 v[156:159], v7 offset:57344\n"
    "ds_read_b128 v[152:155], v7 offset:58368\n"
    "v_mfma_f32_16x16x32_bf16 v[48:51], a[244:247], a[36:39], v[48:51]\n"
    "v_mfma_f32_16x16x32_bf16 v[52:55], a[244:247], a[132:135], v[52:55]\n"
    "ds_read_b128 v[116:119], v7 offset:59392\n"
    "ds_read_b128 v[84:87], v7 offset:60416\n"
    "v_mfma_f32_16x16x32_bf16 v[48:51], a[248:251], a[40:43], v[48:51]\n"
    "buffer_load_dwordx2 v[148:149], v90, s[68:71], s43 offen sc1\n"
    "v_mfma_f32_16x16x32_bf16 v[52:55], a[248:251], a[136:139], v[52:55]\n"
    "ds_read_b128 v[80:83], v7 offset:61440\n"
    "ds_read_b128 v[76:79], v7 offset:62464\n"
    "v_mfma_f32_16x16x32_bf16 v[48:51], a[252:255], a[44:47], v[48:51]\n"
    "s_add_u32 m0, s25, 131072\n"
    "s_add_u32 s5, s5, s10\n"
    "v_mfma_f32_16x16x32_bf16 v[52:55], a[252:255], a[140:143], v[52:55]\n"
    "ds_read_b128 v[72:75], v7 offset:63488\n"
    "ds_read_b128 v[68:71], v7 offset:64512\n"
    "v_mfma_f32_16x16x32_bf16 v[48:51], v[252:255], v[248:251], v[48:51]\n"
    "buffer_load_dwordx4 v2, s[68:71], s5 offen offset:0 sc1 lds\n"
    "v_mfma_f32_16x16x32_bf16 v[52:55], v[252:255], a[160:163], v[52:55]\n"
    "v_mfma_f32_16x16x32_bf16 v[48:51], v[216:219], v[244:247], v[48:51]\n"
    "v_mfma_f32_16x16x32_bf16 v[52:55], v[216:219], a[164:167], v[52:55]\n"
    "v_mfma_f32_16x16x32_bf16 v[48:51], v[212:215], v[240:243], v[48:51]\n"
    "buffer_load_dwordx4 v2, s[68:71], s5 offen offset:1024 sc1 lds\n"
    "v_mfma_f32_16x16x32_bf16 v[52:55], v[212:215], a[168:171], v[52:55]\n"
    "v_mfma_f32_16x16x32_bf16 v[48:51], v[208:211], v[236:239], v[48:51]\n"
    "v_mfma_f32_16x16x32_bf16 v[52:55], v[208:211], a[172:175], v[52:55]\n"
    "v_mfma_f32_16x16x32_bf16 v[48:51], v[204:207], a[64:67], v[48:51]\n"
    "v_mfma_f32_16x16x32_bf16 v[52:55], v[204:207], a[192:195], v[52:55]\n"
    "buffer_load_dwordx4 v2, s[68:71], s5 offen offset:2048 sc1 lds\n"
    "v_mfma_f32_16x16x32_bf16 v[48:51], v[200:203], a[68:71], v[48:51]\n"
    "v_mfma_f32_16x16x32_bf16 v[52:55], v[200:203], a[196:199], v[52:55]\n"
    "v_mfma_f32_16x16x32_bf16 v[48:51], v[196:199], a[72:75], v[48:51]\n"
    "v_mfma_f32_16x16x32_bf16 v[52:55], v[196:199], a[200:203], v[52:55]\n"
    "v_mfma_f32_16x16x32_bf16 v[48:51], v[192:195], a[76:79], v[48:51]\n"
    "buffer_load_dwordx4 v2, s[68:71], s5 offen offset:3072 sc1 lds\n"
    "v_mfma_f32_16x16x32_bf16 v[52:55], v[192:195], a[204:207], v[52:55]\n"
    "s_waitcnt lgkmcnt(0)\n"
    "v_mfma_f32_16x16x32_bf16 v[48:51], v[188:191], a[16:19], v[48:51]\n"
    "s_waitcnt vmcnt(12)\n"
    "s_barrier\n"
    "v_mfma_f32_16x16x32_bf16 v[52:55], v[188:191], a[112:115], v[52:55]\n"
    "ds_read_b128 a[224:227], v7 offset:0\n"
    "ds_read_b128 a[228:231], v7 offset:1024\n"
    "v_mfma_f32_16x16x32_bf16 v[48:51], v[184:187], a[20:23], v[48:51]\n"
    "s_add_u32 m0, s25, 147456\n"
    "s_add_u32 s5, s5, 4096\n"
    "v_mfma_f32_16x16x32_bf16 v[52:55], v[184:187], a[116:119], v[52:55]\n"
    "ds_read_b128 a[232:235], v7 offset:2048\n"
    "ds_read_b128 a[236:239], v7 offset:3072\n"
    "v_mfma_f32_16x16x32_bf16 v[48:51], v[180:183], a[24:27], v[48:51]\n"
    "v_mov_b32 v101, s8\n"
    "v_mov_b32 v103, s8\n"
    "v_mfma_f32_16x16x32_bf16 v[52:55], v[180:183], a[120:123], v[52:55]\n"
    "ds_read_b128 a[240:243], v7 offset:4096\n"
    "ds_read_b128 a[244:247], v7 offset:5120\n"
    "v_mfma_f32_16x16x32_bf16 v[48:51], v[176:179], a[28:31], v[48:51]\n"
    "v_mov_b32 v105, s8\n"
    "v_mov_b32 v107, s8\n"
    "v_mfma_f32_16x16x32_bf16 v[52:55], v[176:179], a[124:127], v[52:55]\n"
    "ds_read_b128 a[248:251], v7 offset:6144\n"
    "ds_read_b128 a[252:255], v7 offset:7168\n"
    "buffer_load_dwordx4 v2, s[68:71], s5 offen offset:0 sc1 lds\n"
    "v_mfma_f32_16x16x32_bf16 v[48:51], v[172:175], a[48:51], v[48:51]\n"
    "v_mfma_f32_16x16x32_bf16 v[52:55], v[172:175], a[144:147], v[52:55]\n"
    "ds_read_b128 v[252:255], v7 offset:8192\n"
    "ds_read_b128 v[216:219], v7 offset:9216\n"
    "v_mfma_f32_16x16x32_bf16 v[48:51], v[168:171], a[52:55], v[48:51]\n"
    "v_mfma_f32_16x16x32_bf16 v[52:55], v[168:171], a[148:151], v[52:55]\n"
    "ds_read_b128 v[212:215], v7 offset:10240\n"
    "ds_read_b128 v[208:211], v7 offset:11264\n"
    "v_mfma_f32_16x16x32_bf16 v[48:51], v[164:167], a[56:59], v[48:51]\n"
    "buffer_load_dwordx4 v2, s[68:71], s5 offen offset:1024 sc1 lds\n"
    "v_mfma_f32_16x16x32_bf16 v[52:55], v[164:167], a[152:155], v[52:55]\n"
    "ds_read_b128 v[204:207], v7 offset:12288\n"
    "ds_read_b128 v[200:203], v7 offset:13312\n"
    "v_mfma_f32_16x16x32_bf16 v[48:51], v[160:163], a[60:63], v[48:51]\n"
    "v_mfma_f32_16x16x32_bf16 v[52:55], v[160:163], a[156:159], v[52:55]\n"
    "ds_read_b128 v[196:199], v7 offset:14336\n"
    "ds_read_b128 v[192:195], v7 offset:15360\n"
    "v_mfma_f32_16x16x32_bf16 v[48:51], v[156:159], v[232:235], v[48:51]\n"
    "v_mfma_f32_16x16x32_bf16 v[52:55], v[156:159], a[176:179], v[52:55]\n"
    "v_mfma_f32_16x16x32_bf16 v[48:51], v[152:155], v[228:231], v[48:51]\n"
    "v_mfma_f32_16x16x32_bf16 v[52:55], v[152:155], a[180:183], v[52:55]\n"
    "v_mfma_f32_16x16x32_bf16 v[48:51], v[116:119], v[224:227], v[48:51]\n"
    "v_mfma_f32_16x16x32_bf16 v[52:55], v[116:119], a[184:187], v[52:55]\n"
    "v_mfma_f32_16x16x32_bf16 v[48:51], v[84:87], v[220:223], v[48:51]\n"
    "v_mfma_f32_16x16x32_bf16 v[52:55], v[84:87], a[188:191], v[52:55]\n"
    "buffer_load_dwordx4 v2, s[68:71], s5 offen offset:2048 sc1 lds\n"
    "v_mfma_f32_16x16x32_bf16 v[48:51], v[80:83], a[80:83], v[48:51]\n"
    "v_mfma_f32_16x16x32_bf16 v[52:55], v[80:83], a[208:211], v[52:55]\n"
    "v_mfma_f32_16x16x32_bf16 v[48:51], v[76:79], a[84:87], v[48:51]\n"
    "v_mfma_f32_16x16x32_bf16 v[52:55], v[76:79], a[212:215], v[52:55]\n"
    "v_mfma_f32_16x16x32_bf16 v[48:51], v[72:75], a[88:91], v[48:51]\n"
    "buffer_load_dwordx4 v2, s[68:71], s5 offen offset:3072 sc1 lds\n"
    "v_mfma_f32_16x16x32_bf16 v[52:55], v[72:75], a[216:219], v[52:55]\n"
    "s_add_u32 m0, s59, 32768\n"
    "s_add_u32 s17, s17, 4096\n"
    "v_mfma_f32_16x16x32_bf16 v[48:51], v[68:71], a[92:95], v[48:51]\n"
    "s_waitcnt vmcnt(9)\n"
    "s_mov_b64 exec, 1\n"
    "buffer_store_dword v5, v9, s[68:71], s16 offen offset:32 sc1\n"
    "s_mov_b64 exec, -1\n"
    "v_mfma_f32_16x16x32_bf16 v[52:55], v[68:71], a[220:223], v[52:55]\n"
    "buffer_load_dwordx4 v2, s[68:71], s17 offen offset:0 sc1 lds\n"
    "s_waitcnt lgkmcnt(0)\n"
    "v_mfma_f32_16x16x32_bf16 v[40:43], a[224:227], a[0:3], 0\n"
    "s_barrier\n"
    "v_mfma_f32_16x16x32_bf16 v[44:47], a[224:227], a[96:99], 0\n"
    "ds_read_b128 v[188:191], v7 offset:16384\n"
    "ds_read_b128 v[184:187], v7 offset:17408\n"
    "v_mfma_f32_16x16x32_bf16 v[40:43], a[228:231], a[4:7], v[40:43]\n"
    "buffer_load_dwordx4 v2, s[68:71], s17 offen offset:1024 sc1 lds\n"
    "v_mfma_f32_16x16x32_bf16 v[44:47], a[228:231], a[100:103], v[44:47]\n"
    "ds_read_b128 v[180:183], v7 offset:18432\n"
    "ds_read_b128 v[176:179], v7 offset:19456\n"
    "v_mfma_f32_16x16x32_bf16 v[40:43], a[232:235], a[8:11], v[40:43]\n"
    "v_cvt_pk_bf16_f32 v100, v48, v49\n"
    "v_cvt_pk_bf16_f32 v102, v50, v51\n"
    "v_mfma_f32_16x16x32_bf16 v[44:47], a[232:235], a[104:107], v[44:47]\n"
    "ds_read_b128 v[172:175], v7 offset:20480\n"
    "ds_read_b128 v[168:171], v7 offset:21504\n"
    "v_mfma_f32_16x16x32_bf16 v[40:43], a[236:239], a[12:15], v[40:43]\n"
    "v_cvt_pk_bf16_f32 v104, v52, v53\n"
    "v_cvt_pk_bf16_f32 v106, v54, v55\n"
    "v_mfma_f32_16x16x32_bf16 v[44:47], a[236:239], a[108:111], v[44:47]\n"
    "ds_read_b128 v[164:167], v7 offset:22528\n"
    "ds_read_b128 v[160:163], v7 offset:23552\n"
    "v_mfma_f32_16x16x32_bf16 v[40:43], a[240:243], a[32:35], v[40:43]\n"
    "buffer_store_dwordx4 v[100:103], v2, s[68:71], s11 offen offset:2048 sc1\n"
    "v_mfma_f32_16x16x32_bf16 v[44:47], a[240:243], a[128:131], v[44:47]\n"
    "ds_read_b128 v[156:159], v7 offset:24576\n"
    "ds_read_b128 v[152:155], v7 offset:25600\n"
    "v_mfma_f32_16x16x32_bf16 v[40:43], a[244:247], a[36:39], v[40:43]\n"
    "buffer_store_dwordx4 v[104:107], v2, s[68:71], s11 offen offset:3072 sc1\n"
    "v_mfma_f32_16x16x32_bf16 v[44:47], a[244:247], a[132:135], v[44:47]\n"
    "ds_read_b128 v[116:119], v7 offset:26624\n"
    "ds_read_b128 v[84:87], v7 offset:27648\n"
    "v_mfma_f32_16x16x32_bf16 v[40:43], a[248:251], a[40:43], v[40:43]\n"
    "s_add_u32 m0, s25, 98304\n"
    "s_add_u32 s5, s5, 61440\n"
    "v_mfma_f32_16x16x32_bf16 v[44:47], a[248:251], a[136:139], v[44:47]\n"
    "ds_read_b128 v[80:83], v7 offset:28672\n"
    "ds_read_b128 v[76:79], v7 offset:29696\n"
    "v_mfma_f32_16x16x32_bf16 v[40:43], a[252:255], a[44:47], v[40:43]\n"
    "v_mfma_f32_16x16x32_bf16 v[44:47], a[252:255], a[140:143], v[44:47]\n"
    "ds_read_b128 v[72:75], v7 offset:30720\n"
    "ds_read_b128 v[68:71], v7 offset:31744\n"
    "v_mfma_f32_16x16x32_bf16 v[40:43], v[252:255], v[248:251], v[40:43]\n"
    "buffer_load_dwordx4 v2, s[68:71], s5 offen offset:0 sc1 lds\n"
    "v_mfma_f32_16x16x32_bf16 v[44:47], v[252:255], a[160:163], v[44:47]\n"
    "v_mfma_f32_16x16x32_bf16 v[40:43], v[216:219], v[244:247], v[40:43]\n"
    "v_mfma_f32_16x16x32_bf16 v[44:47], v[216:219], a[164:167], v[44:47]\n"
    "v_mfma_f32_16x16x32_bf16 v[40:43], v[212:215], v[240:243], v[40:43]\n"
    "buffer_load_dwordx4 v2, s[68:71], s5 offen offset:1024 sc1 lds\n"
    "v_mfma_f32_16x16x32_bf16 v[44:47], v[212:215], a[168:171], v[44:47]\n"
    "v_mfma_f32_16x16x32_bf16 v[40:43], v[208:211], v[236:239], v[40:43]\n"
    "v_mfma_f32_16x16x32_bf16 v[44:47], v[208:211], a[172:175], v[44:47]\n"
    "v_mfma_f32_16x16x32_bf16 v[40:43], v[204:207], a[64:67], v[40:43]\n"
    "v_mfma_f32_16x16x32_bf16 v[44:47], v[204:207], a[192:195], v[44:47]\n"
    "v_mfma_f32_16x16x32_bf16 v[40:43], v[200:203], a[68:71], v[40:43]\n"
    "buffer_load_dwordx4 v2, s[68:71], s5 offen offset:2048 sc1 lds\n"
    "v_mfma_f32_16x16x32_bf16 v[44:47], v[200:203], a[196:199], v[44:47]\n"
    "v_mfma_f32_16x16x32_bf16 v[40:43], v[196:199], a[72:75], v[40:43]\n"
    "v_mfma_f32_16x16x32_bf16 v[44:47], v[196:199], a[200:203], v[44:47]\n"
    "v_mfma_f32_16x16x32_bf16 v[40:43], v[192:195], a[76:79], v[40:43]\n"
    "v_mfma_f32_16x16x32_bf16 v[44:47], v[192:195], a[204:207], v[44:47]\n"
    "buffer_load_dwordx4 v2, s[68:71], s5 offen offset:3072 sc1 lds\n"
    "s_waitcnt lgkmcnt(0)\n"
    "v_mfma_f32_16x16x32_bf16 v[40:43], v[188:191], a[16:19], v[40:43]\n"
    "s_waitcnt vmcnt(13)\n"
    "s_barrier\n"
    "v_mfma_f32_16x16x32_bf16 v[44:47], v[188:191], a[112:115], v[44:47]\n"
    "ds_read_b128 a[224:227], v7 offset:32768\n"
    "ds_read_b128 a[228:231], v7 offset:33792\n"
    "v_mfma_f32_16x16x32_bf16 v[40:43], v[184:187], a[20:23], v[40:43]\n"
    "s_add_u32 m0, s25, 114688\n"
    "s_add_u32 s5, s5, 4096\n"
    "v_mfma_f32_16x16x32_bf16 v[44:47], v[184:187], a[116:119], v[44:47]\n"
    "ds_read_b128 a[232:235], v7 offset:34816\n"
    "ds_read_b128 a[236:239], v7 offset:35840\n"
    "v_mfma_f32_16x16x32_bf16 v[40:43], v[180:183], a[24:27], v[40:43]\n"
    "v_lshlrev_b32 v95, 16, v92\n"
    "v_lshlrev_b32 v94, 16, v92\n"
    "v_mfma_f32_16x16x32_bf16 v[44:47], v[180:183], a[120:123], v[44:47]\n"
    "ds_read_b128 a[240:243], v7 offset:36864\n"
    "ds_read_b128 a[244:247], v7 offset:37888\n"
    "v_mfma_f32_16x16x32_bf16 v[40:43], v[176:179], a[28:31], v[40:43]\n"
    "v_lshlrev_b32 v93, 16, v92\n"
    "v_lshlrev_b32 v92, 16, v92\n"
    "v_mfma_f32_16x16x32_bf16 v[44:47], v[176:179], a[124:127], v[44:47]\n"
    "ds_read_b128 a[248:251], v7 offset:38912\n"
    "ds_read_b128 a[252:255], v7 offset:39936\n"
    "v_mfma_f32_16x16x32_bf16 v[40:43], v[172:175], a[48:51], v[40:43]\n"
    "v_lshlrev_b32 v99, 16, v96\n"
    "v_lshlrev_b32 v98, 16, v96\n"
    "v_mfma_f32_16x16x32_bf16 v[44:47], v[172:175], a[144:147], v[44:47]\n"
    "ds_read_b128 v[252:255], v7 offset:40960\n"
    "ds_read_b128 v[216:219], v7 offset:41984\n"
    "v_mfma_f32_16x16x32_bf16 v[40:43], v[168:171], a[52:55], v[40:43]\n"
    "v_lshlrev_b32 v97, 16, v96\n"
    "v_lshlrev_b32 v96, 16, v96\n"
    "v_mfma_f32_16x16x32_bf16 v[44:47], v[168:171], a[148:151], v[44:47]\n"
    "ds_read_b128 v[212:215], v7 offset:43008\n"
    "ds_read_b128 v[208:211], v7 offset:44032\n"
    "v_mfma_f32_16x16x32_bf16 v[40:43], v[164:167], a[56:59], v[40:43]\n"
    "PAD7_%=:\n"
    "s_cbranch_execz PADRET1_%=\n"
    "v_mfma_f32_16x16x32_bf16 v[44:47], v[164:167], a[152:155], v[44:47]\n"
    "ds_read_b128 v[204:207], v7 offset:45056\n"
    "ds_read_b128 v[200:203], v7 offset:46080\n"
    "v_mfma_f32_16x16x32_bf16 v[40:43], v[160:163], a[60:63], v[40:43]\n"
    "v_mfma_f32_16x16x32_bf16 v[44:47], v[160:163], a[156:159], v[44:47]\n"
    "ds_read_b128 v[196:199], v7 offset:47104\n"
    "ds_read_b128 v[192:195], v7 offset:48128\n"
    "v_mfma_f32_16x16x32_bf16 v[40:43], v[156:159], v[232:235], v[40:43]\n"
    "buffer_load_dwordx4 v2, s[68:71], s5 offen offset:0 sc1 lds\n"
    "v_mfma_f32_16x16x32_bf16 v[44:47], v[156:159], a[176:179], v[44:47]\n"
    "v_mfma_f32_16x16x32_bf16 v[40:43], v[152:155], v[228:231], v[40:43]\n"
    "v_mfma_f32_16x16x32_bf16 v[44:47], v[152:155], a[180:183], v[44:47]\n"
    "v_mfma_f32_16x16x32_bf16 v[40:43], v[116:119], v[224:227], v[40:43]\n"
    "buffer_load_dwordx4 v2, s[68:71], s5 offen offset:1024 sc1 lds\n"
    "v_mfma_f32_16x16x32_bf16 v[44:47], v[116:119], a[184:187], v[44:47]\n"
    "v_mfma_f32_16x16x32_bf16 v[40:43], v[84:87], v[220:223], v[40:43]\n"
    "v_mfma_f32_16x16x32_bf16 v[44:47], v[84:87], a[188:191], v[44:47]\n"
    "v_mfma_f32_16x16x32_bf16 v[40:43], v[80:83], a[80:83], v[40:43]\n"
    "v_mfma_f32_16x16x32_bf16 v[44:47], v[80:83], a[208:211], v[44:47]\n"
    "v_mfma_f32_16x16x32_bf16 v[40:43], v[76:79], a[84:87], v[40:43]\n"
    "v_mfma_f32_16x16x32_bf16 v[44:47], v[76:79], a[212:215], v[44:47]\n"
    "buffer_load_dwordx4 v2, s[68:71], s5 offen offset:2048 sc1 lds\n"
    "v_mfma_f32_16x16x32_bf16 v[40:43], v[72:75], a[88:91], v[40:43]\n"
    "v_mfma_f32_16x16x32_bf16 v[44:47], v[72:75], a[216:219], v[44:47]\n"
    "v_mfma_f32_16x16x32_bf16 v[40:43], v[68:71], a[92:95], v[40:43]\n"
    "v_mfma_f32_16x16x32_bf16 v[44:47], v[68:71], a[220:223], v[44:47]\n"
    "s_waitcnt lgkmcnt(0)\n"
    "v_mfma_f32_16x16x32_bf16 v[48:51], a[224:227], a[0:3], v[92:95]\n"
    "s_waitcnt vmcnt(12)\n"
    "s_barrier\n"
    "v_mfma_f32_16x16x32_bf16 v[52:55], a[224:227], a[96:99], v[96:99]\n"
    "ds_read_b128 v[188:191], v7 offset:49152\n"
    "ds_read_b128 v[184:187], v7 offset:50176\n"
    "v_mfma_f32_16x16x32_bf16 v[48:51], a[228:231], a[4:7], v[48:51]\n"
    "s_mov_b64 exec, 0x1\n"
    "buffer_load_dword v28, v9, s[68:71], s29 offen offset:32 sc1\n"
    "s_mov_b64 exec, -1\n"
    "v_mfma_f32_16x16x32_bf16 v[52:55], a[228:231], a[100:103], v[52:55]\n"
    "ds_read_b128 v[180:183], v7 offset:51200\n"
    "ds_read_b128 v[176:179], v7 offset:52224\n"
    "v_mfma_f32_16x16x32_bf16 v[48:51], a[232:235], a[8:11], v[48:51]\n"
    "v_cvt_pk_bf16_f32 v100, v40, v41\n"
    "v_cvt_pk_bf16_f32 v102, v42, v43\n"
    "v_mfma_f32_16x16x32_bf16 v[52:55], a[232:235], a[104:107], v[52:55]\n"
    "ds_read_b128 v[172:175], v7 offset:53248\n"
    "ds_read_b128 v[168:171], v7 offset:54272\n"
    "v_mfma_f32_16x16x32_bf16 v[48:51], a[236:239], a[12:15], v[48:51]\n"
    "v_cvt_pk_bf16_f32 v104, v44, v45\n"
    "v_cvt_pk_bf16_f32 v106, v46, v47\n"
    "buffer_load_dwordx4 v2, s[68:71], s5 offen offset:3072 sc1 lds\n"
    "v_mfma_f32_16x16x32_bf16 v[52:55], a[236:239], a[108:111], v[52:55]\n"
    "ds_read_b128 v[164:167], v7 offset:55296\n"
    "ds_read_b128 v[160:163], v7 offset:56320\n"
    "v_mfma_f32_16x16x32_bf16 v[48:51], a[240:243], a[32:35], v[48:51]\n"
    "buffer_store_dwordx4 v[100:103], v10, s[68:71], s11 offen offset:0 sc1\n"
    "v_mfma_f32_16x16x32_bf16 v[52:55], a[240:243], a[128:131], v[52:55]\n"
    "ds_read_b128 v[156:159], v7 offset:57344\n"
    "ds_read_b128 v[152:155], v7 offset:58368\n"
    "v_mfma_f32_16x16x32_bf16 v[48:51], a[244:247], a[36:39], v[48:51]\n"
    "buffer_store_dwordx4 v[104:107], v10, s[68:71], s11 offen offset:1024 sc1\n"
    "v_mfma_f32_16x16x32_bf16 v[52:55], a[244:247], a[132:135], v[52:55]\n"
    "ds_read_b128 v[116:119], v7 offset:59392\n"
    "ds_read_b128 v[84:87], v7 offset:60416\n"
    "v_mfma_f32_16x16x32_bf16 v[48:51], a[248:251], a[40:43], v[48:51]\n"
    "s_add_u32 m0, s59, 32768\n"
    "v_mfma_f32_16x16x32_bf16 v[52:55], a[248:251], a[136:139], v[52:55]\n"
    "ds_read_b128 v[80:83], v7 offset:61440\n"
    "ds_read_b128 v[76:79], v7 offset:62464\n"
    "v_mfma_f32_16x16x32_bf16 v[48:51], a[252:255], a[44:47], v[48:51]\n"
    "v_mfma_f32_16x16x32_bf16 v[52:55], a[252:255], a[140:143], v[52:55]\n"
    "ds_read_b128 v[72:75], v7 offset:63488\n"
    "ds_read_b128 v[68:71], v7 offset:64512\n"
    "v_mfma_f32_16x16x32_bf16 v[48:51], v[252:255], v[248:251], v[48:51]\n"
    "buffer_load_dwordx4 v2, s[68:71], s17 offen offset:2048 sc1 lds\n"
    "v_mfma_f32_16x16x32_bf16 v[52:55], v[252:255], a[160:163], v[52:55]\n"
    "v_mfma_f32_16x16x32_bf16 v[48:51], v[216:219], v[244:247], v[48:51]\n"
    "v_mfma_f32_16x16x32_bf16 v[52:55], v[216:219], a[164:167], v[52:55]\n"
    "v_mfma_f32_16x16x32_bf16 v[48:51], v[212:215], v[240:243], v[48:51]\n"
    "buffer_load_dwordx4 v2, s[68:71], s17 offen offset:3072 sc1 lds\n"
    "v_mfma_f32_16x16x32_bf16 v[52:55], v[212:215], a[168:171], v[52:55]\n"
    "s_add_u32 m0, s25, 131072\n"
    "s_add_u32 s5, s5, 61440\n"
    "v_mfma_f32_16x16x32_bf16 v[48:51], v[208:211], v[236:239], v[48:51]\n"
    "v_mfma_f32_16x16x32_bf16 v[52:55], v[208:211], a[172:175], v[52:55]\n"
    "v_mfma_f32_16x16x32_bf16 v[48:51], v[204:207], a[64:67], v[48:51]\n"
    "v_mfma_f32_16x16x32_bf16 v[52:55], v[204:207], a[192:195], v[52:55]\n"
    "v_mfma_f32_16x16x32_bf16 v[48:51], v[200:203], a[68:71], v[48:51]\n"
    "buffer_load_dwordx4 v2, s[68:71], s5 offen offset:0 sc1 lds\n"
    "v_mfma_f32_16x16x32_bf16 v[52:55], v[200:203], a[196:199], v[52:55]\n"
    "v_mfma_f32_16x16x32_bf16 v[48:51], v[196:199], a[72:75], v[48:51]\n"
    "v_mfma_f32_16x16x32_bf16 v[52:55], v[196:199], a[200:203], v[52:55]\n"
    "v_mfma_f32_16x16x32_bf16 v[48:51], v[192:195], a[76:79], v[48:51]\n"
    "v_mfma_f32_16x16x32_bf16 v[52:55], v[192:195], a[204:207], v[52:55]\n"
    "buffer_load_dwordx4 v2, s[68:71], s5 offen offset:1024 sc1 lds\n"
    "s_waitcnt lgkmcnt(0)\n"
    "v_mfma_f32_16x16x32_bf16 v[48:51], v[188:191], a[16:19], v[48:51]\n"
    "s_waitcnt vmcnt(11)\n"
    "s_barrier\n"
    "v_mfma_f32_16x16x32_bf16 v[52:55], v[188:191], a[112:115], v[52:55]\n"
    "ds_read_b128 a[224:227], v7 offset:0\n"
    "ds_read_b128 a[228:231], v7 offset:1024\n"
    "v_mfma_f32_16x16x32_bf16 v[48:51], v[184:187], a[20:23], v[48:51]\n"
    "v_mfma_f32_16x16x32_bf16 v[52:55], v[184:187], a[116:119], v[52:55]\n"
    "ds_read_b128 a[232:235], v7 offset:2048\n"
    "ds_read_b128 a[236:239], v7 offset:3072\n"
    "v_mfma_f32_16x16x32_bf16 v[48:51], v[180:183], a[24:27], v[48:51]\n"
    "buffer_load_dwordx4 v2, s[68:71], s5 offen offset:2048 sc1 lds\n"
    "v_mfma_f32_16x16x32_bf16 v[52:55], v[180:183], a[120:123], v[52:55]\n"
    "ds_read_b128 a[240:243], v7 offset:4096\n"
    "ds_read_b128 a[244:247], v7 offset:5120\n"
    "v_mfma_f32_16x16x32_bf16 v[48:51], v[176:179], a[28:31], v[48:51]\n"
    "v_mfma_f32_16x16x32_bf16 v[52:55], v[176:179], a[124:127], v[52:55]\n"
    "ds_read_b128 a[248:251], v7 offset:6144\n"
    "ds_read_b128 a[252:255], v7 offset:7168\n"
    "v_mfma_f32_16x16x32_bf16 v[48:51], v[172:175], a[48:51], v[48:51]\n"
    "v_mfma_f32_16x16x32_bf16 v[52:55], v[172:175], a[144:147], v[52:55]\n"
    "ds_read_b128 v[252:255], v7 offset:8192\n"
    "ds_read_b128 v[216:219], v7 offset:9216\n"
    "v_mfma_f32_16x16x32_bf16 v[48:51], v[168:171], a[52:55], v[48:51]\n"
    "v_mfma_f32_16x16x32_bf16 v[52:55], v[168:171], a[148:151], v[52:55]\n"
    "ds_read_b128 v[212:215], v7 offset:10240\n"
    "ds_read_b128 v[208:211], v7 offset:11264\n"
    "v_mfma_f32_16x16x32_bf16 v[48:51], v[164:167], a[56:59], v[48:51]\n"
    "buffer_load_dwordx4 v2, s[68:71], s5 offen offset:3072 sc1 lds\n"
    "v_mfma_f32_16x16x32_bf16 v[52:55], v[164:167], a[152:155], v[52:55]\n"
    "ds_read_b128 v[204:207], v7 offset:12288\n"
    "ds_read_b128 v[200:203], v7 offset:13312\n"
    "v_mfma_f32_16x16x32_bf16 v[48:51], v[160:163], a[60:63], v[48:51]\n"
    "s_add_u32 m0, s25, 147456\n"
    "s_add_u32 s5, s5, 4096\n"
    "v_mfma_f32_16x16x32_bf16 v[52:55], v[160:163], a[156:159], v[52:55]\n"
    "ds_read_b128 v[196:199], v7 offset:14336\n"
    "ds_read_b128 v[192:195], v7 offset:15360\n"
    "v_mfma_f32_16x16x32_bf16 v[48:51], v[156:159], v[232:235], v[48:51]\n"
    "v_mfma_f32_16x16x32_bf16 v[52:55], v[156:159], a[176:179], v[52:55]\n"
    "v_mfma_f32_16x16x32_bf16 v[48:51], v[152:155], v[228:231], v[48:51]\n"
    "s_waitcnt vmcnt(9)\n"
    "v_cmp_ne_u32 vcc, s8, v28\n"
    "s_nop 0\n"
    "s_and_b64 vcc, vcc, 0x1\n"
    "s_cbranch_vccnz R25_%=\n"
    "B26_%=:\n"
    "v_mfma_f32_16x16x32_bf16 v[52:55], v[152:155], a[180:183], v[52:55]\n"
    "buffer_load_dwordx4 v[24:27], v2, s[68:71], s28 offen sc1\n"
    "v_mfma_f32_16x16x32_bf16 v[48:51], v[116:119], v[224:227], v[48:51]\n"
    "v_mfma_f32_16x16x32_bf16 v[52:55], v[116:119], a[184:187], v[52:55]\n"
    "v_mfma_f32_16x16x32_bf16 v[48:51], v[84:87], v[220:223], v[48:51]\n"
    "v_mfma_f32_16x16x32_bf16 v[52:55], v[84:87], a[188:191], v[52:55]\n"
    "v_mfma_f32_16x16x32_bf16 v[48:51], v[80:83], a[80:83], v[48:51]\n"
    "v_mfma_f32_16x16x32_bf16 v[52:55], v[80:83], a[208:211], v[52:55]\n"
    "v_mfma_f32_16x16x32_bf16 v[48:51], v[76:79], a[84:87], v[48:51]\n"
    "v_mfma_f32_16x16x32_bf16 v[52:55], v[76:79], a[212:215], v[52:55]\n"
    "buffer_load_dwordx4 v2, s[68:71], s5 offen offset:0 sc1 lds\n"
    "v_mfma_f32_16x16x32_bf16 v[48:51], v[72:75], a[88:91], v[48:51]\n"
    "v_mfma_f32_16x16x32_bf16 v[52:55], v[72:75], a[216:219], v[52:55]\n"
    "v_mfma_f32_16x16x32_bf16 v[48:51], v[68:71], a[92:95], v[48:51]\n"
    "v_mfma_f32_16x16x32_bf16 v[52:55], v[68:71], a[220:223], v[52:55]\n"
    "s_waitcnt lgkmcnt(0)\n"
    "v_mfma_f32_16x16x32_bf16 v[40:43], a[224:227], a[0:3], v[92:95]\n"
    "s_waitcnt vmcnt(10)\n"
    "s_barrier\n"
    "v_mfma_f32_16x16x32_bf16 v[44:47], a[224:227], a[96:99], v[96:99]\n"
    "ds_read_b128 v[188:191], v7 offset:16384\n"
    "ds_read_b128 v[184:187], v7 offset:17408\n"
    "v_mfma_f32_16x16x32_bf16 v[40:43], a[228:231], a[4:7], v[40:43]\n"
    "buffer_load_dwordx4 v[132:135], v2, s[68:71], s28 offen offset:2048 sc1\n"
    "v_mfma_f32_16x16x32_bf16 v[44:47], a[228:231], a[100:103], v[44:47]\n"
    "ds_read_b128 v[180:183], v7 offset:18432\n"
    "ds_read_b128 v[176:179], v7 offset:19456\n"
    "v_mfma_f32_16x16x32_bf16 v[40:43], a[232:235], a[8:11], v[40:43]\n"
    "v_mfma_f32_16x16x32_bf16 v[44:47], a[232:235], a[104:107], v[44:47]\n"
    "ds_read_b128 v[172:175], v7 offset:20480\n"
    "ds_read_b128 v[168:171], v7 offset:21504\n"
    "v_mfma_f32_16x16x32_bf16 v[40:43], a[236:239], a[12:15], v[40:43]\n"
    "buffer_load_dwordx4 v[136:139], v2, s[68:71], s28 offen offset:3072 sc1\n"
    "v_mfma_f32_16x16x32_bf16 v[44:47], a[236:239], a[108:111], v[44:47]\n"
    "ds_read_b128 v[164:167], v7 offset:22528\n"
    "ds_read_b128 v[160:163], v7 offset:23552\n"
    "v_mfma_f32_16x16x32_bf16 v[40:43], a[240:243], a[32:35], v[40:43]\n"
    "v_mfma_f32_16x16x32_bf16 v[44:47], a[240:243], a[128:131], v[44:47]\n"
    "ds_read_b128 v[156:159], v7 offset:24576\n"
    "ds_read_b128 v[152:155], v7 offset:25600\n"
    "v_mfma_f32_16x16x32_bf16 v[40:43], a[244:247], a[36:39], v[40:43]\n"
    "buffer_load_dwordx4 v2, s[68:71], s5 offen offset:1024 sc1 lds\n"
    "v_mfma_f32_16x16x32_bf16 v[44:47], a[244:247], a[132:135], v[44:47]\n"
    "ds_read_b128 v[116:119], v7 offset:26624\n"
    "ds_read_b128 v[84:87], v7 offset:27648\n"
    "v_mfma_f32_16x16x32_bf16 v[40:43], a[248:251], a[40:43], v[40:43]\n"
    "v_mfma_f32_16x16x32_bf16 v[44:47], a[248:251], a[136:139], v[44:47]\n"
    "ds_read_b128 v[80:83], v7 offset:28672\n"
    "ds_read_b128 v[76:79], v7 offset:29696\n"
    "v_mfma_f32_16x16x32_bf16 v[40:43], a[252:255], a[44:47], v[40:43]\n"
    "v_mfma_f32_16x16x32_bf16 v[44:47], a[252:255], a[140:143], v[44:47]\n"
    "ds_read_b128 v[72:75], v7 offset:30720\n"
    "ds_read_b128 v[68:71], v7 offset:31744\n"
    "v_mfma_f32_16x16x32_bf16 v[40:43], v[252:255], v[248:251], v[40:43]\n"
    "buffer_load_dwordx4 v2, s[68:71], s5 offen offset:2048 sc1 lds\n"
    "v_mfma_f32_16x16x32_bf16 v[44:47], v[252:255], a[160:163], v[44:47]\n"
    "v_mfma_f32_16x16x32_bf16 v[40:43], v[216:219], v[244:247], v[40:43]\n"
    "v_mfma_f32_16x16x32_bf16 v[44:47], v[216:219], a[164:167], v[44:47]\n"
    "v_mfma_f32_16x16x32_bf16 v[40:43], v[212:215], v[240:243], v[40:43]\n"
    "buffer_load_dwordx4 v2, s[68:71], s5 offen offset:3072 sc1 lds\n"
    "v_mfma_f32_16x16x32_bf16 v[44:47], v[212:215], a[168:171], v[44:47]\n"
    "s_add_u32 m0, s59, 36864\n"
    "s_add_u32 s17, s17, 4096\n"
    "v_mfma_f32_16x16x32_bf16 v[40:43], v[208:211], v[236:239], v[40:43]\n"
    "v_mfma_f32_16x16x32_bf16 v[44:47], v[208:211], a[172:175], v[44:47]\n"
    "v_mfma_f32_16x16x32_bf16 v[40:43], v[204:207], a[64:67], v[40:43]\n"
    "v_mfma_f32_16x16x32_bf16 v[44:47], v[204:207], a[192:195], v[44:47]\n"
    "buffer_load_dwordx4 v2, s[68:71], s17 offen offset:0 sc1 lds\n"
    "v_mfma_f32_16x16x32_bf16 v[40:43], v[200:203], a[68:71], v[40:43]\n"
    "v_mfma_f32_16x16x32_bf16 v[44:47], v[200:203], a[196:199], v[44:47]\n"
    "v_mfma_f32_16x16x32_bf16 v[40:43], v[196:199], a[72:75], v[40:43]\n"
    "v_mfma_f32_16x16x32_bf16 v[44:47], v[196:199], a[200:203], v[44:47]\n"
    "v_mfma_f32_16x16x32_bf16 v[40:43], v[192:195], a[76:79], v[40:43]\n"
    "v_mfma_f32_16x16x32_bf16 v[44:47], v[192:195], a[204:207], v[44:47]\n"
    "s_waitcnt lgkmcnt(0)\n"
    "v_mfma_f32_16x16x32_bf16 v[40:43], v[188:191], a[16:19], v[40:43]\n"
    "s_waitcnt vmcnt(8)\n"
    "s_barrier\n"
    "v_mfma_f32_16x16x32_bf16 v[44:47], v[188:191], a[112:115], v[44:47]\n"
    "ds_read_b128 a[224:227], v7 offset:32768\n"
    "ds_read_b128 a[228:231], v7 offset:33792\n"
    "v_mfma_f32_16x16x32_bf16 v[40:43], v[184:187], a[20:23], v[40:43]\n"
    "s_waitcnt vmcnt(7)\n"
    "v_and_b32 v29, 0xffff0000, v24\n"
    "v_add_f32 v49, v49, v29\n"
    "v_mfma_f32_16x16x32_bf16 v[44:47], v[184:187], a[116:119], v[44:47]\n"
    "ds_read_b128 a[232:235], v7 offset:34816\n"
    "ds_read_b128 a[236:239], v7 offset:35840\n"
    "v_mfma_f32_16x16x32_bf16 v[40:43], v[180:183], a[24:27], v[40:43]\n"
    "v_lshlrev_b32 v29, 16, v24\n"
    "v_add_f32 v48, v48, v29\n"
    "v_mfma_f32_16x16x32_bf16 v[44:47], v[180:183], a[120:123], v[44:47]\n"
    "ds_read_b128 a[240:243], v7 offset:36864\n"
    "ds_read_b128 a[244:247], v7 offset:37888\n"
    "buffer_load_dwordx4 v2, s[68:71], s17 offen offset:1024 sc1 lds\n"
    "v_mfma_f32_16x16x32_bf16 v[40:43], v[176:179], a[28:31], v[40:43]\n"
    "v_and_b32 v29, 0xffff0000, v25\n"
    "v_add_f32 v51, v51, v29\n"
    "v_mfma_f32_16x16x32_bf16 v[44:47], v[176:179], a[124:127], v[44:47]\n"
    "ds_read_b128 a[248:251], v7 offset:38912\n"
    "ds_read_b128 a[252:255], v7 offset:39936\n"
    "v_mfma_f32_16x16x32_bf16 v[40:43], v[172:175], a[48:51], v[40:43]\n"
    "v_lshlrev_b32 v29, 16, v25\n"
    "v_add_f32 v50, v50, v29\n"
    "v_mfma_f32_16x16x32_bf16 v[44:47], v[172:175], a[144:147], v[44:47]\n"
    "ds_read_b128 v[252:255], v7 offset:40960\n"
    "ds_read_b128 v[216:219], v7 offset:41984\n"
    "v_mfma_f32_16x16x32_bf16 v[40:43], v[168:171], a[52:55], v[40:43]\n"
    "v_and_b32 v29, 0xffff0000, v26\n"
    "v_add_f32 v53, v53, v29\n"
    "v_mfma_f32_16x16x32_bf16 v[44:47], v[168:171], a[148:151], v[44:47]\n"
    "ds_read_b128 v[212:215], v7 offset:43008\n"
    "ds_read_b128 v[208:211], v7 offset:44032\n"
    "v_mfma_f32_16x16x32_bf16 v[40:43], v[164:167], a[56:59], v[40:43]\n"
    "v_lshlrev_b32 v29, 16, v26\n"
    "v_add_f32 v52, v52, v29\n"
    "v_mfma_f32_16x16x32_bf16 v[44:47], v[164:167], a[152:155], v[44:47]\n"
    "ds_read_b128 v[204:207], v7 offset:45056\n"
    "ds_read_b128 v[200:203], v7 offset:46080\n"
    "v_mfma_f32_16x16x32_bf16 v[40:43], v[160:163], a[60:63], v[40:43]\n"
    "v_and_b32 v29, 0xffff0000, v27\n"
    "v_add_f32 v55, v55, v29\n"
    "buffer_load_dwordx4 v2, s[68:71], s17 offen offset:2048 sc1 lds\n"
    "v_mfma_f32_16x16x32_bf16 v[44:47], v[160:163], a[156:159], v[44:47]\n"
    "ds_read_b128 v[196:199], v7 offset:47104\n"
    "ds_read_b128 v[192:195], v7 offset:48128\n"
    "v_mfma_f32_16x16x32_bf16 v[40:43], v[156:159], v[232:235], v[40:43]\n"
    "v_lshlrev_b32 v29, 16, v27\n"
    "v_add_f32 v54, v54, v29\n"
    "v_mfma_f32_16x16x32_bf16 v[44:47], v[156:159], a[176:179], v[44:47]\n"
    "v_mul_f32 v48, v48, v20\n"
    "v_mul_f32 v49, v49, v21\n"
    "v_mfma_f32_16x16x32_bf16 v[40:43], v[152:155], v[228:231], v[40:43]\n"
    "v_mul_f32 v50, v50, v22\n"
    "v_mul_f32 v51, v51, v23\n"
    "v_mfma_f32_16x16x32_bf16 v[44:47], v[152:155], a[180:183], v[44:47]\n"
    "v_mul_f32 v52, v52, v120\n"
    "v_mul_f32 v53, v53, v121\n"
    "v_mfma_f32_16x16x32_bf16 v[40:43], v[116:119], v[224:227], v[40:43]\n"
    "v_mul_f32 v54, v54, v122\n"
    "v_mul_f32 v55, v55, v123\n"
    "buffer_load_dwordx4 v2, s[68:71], s17 offen offset:3072 sc1 lds\n"
    "v_mfma_f32_16x16x32_bf16 v[44:47], v[116:119], a[184:187], v[44:47]\n"
    "v_cvt_pk_bf16_f32 v100, v48, v49\n"
    "v_cvt_pk_bf16_f32 v101, v50, v51\n"
    "v_mfma_f32_16x16x32_bf16 v[40:43], v[84:87], v[220:223], v[40:43]\n"
    "v_cvt_pk_bf16_f32 v104, v52, v53\n"
    "v_cvt_pk_bf16_f32 v105, v54, v55\n"
    "v_mfma_f32_16x16x32_bf16 v[44:47], v[84:87], a[188:191], v[44:47]\n"
    "buffer_load_dwordx4 v[56:59], v10, s[68:71], s28 offen offset:0 sc1\n"
    "v_mfma_f32_16x16x32_bf16 v[40:43], v[80:83], a[80:83], v[40:43]\n"
    "s_add_u32 m0, s59, 65536\n"
    "s_add_u32 s17, s17, 4096\n"
    "v_mfma_f32_16x16x32_bf16 v[44:47], v[80:83], a[208:211], v[44:47]\n"
    "v_mfma_f32_16x16x32_bf16 v[40:43], v[76:79], a[84:87], v[40:43]\n"
    "buffer_load_dwordx4 v[60:63], v10, s[68:71], s28 offen offset:1024 sc1\n"
    "v_mfma_f32_16x16x32_bf16 v[44:47], v[76:79], a[212:215], v[44:47]\n"
    "v_mfma_f32_16x16x32_bf16 v[40:43], v[72:75], a[88:91], v[40:43]\n"
    "v_mfma_f32_16x16x32_bf16 v[44:47], v[72:75], a[216:219], v[44:47]\n"
    "v_mfma_f32_16x16x32_bf16 v[40:43], v[68:71], a[92:95], v[40:43]\n"
    "v_mfma_f32_16x16x32_bf16 v[44:47], v[68:71], a[220:223], v[44:47]\n"
    "buffer_load_dwordx4 v2, s[68:71], s17 offen offset:0 sc1 lds\n"
    "s_waitcnt lgkmcnt(0)\n"
    "v_mfma_f32_16x16x32_bf16 v[48:51], a[224:227], a[0:3], v[92:95]\n"
    "s_waitcnt vmcnt(7)\n"
    "s_barrier\n"
    "v_mfma_f32_16x16x32_bf16 v[52:55], a[224:227], a[96:99], v[96:99]\n"
    "ds_read_b128 v[188:191], v7 offset:49152\n"
    "ds_read_b128 v[184:187], v7 offset:50176\n"
    "v_mfma_f32_16x16x32_bf16 v[48:51], a[228:231], a[4:7], v[48:51]\n"
    "v_xor_b32 v133, s8, v133\n"
    "v_bitop3_b32 v133, s8, v135, v133 bitop3:0xbe\n"
    "v_mfma_f32_16x16x32_bf16 v[52:55], a[228:231], a[100:103], v[52:55]\n"
    "ds_read_b128 v[180:183], v7 offset:51200\n"
    "ds_read_b128 v[176:179], v7 offset:52224\n"
    "v_mfma_f32_16x16x32_bf16 v[48:51], a[232:235], a[8:11], v[48:51]\n"
    "v_bitop3_b32 v133, s8, v137, v133 bitop3:0xbe\n"
    "v_bitop3_b32 v133, s8, v139, v133 bitop3:0xbe\n"
    "v_mfma_f32_16x16x32_bf16 v[52:55], a[232:235], a[104:107], v[52:55]\n"
    "ds_read_b128 v[172:175], v7 offset:53248\n"
    "ds_read_b128 v[168:171], v7 offset:54272\n"
    "buffer_load_dwordx4 v2, s[68:71], s17 offen offset:1024 sc1 lds\n"
    "v_mfma_f32_16x16x32_bf16 v[48:51], a[236:239], a[12:15], v[48:51]\n"
    "v_cmp_ne_u32 vcc, 0, v133\n"
    "s_nop 0\n"
    "s_cbranch_vccnz R27_%=\n"
    "B28_%=:\n"
    "v_mfma_f32_16x16x32_bf16 v[52:55], a[236:239], a[108:111], v[52:55]\n"
    "ds_read_b128 v[164:167], v7 offset:55296\n"
    "ds_read_b128 v[160:163], v7 offset:56320\n"
    "v_mfma_f32_16x16x32_bf16 v[48:51], a[240:243], a[32:35], v[48:51]\n"
    "v_and_b32 v133, 0xffff0000, v132\n"
    "v_lshlrev_b32 v132, 16, v132\n"
    "v_mfma_f32_16x16x32_bf16 v[52:55], a[240:243], a[128:131], v[52:55]\n"
    "ds_read_b128 v[156:159], v7 offset:57344\n"
    "ds_read_b128 v[152:155], v7 offset:58368\n"
    "buffer_load_dwordx4 v2, s[68:71], s17 offen offset:2048 sc1 lds\n"
    "v_mfma_f32_16x16x32_bf16 v[48:51], a[244:247], a[36:39], v[48:51]\n"
    "v_and_b32 v135, 0xffff0000, v134\n"
    "v_lshlrev_b32 v134, 16, v134\n"
    "v_mfma_f32_16x16x32_bf16 v[52:55], a[244:247], a[132:135], v[52:55]\n"
    "ds_read_b128 v[116:119], v7 offset:59392\n"
    "ds_read_b128 v[84:87], v7 offset:60416\n"
    "v_mfma_f32_16x16x32_bf16 v[48:51], a[248:251], a[40:43], v[48:51]\n"
    "v_and_b32 v137, 0xffff0000, v136\n"
    "v_lshlrev_b32 v136, 16, v136\n"
    "v_mfma_f32_16x16x32_bf16 v[52:55], a[248:251], a[136:139], v[52:55]\n"
    "ds_read_b128 v[80:83], v7 offset:61440\n"
    "ds_read_b128 v[76:79], v7 offset:62464\n"
    "v_mfma_f32_16x16x32_bf16 v[48:51], a[252:255], a[44:47], v[48:51]\n"
    "v_and_b32 v139, 0xffff0000, v138\n"
    "v_lshlrev_b32 v138, 16, v138\n"
    "v_mfma_f32_16x16x32_bf16 v[52:55], a[252:255], a[140:143], v[52:55]\n"
    "ds_read_b128 v[72:75], v7 offset:63488\n"
    "ds_read_b128 v[68:71], v7 offset:64512\n"
    "buffer_load_dwordx4 v2, s[68:71], s17 offen offset:3072 sc1 lds\n"
    "v_mfma_f32_16x16x32_bf16 v[48:51], v[252:255], v[248:251], v[48:51]\n"
    "v_add_f32 v40, v40, v132\n"
    "v_add_f32 v41, v41, v133\n"
    "v_mfma_f32_16x16x32_bf16 v[52:55], v[252:255], a[160:163], v[52:55]\n"
    "v_add_f32 v42, v42, v134\n"
    "v_add_f32 v43, v43, v135\n"
    "v_mfma_f32_16x16x32_bf16 v[48:51], v[216:219], v[244:247], v[48:51]\n"
    "v_add_f32 v44, v44, v136\n"
    "v_add_f32 v45, v45, v137\n"
    "v_mfma_f32_16x16x32_bf16 v[52:55], v[216:219], a[164:167], v[52:55]\n"
    "v_add_f32 v46, v46, v138\n"
    "v_add_f32 v47, v47, v139\n"
    "v_mfma_f32_16x16x32_bf16 v[48:51], v[212:215], v[240:243], v[48:51]\n"
    "v_mul_f32 v40, v40, v108\n"
    "v_mul_f32 v41, v41, v109\n"
    "v_mfma_f32_16x16x32_bf16 v[52:55], v[212:215], a[168:171], v[52:55]\n"
    "v_mul_f32 v42, v42, v110\n"
    "v_mul_f32 v43, v43, v111\n"
    "s_add_u32 m0, s59, 69632\n"
    "s_add_u32 s17, s17, 4096\n"
    "v_mfma_f32_16x16x32_bf16 v[48:51], v[208:211], v[236:239], v[48:51]\n"
    "v_mul_f32 v44, v44, v124\n"
    "v_mul_f32 v45, v45, v125\n"
    "buffer_load_dwordx4 v2, s[68:71], s17 offen offset:0 sc1 lds\n"
    "v_mfma_f32_16x16x32_bf16 v[52:55], v[208:211], a[172:175], v[52:55]\n"
    "v_mul_f32 v46, v46, v126\n"
    "v_mul_f32 v47, v47, v127\n"
    "v_mfma_f32_16x16x32_bf16 v[48:51], v[204:207], a[64:67], v[48:51]\n"
    "v_cvt_pk_bf16_f32 v102, v40, v41\n"
    "v_cvt_pk_bf16_f32 v103, v42, v43\n"
    "v_mfma_f32_16x16x32_bf16 v[52:55], v[204:207], a[192:195], v[52:55]\n"
    "v_cvt_pk_bf16_f32 v106, v44, v45\n"
    "v_cvt_pk_bf16_f32 v107, v46, v47\n"
    "v_mfma_f32_16x16x32_bf16 v[48:51], v[200:203], a[68:71], v[48:51]\n"
    "buffer_store_dwordx4 v[100:103], v2, s[68:71], s26 offen sc1\n"
    "v_mfma_f32_16x16x32_bf16 v[52:55], v[200:203], a[196:199], v[52:55]\n"
    "buffer_store_dwordx4 v[104:107], v2, s[68:71], s27 offen sc1\n"
    "v_mfma_f32_16x16x32_bf16 v[48:51], v[196:199], a[72:75], v[48:51]\n"
    "v_mfma_f32_16x16x32_bf16 v[52:55], v[196:199], a[200:203], v[52:55]\n"
    "v_mfma_f32_16x16x32_bf16 v[48:51], v[192:195], a[76:79], v[48:51]\n"
    "v_mfma_f32_16x16x32_bf16 v[52:55], v[192:195], a[204:207], v[52:55]\n"
    "s_waitcnt lgkmcnt(0)\n"
    "v_mfma_f32_16x16x32_bf16 v[48:51], v[188:191], a[16:19], v[48:51]\n"
    "v_mfma_f32_16x16x32_bf16 v[52:55], v[188:191], a[112:115], v[52:55]\n"
    "v_mfma_f32_16x16x32_bf16 v[48:51], v[184:187], a[20:23], v[48:51]\n"
    "buffer_load_dwordx4 v2, s[68:71], s17 offen offset:1024 sc1 lds\n"
    "v_mfma_f32_16x16x32_bf16 v[52:55], v[184:187], a[116:119], v[52:55]\n"
    "v_mfma_f32_16x16x32_bf16 v[48:51], v[180:183], a[24:27], v[48:51]\n"
    "v_mfma_f32_16x16x32_bf16 v[52:55], v[180:183], a[120:123], v[52:55]\n"
    "v_mfma_f32_16x16x32_bf16 v[48:51], v[176:179], a[28:31], v[48:51]\n"
    "v_mfma_f32_16x16x32_bf16 v[52:55], v[176:179], a[124:127], v[52:55]\n"
    "v_mfma_f32_16x16x32_bf16 v[48:51], v[172:175], a[48:51], v[48:51]\n"
    "v_mfma_f32_16x16x32_bf16 v[52:55], v[172:175], a[144:147], v[52:55]\n"
    "v_mfma_f32_16x16x32_bf16 v[48:51], v[168:171], a[52:55], v[48:51]\n"
    "v_mfma_f32_16x16x32_bf16 v[52:55], v[168:171], a[148:151], v[52:55]\n"
    "v_mfma_f32_16x16x32_bf16 v[48:51], v[164:167], a[56:59], v[48:51]\n"
    "v_mfma_f32_16x16x32_bf16 v[52:55], v[164:167], a[152:155], v[52:55]\n"
    "v_mfma_f32_16x16x32_bf16 v[48:51], v[160:163], a[60:63], v[48:51]\n"
    "v_mfma_f32_16x16x32_bf16 v[52:55], v[160:163], a[156:159], v[52:55]\n"
    "v_mfma_f32_16x16x32_bf16 v[48:51], v[156:159], v[232:235], v[48:51]\n"
    "s_waitcnt vmcnt(9)\n"
    "v_xor_b32 v57, s8, v57\n"
    "v_bitop3_b32 v57, s8, v59, v57 bitop3:0xbe\n"
    "v_mfma_f32_16x16x32_bf16 v[52:55], v[156:159], a[176:179], v[52:55]\n"
    "s_waitcnt vmcnt(8)\n"
    "v_bitop3_b32 v57, s8, v61, v57 bitop3:0xbe\n"
    "v_bitop3_b32 v57, s8, v63, v57 bitop3:0xbe\n"
    "v_mfma_f32_16x16x32_bf16 v[48:51], v[152:155], v[228:231], v[48:51]\n"
    "v_cmp_ne_u32 vcc, 0, v57\n"
    "s_nop 0\n"
    "s_cbranch_vccnz R29_%=\n"
    "B30_%=:\n"
    "v_mfma_f32_16x16x32_bf16 v[52:55], v[152:155], a[180:183], v[52:55]\n"
    "v_and_b32 v57, 0xffff0000, v56\n"
    "v_lshlrev_b32 v56, 16, v56\n"
    "v_mfma_f32_16x16x32_bf16 v[48:51], v[116:119], v[224:227], v[48:51]\n"
    "v_and_b32 v59, 0xffff0000, v58\n"
    "v_lshlrev_b32 v58, 16, v58\n"
    "v_mfma_f32_16x16x32_bf16 v[52:55], v[116:119], a[184:187], v[52:55]\n"
    "v_and_b32 v61, 0xffff0000, v60\n"
    "v_lshlrev_b32 v60, 16, v60\n"
    "v_mfma_f32_16x16x32_bf16 v[48:51], v[84:87], v[220:223], v[48:51]\n"
    "v_and_b32 v63, 0xffff0000, v62\n"
    "v_lshlrev_b32 v62, 16, v62\n"
    "v_mfma_f32_16x16x32_bf16 v[52:55], v[84:87], a[188:191], v[52:55]\n"
    "v_mfma_f32_16x16x32_bf16 v[48:51], v[80:83], a[80:83], v[48:51]\n"
    "v_mfma_f32_16x16x32_bf16 v[52:55], v[80:83], a[208:211], v[52:55]\n"
    "v_mfma_f32_16x16x32_bf16 v[48:51], v[76:79], a[84:87], v[48:51]\n"
    "v_mfma_f32_16x16x32_bf16 v[52:55], v[76:79], a[212:215], v[52:55]\n"
    "v_mfma_f32_16x16x32_bf16 v[48:51], v[72:75], a[88:91], v[48:51]\n"
    "v_mfma_f32_16x16x32_bf16 v[52:55], v[72:75], a[216:219], v[52:55]\n"
    "v_mfma_f32_16x16x32_bf16 v[48:51], v[68:71], a[92:95], v[48:51]\n"
    "v_mfma_f32_16x16x32_bf16 v[52:55], v[68:71], a[220:223], v[52:55]\n"
    "s_nop 6\n"
    "v_pk_add_f32 v[48:49], v[48:49], v[56:57]\n"
    "v_pk_add_f32 v[50:51], v[50:51], v[58:59]\n"
    "v_pk_add_f32 v[52:53], v[52:53], v[60:61]\n"
    "v_pk_add_f32 v[54:55], v[54:55], v[62:63]\n"
    "v_pk_mul_f32 v[48:49], v[48:49], v[112:113]\n"
    "v_pk_mul_f32 v[50:51], v[50:51], v[114:115]\n"
    "v_pk_mul_f32 v[52:53], v[52:53], v[128:129]\n"
    "v_pk_mul_f32 v[54:55], v[54:55], v[130:131]\n"
    "v_cvt_pk_bf16_f32 v136, v48, v49\n"
    "v_cvt_pk_bf16_f32 v137, v50, v51\n"
    "v_cvt_pk_bf16_f32 v138, v52, v53\n"
    "v_cvt_pk_bf16_f32 v139, v54, v55\n"
    "s_nop 0\n"
    "v_permlane16_swap_b32 v136, v138\n"
    "v_permlane16_swap_b32 v137, v139\n"
    "buffer_store_dwordx4 v[136:139], v15, s[68:71], s22 offen offset:2048 sc1\n"
    "s_waitcnt vmcnt(5)\n"
    "s_barrier\n"
    "s_add_u32 m0, s59, 69632\n"
    "s_nop 0\n"
    "buffer_load_dwordx4 v2, s[68:71], s17 offen offset:2048 sc1 lds\n"
    "buffer_load_dwordx4 v2, s[68:71], s17 offen offset:3072 sc1 lds\n"
    "s_add_u32 m0, s59, 98304\n"
    "s_add_u32 s17, s17, 4096\n"
    "buffer_load_dwordx4 v2, s[68:71], s17 offen offset:0 sc1 lds\n"
    "v_add_u32 v89, 65536, v2\n"
    "ds_read_b128 a[96:99], v2 offset:0\n"
    "ds_read_b128 a[100:103], v2 offset:1024\n"
    "ds_read_b128 a[104:107], v2 offset:2048\n"
    "ds_read_b128 a[108:111], v2 offset:3072\n"
    "ds_read_b128 a[112:115], v2 offset:4096\n"
    "ds_read_b128 a[116:119], v2 offset:5120\n"
    "ds_read_b128 a[120:123], v2 offset:6144\n"
    "ds_read_b128 a[124:127], v2 offset:7168\n"
    "ds_read_b128 a[128:131], v2 offset:32768\n"
    "ds_read_b128 a[132:135], v2 offset:33792\n"
    "ds_read_b128 a[136:139], v2 offset:34816\n"
    "ds_read_b128 a[140:143], v2 offset:35840\n"
    "ds_read_b128 a[144:147], v2 offset:36864\n"
    "ds_read_b128 a[148:151], v2 offset:37888\n"
    "ds_read_b128 a[152:155], v2 offset:38912\n"
    "ds_read_b128 a[156:159], v2 offset:39936\n"
    "ds_read_b128 a[160:163], v89 offset:0\n"
    "ds_read_b128 a[164:167], v89 offset:1024\n"
    "ds_read_b128 a[168:171], v89 offset:2048\n"
    "ds_read_b128 a[172:175], v89 offset:3072\n"
    "ds_read_b128 a[176:179], v2 offset:8192\n"
    "ds_read_b128 a[180:183], v2 offset:9216\n"
    "ds_read_b128 a[184:187], v2 offset:10240\n"
    "ds_read_b128 a[188:191], v2 offset:11264\n"
    "ds_read_b128 a[192:195], v2 offset:12288\n"
    "ds_read_b128 a[196:199], v2 offset:13312\n"
    "ds_read_b128 a[200:203], v2 offset:14336\n"
    "ds_read_b128 a[204:207], v2 offset:15360\n"
    "ds_read_b128 a[208:211], v2 offset:40960\n"
    "ds_read_b128 a[212:215], v2 offset:41984\n"
    "ds_read_b128 a[216:219], v2 offset:43008\n"
    "ds_read_b128 a[220:223], v2 offset:44032\n"
    "ds_read_b128 a[224:227], v2 offset:45056\n"
    "ds_read_b128 a[228:231], v2 offset:46080\n"
    "ds_read_b128 a[232:235], v2 offset:47104\n"
    "ds_read_b128 a[236:239], v2 offset:48128\n"
    "ds_read_b128 a[240:243], v89 offset:8192\n"
    "ds_read_b128 a[244:247], v89 offset:9216\n"
    "ds_read_b128 a[248:251], v89 offset:10240\n"
    "ds_read_b128 a[252:255], v89 offset:11264\n"
    "s_waitcnt vmcnt(3)\n"
    "s_barrier\n"
    "s_cmp_eq_u32 s6, 0\n"
    "s_cbranch_scc0 F31_%=\n"
    "s_mov_b64 exec, 1\n"
    "buffer_atomic_swap v5, v9, s[68:71], s23 offen sc1\n"
    "s_mov_b64 exec, -1\n"
    "F31_%=:\n"
    "buffer_load_dwordx4 v2, s[68:71], s17 offen offset:1024 sc1 lds\n"
    "buffer_load_dwordx4 v2, s[68:71], s17 offen offset:2048 sc1 lds\n"
    "buffer_load_dwordx4 v2, s[68:71], s17 offen offset:3072 sc1 lds\n"
    "s_add_u32 m0, s59, 102400\n"
    "s_add_u32 s17, s17, 4096\n"
    "buffer_load_dwordx4 v2, s[68:71], s17 offen offset:0 sc1 lds\n"
    "buffer_load_dwordx4 v2, s[68:71], s17 offen offset:1024 sc1 lds\n"
    "ds_read_b128 v[252:255], v2 offset:16384\n"
    "ds_read_b128 v[248:251], v2 offset:17408\n"
    "ds_read_b128 v[244:247], v2 offset:18432\n"
    "ds_read_b128 v[240:243], v2 offset:19456\n"
    "ds_read_b128 v[236:239], v2 offset:20480\n"
    "ds_read_b128 v[232:235], v2 offset:21504\n"
    "ds_read_b128 v[228:231], v2 offset:22528\n"
    "ds_read_b128 v[224:227], v2 offset:23552\n"
    "ds_read_b128 v[220:223], v2 offset:49152\n"
    "ds_read_b128 v[216:219], v2 offset:50176\n"
    "ds_read_b128 v[212:215], v2 offset:51200\n"
    "ds_read_b128 v[208:211], v2 offset:52224\n"
    "ds_read_b128 v[204:207], v2 offset:53248\n"
    "ds_read_b128 v[200:203], v2 offset:54272\n"
    "ds_read_b128 v[196:199], v2 offset:55296\n"
    "v_mov_b32 v28, 0\n"
    "s_mov_b64 exec, 0xf\n"
    "buffer_atomic_or v28, v9, s[68:71], s12 offen offset:0 sc0 sc1\n"
    "s_mov_b64 exec, -1\n"
    "ds_read_b128 v[192:195], v2 offset:56320\n"
    "ds_read_b128 v[188:191], v89 offset:16384\n"
    "ds_read_b128 v[184:187], v89 offset:17408\n"
    "ds_read_b128 v[180:183], v89 offset:18432\n"
    "ds_read_b128 v[176:179], v89 offset:19456\n"
    "ds_read_b128 v[172:175], v2 offset:24576\n"
    "ds_read_b128 v[168:171], v2 offset:25600\n"
    "ds_read_b128 v[164:167], v2 offset:26624\n"
    "ds_read_b128 v[160:163], v2 offset:27648\n"
    "ds_read_b128 v[156:159], v2 offset:28672\n"
    "PAD8_%=:\n"
    "s_cbranch_execz PADRET2_%=\n"
    "ds_read_b128 v[152:155], v2 offset:29696\n"
    "ds_read_b128 v[128:131], v2 offset:30720\n"
    "ds_read_b128 v[124:127], v2 offset:31744\n"
    "ds_read_b128 v[120:123], v2 offset:57344\n"
    "ds_read_b128 v[116:119], v2 offset:58368\n"
    "ds_read_b128 v[112:115], v2 offset:59392\n"
    "ds_read_b128 v[108:111], v2 offset:60416\n"
    "ds_read_b128 v[104:107], v2 offset:61440\n"
    "ds_read_b128 v[100:103], v2 offset:62464\n"
    "ds_read_b128 v[96:99], v2 offset:63488\n"
    "ds_read_b128 v[92:95], v2 offset:64512\n"
    "ds_read_b128 v[84:87], v89 offset:24576\n"
    "ds_read_b128 v[80:83], v89 offset:25600\n"
    "ds_read_b128 v[76:79], v89 offset:26624\n"
    "ds_read_b128 v[72:75], v89 offset:27648\n"
    "v_lshlrev_b32 v20, 16, v140\n"
    "v_and_b32 v21, 0xffff0000, v140\n"
    "v_lshlrev_b32 v22, 16, v141\n"
    "v_and_b32 v23, 0xffff0000, v141\n"
    "v_lshlrev_b32 v24, 16, v142\n"
    "v_and_b32 v25, 0xffff0000, v142\n"
    "v_lshlrev_b32 v26, 16, v143\n"
    "v_and_b32 v27, 0xffff0000, v143\n"
    "v_lshlrev_b32 v32, 16, v144\n"
    "v_and_b32 v33, 0xffff0000, v144\n"
    "v_lshlrev_b32 v34, 16, v145\n"
    "v_and_b32 v35, 0xffff0000, v145\n"
    "v_lshlrev_b32 v36, 16, v146\n"
    "v_and_b32 v37, 0xffff0000, v146\n"
    "v_lshlrev_b32 v38, 16, v147\n"
    "v_and_b32 v39, 0xffff0000, v147\n"
    "v_lshlrev_b32 v40, 16, v148\n"
    "v_and_b32 v41, 0xffff0000, v148\n"
    "v_lshlrev_b32 v42, 16, v149\n"
    "v_and_b32 v43, 0xffff0000, v149\n"
    "s_lshl_b32 s38, s6, 13\n"
    "s_add_u32 s38, s38, 131072\n"
    "v_add_u32 v150, 0, v1\n"
    "v_mul_u32_u24 v151, 205, v150\n"
    "v_lshrrev_b32 v151, 11, v151\n"
    "v_mul_u32_u24 v151, 1120, v151\n"
    "v_lshl_add_u32 v7, v150, 4, v151\n"
    "v_add_u32 v150, 64, v1\n"
    "v_mul_u32_u24 v151, 205, v150\n"
    "v_lshrrev_b32 v151, 11, v151\n"
    "v_mul_u32_u24 v151, 1120, v151\n"
    "v_lshl_add_u32 v11, v150, 4, v151\n"
    "v_add_u32 v150, 128, v1\n"
    "v_mul_u32_u24 v151, 205, v150\n"
    "v_lshrrev_b32 v151, 11, v151\n"
    "v_mul_u32_u24 v151, 1120, v151\n"
    "v_lshl_add_u32 v31, v150, 4, v151\n"
    "v_mul_u32_u24 v151, 160, v3\n"
    "v_add_u32 v151, s38, v151\n"
    "v_lshl_add_u32 v150, v4, 4, v151\n"
    "v_lshl_add_u32 v151, v4, 3, v151\n"
    "s_waitcnt vmcnt(0)\n"
    "v_cmp_ne_u32 vcc, s8, v28\n"
    "s_nop 0\n"
    "s_and_b64 vcc, vcc, 0xf\n"
    "s_cbranch_vccnz R32_%=\n"
    "B33_%=:\n"
    "s_barrier\n"
    "buffer_load_dwordx4 a[0:3], v2, s[68:71], s30 offen offset:0 sc1\n"
    "buffer_load_dwordx4 a[4:7], v2, s[68:71], s30 offen offset:1024 sc1\n"
    "buffer_load_dwordx4 a[8:11], v2, s[68:71], s30 offen offset:3072 sc1\n"
    "buffer_load_dwordx4 a[12:15], v10, s[68:71], s30 offen offset:0 sc1\n"
    "buffer_load_dwordx4 a[16:19], v10, s[68:71], s30 offen offset:2048 sc1\n"
    "buffer_load_dwordx4 a[20:23], v10, s[68:71], s30 offen offset:3072 sc1\n"
    "buffer_load_dwordx4 a[24:27], v2, s[68:71], s31 offen offset:1024 sc1\n"
    "buffer_load_dwordx4 a[28:31], v2, s[68:71], s31 offen offset:2048 sc1\n"
    "s_waitcnt vmcnt(7) lgkmcnt(15)\n"
    "v_mfma_f32_16x16x32_bf16 v[20:23], a[96:99], a[0:3], v[20:23]\n"
    "buffer_load_dwordx4 a[32:35], v10, s[68:71], s31 offen offset:0 sc1\n"
    "v_mfma_f32_16x16x32_bf16 v[24:27], a[100:103], a[0:3], v[24:27]\n"
    "v_mfma_f32_16x16x32_bf16 v[32:35], a[104:107], a[0:3], v[32:35]\n"
    "v_mfma_f32_16x16x32_bf16 v[36:39], a[108:111], a[0:3], v[36:39]\n"
    "v_mfma_f32_16x16x32_bf16 v[40:43], a[112:115], a[0:3], v[40:43]\n"
    "s_waitcnt vmcnt(7)\n"
    "v_mfma_f32_16x16x32_bf16 v[20:23], a[116:119], a[4:7], v[20:23]\n"
    "ds_read_b128 a[96:99], v89 offset:4096\n"
    "ds_read_b128 a[100:103], v89 offset:5120\n"
    "v_mfma_f32_16x16x32_bf16 v[24:27], a[120:123], a[4:7], v[24:27]\n"
    "ds_read_b128 a[104:107], v89 offset:6144\n"
    "ds_read_b128 a[108:111], v89 offset:7168\n"
    "v_mfma_f32_16x16x32_bf16 v[32:35], a[124:127], a[4:7], v[32:35]\n"
    "ds_read_b128 a[112:115], v89 offset:32768\n"
    "v_mfma_f32_16x16x32_bf16 v[36:39], a[128:131], a[4:7], v[36:39]\n"
    "buffer_load_dwordx4 a[36:39], v10, s[68:71], s31 offen offset:1024 sc1\n"
    "v_mfma_f32_16x16x32_bf16 v[40:43], a[132:135], a[4:7], v[40:43]\n"
    "s_waitcnt vmcnt(7)\n"
    "v_mfma_f32_16x16x32_bf16 v[20:23], a[136:139], a[8:11], v[20:23]\n"
    "ds_read_b128 a[116:119], v89 offset:33792\n"
    "ds_read_b128 a[120:123], v89 offset:34816\n"
    "v_mfma_f32_16x16x32_bf16 v[24:27], a[140:143], a[8:11], v[24:27]\n"
    "ds_read_b128 a[124:127], v89 offset:35840\n"
    "ds_read_b128 a[128:131], v89 offset:36864\n"
    "v_mfma_f32_16x16x32_bf16 v[32:35], a[144:147], a[8:11], v[32:35]\n"
    "ds_read_b128 a[132:135], v89 offset:37888\n"
    "v_mfma_f32_16x16x32_bf16 v[36:39], a[148:151], a[8:11], v[36:39]\n"
    "buffer_load_dwordx4 a[40:43], v10, s[68:71], s31 offen offset:3072 sc1\n"
    "v_mfma_f32_16x16x32_bf16 v[40:43], a[152:155], a[8:11], v[40:43]\n"
    "s_waitcnt vmcnt(7)\n"
    "v_mfma_f32_16x16x32_bf16 v[20:23], a[156:159], a[12:15], v[20:23]\n"
    "ds_read_b128 a[136:139], v89 offset:12288\n"
    "ds_read_b128 a[140:143], v89 offset:13312\n"
    "v_mfma_f32_16x16x32_bf16 v[24:27], a[160:163], a[12:15], v[24:27]\n"
    "ds_read_b128 a[144:147], v89 offset:14336\n"
    "ds_read_b128 a[148:151], v89 offset:15360\n"
    "v_mfma_f32_16x16x32_bf16 v[32:35], a[164:167], a[12:15], v[32:35]\n"
    "ds_read_b128 a[152:155], v89 offset:40960\n"
    "v_mfma_f32_16x16x32_bf16 v[36:39], a[168:171], a[12:15], v[36:39]\n"
    "buffer_load_dwordx4 a[44:47], v2, s[68:71], s41 offen offset:0 sc1\n"
    "v_mfma_f32_16x16x32_bf16 v[40:43], a[172:175], a[12:15], v[40:43]\n"
    "s_waitcnt vmcnt(7)\n"
    "v_mfma_f32_16x16x32_bf16 v[20:23], a[176:179], a[16:19], v[20:23]\n"
    "ds_read_b128 a[156:159], v89 offset:41984\n"
    "ds_read_b128 a[160:163], v89 offset:43008\n"
    "v_mfma_f32_16x16x32_bf16 v[24:27], a[180:183], a[16:19], v[24:27]\n"
    "ds_read_b128 a[164:167], v89 offset:44032\n"
    "ds_read_b128 a[168:171], v89 offset:45056\n"
    "v_mfma_f32_16x16x32_bf16 v[32:35], a[184:187], a[16:19], v[32:35]\n"
    "ds_read_b128 a[172:175], v89 offset:46080\n"
    "v_mfma_f32_16x16x32_bf16 v[36:39], a[188:191], a[16:19], v[36:39]\n"
    "buffer_load_dwordx4 a[48:51], v2, s[68:71], s41 offen offset:2048 sc1\n"
    "v_mfma_f32_16x16x32_bf16 v[40:43], a[192:195], a[16:19], v[40:43]\n"
    "s_waitcnt vmcnt(7)\n"
    "v_mfma_f32_16x16x32_bf16 v[20:23], a[196:199], a[20:23], v[20:23]\n"
    "ds_read_b128 a[176:179], v89 offset:20480\n"
    "ds_read_b128 a[180:183], v89 offset:21504\n"
    "v_mfma_f32_16x16x32_bf16 v[24:27], a[200:203], a[20:23], v[24:27]\n"
    "ds_read_b128 a[184:187], v89 offset:22528\n"
    "ds_read_b128 a[188:191], v89 offset:23552\n"
    "v_mfma_f32_16x16x32_bf16 v[32:35], a[204:207], a[20:23], v[32:35]\n"
    "ds_read_b128 a[192:195], v89 offset:49152\n"
    "v_mfma_f32_16x16x32_bf16 v[36:39], a[208:211], a[20:23], v[36:39]\n"
    "buffer_load_dwordx4 a[52:55], v2, s[68:71], s41 offen offset:3072 sc1\n"
    "v_mfma_f32_16x16x32_bf16 v[40:43], a[212:215], a[20:23], v[40:43]\n"
    "s_waitcnt vmcnt(7)\n"
    "v_mfma_f32_16x16x32_bf16 v[20:23], a[216:219], a[24:27], v[20:23]\n"
    "ds_read_b128 a[196:199], v89 offset:50176\n"
    "ds_read_b128 a[200:203], v89 offset:51200\n"
    "v_mfma_f32_16x16x32_bf16 v[24:27], a[220:223], a[24:27], v[24:27]\n"
    "ds_read_b128 a[204:207], v89 offset:52224\n"
    "ds_read_b128 a[208:211], v89 offset:53248\n"
    "v_mfma_f32_16x16x32_bf16 v[32:35], a[224:227], a[24:27], v[32:35]\n"
    "ds_read_b128 a[212:215], v89 offset:54272\n"
    "v_mfma_f32_16x16x32_bf16 v[36:39], a[228:231], a[24:27], v[36:39]\n"
    "buffer_load_dwordx4 a[56:59], v10, s[68:71], s41 offen offset:1024 sc1\n"
    "v_mfma_f32_16x16x32_bf16 v[40:43], a[232:235], a[24:27], v[40:43]\n"
    "s_waitcnt vmcnt(7)\n"
    "v_mfma_f32_16x16x32_bf16 v[20:23], a[236:239], a[28:31], v[20:23]\n"
    "ds_read_b128 a[216:219], v89 offset:28672\n"
    "ds_read_b128 a[220:223], v89 offset:29696\n"
    "v_mfma_f32_16x16x32_bf16 v[24:27], a[240:243], a[28:31], v[24:27]\n"
    "ds_read_b128 a[224:227], v89 offset:30720\n"
    "ds_read_b128 a[228:231], v89 offset:31744\n"
    "v_mfma_f32_16x16x32_bf16 v[32:35], a[244:247], a[28:31], v[32:35]\n"
    "ds_read_b128 a[232:235], v89 offset:57344\n"
    "v_mfma_f32_16x16x32_bf16 v[36:39], a[248:251], a[28:31], v[36:39]\n"
    "buffer_load_dwordx4 a[60:63], v10, s[68:71], s41 offen offset:2048 sc1\n"
    "v_mfma_f32_16x16x32_bf16 v[40:43], a[252:255], a[28:31], v[40:43]\n"
    "s_waitcnt vmcnt(7)\n"
    "v_mfma_f32_16x16x32_bf16 v[20:23], v[252:255], a[32:35], v[20:23]\n"
    "ds_read_b128 a[236:239], v89 offset:58368\n"
    "ds_read_b128 a[240:243], v89 offset:59392\n"
    "v_mfma_f32_16x16x32_bf16 v[24:27], v[248:251], a[32:35], v[24:27]\n"
    "ds_read_b128 a[244:247], v89 offset:60416\n"
    "ds_read_b128 a[248:251], v89 offset:61440\n"
    "v_mfma_f32_16x16x32_bf16 v[32:35], v[244:247], a[32:35], v[32:35]\n"
    "ds_read_b128 a[252:255], v89 offset:62464\n"
    "v_mfma_f32_16x16x32_bf16 v[36:39], v[240:243], a[32:35], v[36:39]\n"
    "buffer_load_dwordx4 a[64:67], v2, s[68:71], s30 offen offset:2048 sc1\n"
    "v_mfma_f32_16x16x32_bf16 v[40:43], v[236:239], a[32:35], v[40:43]\n"
    "s_waitcnt vmcnt(7)\n"
    "v_mfma_f32_16x16x32_bf16 v[20:23], v[232:235], a[36:39], v[20:23]\n"
    "buffer_load_dwordx4 a[68:71], v10, s[68:71], s30 offen offset:1024 sc1\n"
    "v_mfma_f32_16x16x32_bf16 v[24:27], v[228:231], a[36:39], v[24:27]\n"
    "v_mfma_f32_16x16x32_bf16 v[32:35], v[224:227], a[36:39], v[32:35]\n"
    "v_mfma_f32_16x16x32_bf16 v[36:39], v[220:223], a[36:39], v[36:39]\n"
    "v_mfma_f32_16x16x32_bf16 v[40:43], v[216:219], a[36:39], v[40:43]\n"
    "s_waitcnt vmcnt(7)\n"
    "v_mfma_f32_16x16x32_bf16 v[20:23], v[212:215], a[40:43], v[20:23]\n"
    "buffer_load_dwordx4 a[72:75], v2, s[68:71], s31 offen offset:0 sc1\n"
    "v_mfma_f32_16x16x32_bf16 v[24:27], v[208:211], a[40:43], v[24:27]\n"
    "v_mfma_f32_16x16x32_bf16 v[32:35], v[204:207], a[40:43], v[32:35]\n"
    "v_mfma_f32_16x16x32_bf16 v[36:39], v[200:203], a[40:43], v[36:39]\n"
    "v_mfma_f32_16x16x32_bf16 v[40:43], v[196:199], a[40:43], v[40:43]\n"
    "s_waitcnt vmcnt(7)\n"
    "v_mfma_f32_16x16x32_bf16 v[20:23], v[192:195], a[44:47], v[20:23]\n"
    "buffer_load_dwordx4 a[76:79], v2, s[68:71], s31 offen offset:3072 sc1\n"
    "v_mfma_f32_16x16x32_bf16 v[24:27], v[188:191], a[44:47], v[24:27]\n"
    "v_mfma_f32_16x16x32_bf16 v[32:35], v[184:187], a[44:47], v[32:35]\n"
    "v_mfma_f32_16x16x32_bf16 v[36:39], v[180:183], a[44:47], v[36:39]\n"
    "v_mfma_f32_16x16x32_bf16 v[40:43], v[176:179], a[44:47], v[40:43]\n"
    "s_waitcnt vmcnt(7)\n"
    "v_mfma_f32_16x16x32_bf16 v[20:23], v[172:175], a[48:51], v[20:23]\n"
    "buffer_load_dwordx4 a[80:83], v10, s[68:71], s31 offen offset:2048 sc1\n"
    "v_mfma_f32_16x16x32_bf16 v[24:27], v[168:171], a[48:51], v[24:27]\n"
    "v_mfma_f32_16x16x32_bf16 v[32:35], v[164:167], a[48:51], v[32:35]\n"
    "v_mfma_f32_16x16x32_bf16 v[36:39], v[160:163], a[48:51], v[36:39]\n"
    "v_mfma_f32_16x16x32_bf16 v[40:43], v[156:159], a[48:51], v[40:43]\n"
    "s_waitcnt vmcnt(7) lgkmcnt(15)\n"
    "v_mfma_f32_16x16x32_bf16 v[20:23], v[152:155], a[52:55], v[20:23]\n"
    "buffer_load_dwordx4 a[84:87], v2, s[68:71], s41 offen offset:1024 sc1\n"
    "v_mfma_f32_16x16x32_bf16 v[24:27], v[128:131], a[52:55], v[24:27]\n"
    "v_mfma_f32_16x16x32_bf16 v[32:35], v[124:127], a[52:55], v[32:35]\n"
    "v_mfma_f32_16x16x32_bf16 v[36:39], v[120:123], a[52:55], v[36:39]\n"
    "v_mfma_f32_16x16x32_bf16 v[40:43], v[116:119], a[52:55], v[40:43]\n"
    "s_waitcnt vmcnt(7)\n"
    "v_mfma_f32_16x16x32_bf16 v[20:23], v[112:115], a[56:59], v[20:23]\n"
    "buffer_load_dwordx4 a[88:91], v10, s[68:71], s41 offen offset:0 sc1\n"
    "v_mfma_f32_16x16x32_bf16 v[24:27], v[108:111], a[56:59], v[24:27]\n"
    "v_mfma_f32_16x16x32_bf16 v[32:35], v[104:107], a[56:59], v[32:35]\n"
    "v_mfma_f32_16x16x32_bf16 v[36:39], v[100:103], a[56:59], v[36:39]\n"
    "v_mfma_f32_16x16x32_bf16 v[40:43], v[96:99], a[56:59], v[40:43]\n"
    "s_waitcnt vmcnt(7)\n"
    "v_mfma_f32_16x16x32_bf16 v[20:23], v[92:95], a[60:63], v[20:23]\n"
    "buffer_load_dwordx4 a[92:95], v10, s[68:71], s41 offen offset:3072 sc1\n"
    "v_mfma_f32_16x16x32_bf16 v[24:27], v[84:87], a[60:63], v[24:27]\n"
    "v_mfma_f32_16x16x32_bf16 v[32:35], v[80:83], a[60:63], v[32:35]\n"
    "v_mfma_f32_16x16x32_bf16 v[36:39], v[76:79], a[60:63], v[36:39]\n"
    "v_mfma_f32_16x16x32_bf16 v[40:43], v[72:75], a[60:63], v[40:43]\n"
    "s_waitcnt vmcnt(7)\n"
    "v_mfma_f32_16x16x32_bf16 v[20:23], a[96:99], a[64:67], v[20:23]\n"
    "v_mfma_f32_16x16x32_bf16 v[24:27], a[100:103], a[64:67], v[24:27]\n"
    "v_mfma_f32_16x16x32_bf16 v[32:35], a[104:107], a[64:67], v[32:35]\n"
    "v_mfma_f32_16x16x32_bf16 v[36:39], a[108:111], a[64:67], v[36:39]\n"
    "v_mfma_f32_16x16x32_bf16 v[40:43], a[112:115], a[64:67], v[40:43]\n"
    "s_waitcnt vmcnt(6)\n"
    "v_mfma_f32_16x16x32_bf16 v[20:23], a[116:119], a[68:71], v[20:23]\n"
    "v_mfma_f32_16x16x32_bf16 v[24:27], a[120:123], a[68:71], v[24:27]\n"
    "v_mfma_f32_16x16x32_bf16 v[32:35], a[124:127], a[68:71], v[32:35]\n"
    "v_mfma_f32_16x16x32_bf16 v[36:39], a[128:131], a[68:71], v[36:39]\n"
    "v_mfma_f32_16x16x32_bf16 v[40:43], a[132:135], a[68:71], v[40:43]\n"
    "s_waitcnt vmcnt(5)\n"
    "v_mfma_f32_16x16x32_bf16 v[20:23], a[136:139], a[72:75], v[20:23]\n"
    "v_mfma_f32_16x16x32_bf16 v[24:27], a[140:143], a[72:75], v[24:27]\n"
    "v_mfma_f32_16x16x32_bf16 v[32:35], a[144:147], a[72:75], v[32:35]\n"
    "v_mfma_f32_16x16x32_bf16 v[36:39], a[148:151], a[72:75], v[36:39]\n"
    "v_mfma_f32_16x16x32_bf16 v[40:43], a[152:155], a[72:75], v[40:43]\n"
    "s_waitcnt vmcnt(4)\n"
    "v_mfma_f32_16x16x32_bf16 v[20:23], a[156:159], a[76:79], v[20:23]\n"
    "v_mfma_f32_16x16x32_bf16 v[24:27], a[160:163], a[76:79], v[24:27]\n"
    "v_mfma_f32_16x16x32_bf16 v[32:35], a[164:167], a[76:79], v[32:35]\n"
    "v_mfma_f32_16x16x32_bf16 v[36:39], a[168:171], a[76:79], v[36:39]\n"
    "v_mfma_f32_16x16x32_bf16 v[40:43], a[172:175], a[76:79], v[40:43]\n"
    "s_waitcnt vmcnt(3)\n"
    "v_mfma_f32_16x16x32_bf16 v[20:23], a[176:179], a[80:83], v[20:23]\n"
    "v_mfma_f32_16x16x32_bf16 v[24:27], a[180:183], a[80:83], v[24:27]\n"
    "v_mfma_f32_16x16x32_bf16 v[32:35], a[184:187], a[80:83], v[32:35]\n"
    "v_mfma_f32_16x16x32_bf16 v[36:39], a[188:191], a[80:83], v[36:39]\n"
    "v_mfma_f32_16x16x32_bf16 v[40:43], a[192:195], a[80:83], v[40:43]\n"
    "s_waitcnt vmcnt(2) lgkmcnt(14)\n"
    "v_mfma_f32_16x16x32_bf16 v[20:23], a[196:199], a[84:87], v[20:23]\n"
    "s_waitcnt lgkmcnt(13)\n"
    "v_mfma_f32_16x16x32_bf16 v[24:27], a[200:203], a[84:87], v[24:27]\n"
    "s_waitcnt lgkmcnt(12)\n"
    "v_mfma_f32_16x16x32_bf16 v[32:35], a[204:207], a[84:87], v[32:35]\n"
    "s_waitcnt lgkmcnt(11)\n"
    "v_mfma_f32_16x16x32_bf16 v[36:39], a[208:211], a[84:87], v[36:39]\n"
    "s_waitcnt lgkmcnt(10)\n"
    "v_mfma_f32_16x16x32_bf16 v[40:43], a[212:215], a[84:87], v[40:43]\n"
    "s_waitcnt vmcnt(1) lgkmcnt(9)\n"
    "v_mfma_f32_16x16x32_bf16 v[20:23], a[216:219], a[88:91], v[20:23]\n"
    "s_waitcnt lgkmcnt(8)\n"
    "v_mfma_f32_16x16x32_bf16 v[24:27], a[220:223], a[88:91], v[24:27]\n"
    "s_waitcnt lgkmcnt(7)\n"
    "v_mfma_f32_16x16x32_bf16 v[32:35], a[224:227], a[88:91], v[32:35]\n"
    "s_waitcnt lgkmcnt(6)\n"
    "v_mfma_f32_16x16x32_bf16 v[36:39], a[228:231], a[88:91], v[36:39]\n"
    "s_waitcnt lgkmcnt(5)\n"
    "v_mfma_f32_16x16x32_bf16 v[40:43], a[232:235], a[88:91], v[40:43]\n"
    "s_waitcnt vmcnt(0) lgkmcnt(4)\n"
    "v_mfma_f32_16x16x32_bf16 v[20:23], a[236:239], a[92:95], v[20:23]\n"
    "s_waitcnt lgkmcnt(3)\n"
    "v_mfma_f32_16x16x32_bf16 v[24:27], a[240:243], a[92:95], v[24:27]\n"
    "s_waitcnt lgkmcnt(2)\n"
    "v_mfma_f32_16x16x32_bf16 v[32:35], a[244:247], a[92:95], v[32:35]\n"
    "s_waitcnt lgkmcnt(1)\n"
    "v_mfma_f32_16x16x32_bf16 v[36:39], a[248:251], a[92:95], v[36:39]\n"
    "s_waitcnt lgkmcnt(0)\n"
    "v_mfma_f32_16x16x32_bf16 v[40:43], a[252:255], a[92:95], v[40:43]\n"
    "buffer_store_dword v5, v30, s[68:71], 0 offen sc1\n"
    "v_cvt_pk_bf16_f32 v140, v20, v21\n"
    "v_cvt_pk_bf16_f32 v141, v22, v23\n"
    "v_cvt_pk_bf16_f32 v142, v24, v25\n"
    "v_cvt_pk_bf16_f32 v143, v26, v27\n"
    "ds_write_b128 v150, v[140:143] offset:0\n"
    "v_cvt_pk_bf16_f32 v144, v32, v33\n"
    "v_cvt_pk_bf16_f32 v145, v34, v35\n"
    "v_cvt_pk_bf16_f32 v146, v36, v37\n"
    "v_cvt_pk_bf16_f32 v147, v38, v39\n"
    "ds_write_b128 v150, v[144:147] offset:64\n"
    "v_cvt_pk_bf16_f32 v148, v40, v41\n"
    "v_cvt_pk_bf16_f32 v149, v42, v43\n"
    "ds_write_b64 v151, v[148:149] offset:128\n"
    "ds_read_b128 v[20:23], v0 offset:0\n"
    "ds_read_b128 v[24:27], v0 offset:1024\n"
    "ds_read_b128 v[32:35], v0 offset:2048\n"
    "s_waitcnt lgkmcnt(2)\n"
    "buffer_store_dwordx4 v[20:23], v7, s[84:87], s13 offen\n"
    "s_waitcnt lgkmcnt(1)\n"
    "buffer_store_dwordx4 v[24:27], v11, s[84:87], s13 offen\n"
    "s_lshr_b64 exec, -1, 32\n"
    "s_waitcnt lgkmcnt(0)\n"
    "buffer_store_dwordx4 v[32:35], v31, s[84:87], s13 offen\n"
    "s_mov_b64 exec, -1\n"
    "s_waitcnt vmcnt(0)\n"
    "s_endpgm\n"
    "R4_%=:\n"
    "PAD9_%=:\n"
    "s_cbranch_execz PADRET2_%=\n"
    "s_sleep 1\n"
    "buffer_load_dwordx4 v[32:35], v51, s[68:71], s28 offen offset:0 sc1\n"
    "buffer_load_dwordx4 v[36:39], v51, s[68:71], s28 offen offset:64 sc1\n"
    "s_waitcnt vmcnt(0)\n"
    "v_xor_b32 v33, s8, v33\n"
    "v_bitop3_b32 v33, s8, v35, v33 bitop3:0xbe\n"
    "v_bitop3_b32 v33, s8, v37, v33 bitop3:0xbe\n"
    "v_bitop3_b32 v33, s8, v39, v33 bitop3:0xbe\n"
    "v_cmp_ne_u32 vcc, 0, v33\n"
    "s_add_u32 s9, s9, 1\n"
    "s_cbranch_vccz B5_%=\n"
    "s_cmp_lt_u32 s9, 0x40000\n"
    "s_cbranch_scc1 R4_%=\n"
    "s_branch B5_%=\n"
    "R7_%=:\n"
    "s_sleep 1\n"
    "v_mov_b32 v28, 0\n"
    "s_mov_b64 exec, 0xf\n"
    "buffer_atomic_or v28, v9, s[68:71], s26 offen offset:0 sc0 sc1\n"
    "s_mov_b64 exec, -1\n"
    "s_waitcnt vmcnt(0)\n"
    "v_cmp_ne_u32 vcc, s8, v28\n"
    "s_nop 0\n"
    "s_and_b64 vcc, vcc, 0xf\n"
    "s_add_u32 s9, s9, 1\n"
    "s_cbranch_vccz B8_%=\n"
    "s_cmp_lt_u32 s9, 0x40000\n"
    "s_cbranch_scc1 R7_%=\n"
    "s_branch B8_%=\n"
    "R9_%=:\n"
    "s_sleep 1\n"
    "s_lshr_b64 exec, -1, 32\n"
    "buffer_load_dword v15, v9, s[68:71], s52 offen offset:0 sc1\n"
    "s_mov_b64 exec, -1\n"
    "s_waitcnt vmcnt(0)\n"
    "v_cmp_ne_u32 vcc, s8, v15\n"
    "s_nop 0\n"
    "s_lshl_b64 vcc, vcc, 32\n"
    "s_add_u32 s9, s9, 1\n"
    "s_cbranch_vccz B10_%=\n"
    "s_cmp_lt_u32 s9, 0x40000\n"
    "s_cbranch_scc1 R9_%=\n"
    "s_branch B10_%=\n"
    "R11_%=:\n"
    "s_sleep 1\n"
    "s_mov_b64 exec, 0x1\n"
    "buffer_load_dword v28, v9, s[68:71], s29 offen offset:0 sc1\n"
    "s_mov_b64 exec, -1\n"
    "s_waitcnt vmcnt(0)\n"
    "v_cmp_ne_u32 vcc, s8, v28\n"
    "s_nop 0\n"
    "s_and_b64 vcc, vcc, 0x1\n"
    "s_add_u32 s9, s9, 1\n"
    "s_cbranch_vccz B12_%=\n"
    "s_cmp_lt_u32 s9, 0x40000\n"
    "s_cbranch_scc1 R11_%=\n"
    "s_branch B12_%=\n"
    "R13_%=:\n"
    "s_sleep 1\n"
    "s_mov_b64 exec, 0x1\n"
    "buffer_load_dword v28, v9, s[68:71], s29 offen offset:4 sc1\n"
    "s_mov_b64 exec, -1\n"
    "s_waitcnt vmcnt(0)\n"
    "v_cmp_ne_u32 vcc, s8, v28\n"
    "s_nop 0\n"
    "s_and_b64 vcc, vcc, 0x1\n"
    "s_add_u32 s9, s9, 1\n"
    "s_cbranch_vccz B14_%=\n"
    "s_cmp_lt_u32 s9, 0x40000\n"
    "s_cbranch_scc1 R13_%=\n"
    "s_branch B14_%=\n"
    "R15_%=:\n"
    "s_sleep 1\n"
    "s_mov_b64 exec, 0x1\n"
    "buffer_load_dword v31, v9, s[68:71], s29 offen offset:8 sc1\n"
    "s_mov_b64 exec, -1\n"
    "s_waitcnt vmcnt(0)\n"
    "v_cmp_ne_u32 vcc, s8, v31\n"
    "s_nop 0\n"
    "s_and_b64 vcc, vcc, 0x1\n"
    "s_add_u32 s9, s9, 1\n"
    "s_cbranch_vccz B16_%=\n"
    "s_cmp_lt_u32 s9, 0x40000\n"
    "s_cbranch_scc1 R15_%=\n"
    "s_branch B16_%=\n"
    "R18_%=:\n"
    "s_sleep 1\n"
    "v_mov_b32 v28, 0\n"
    "s_mov_b64 exec, 0xf\n"
    "buffer_atomic_or v28, v9, s[68:71], s4 offen offset:0 sc0 sc1\n"
    "s_mov_b64 exec, -1\n"
    "s_waitcnt vmcnt(0)\n"
    "v_cmp_ne_u32 vcc, s8, v28\n"
    "s_nop 0\n"
    "s_and_b64 vcc, vcc, 0xf\n"
    "s_add_u32 s9, s9, 1\n"
    "s_cbranch_vccz B19_%=\n"
    "s_cmp_lt_u32 s9, 0x40000\n"
    "s_cbranch_scc1 R18_%=\n"
    "s_branch B19_%=\n"
    "R20_%=:\n"
    "s_sleep 1\n"
    "buffer_load_dwordx4 v[64:67], v0, s[68:71], s10 offen offset:0 sc1\n"
    "buffer_load_dwordx4 v[68:71], v0, s[68:71], s10 offen offset:1024 sc1\n"
    "buffer_load_dwordx4 v[72:75], v0, s[68:71], s10 offen offset:2048 sc1\n"
    "buffer_load_dwordx4 v[76:79], v0, s[68:71], s10 offen offset:3072 sc1\n"
    "buffer_load_dwordx4 v[80:83], v0, s[68:71], s5 offen offset:0 sc1\n"
    "buffer_load_dwordx4 v[84:87], v0, s[68:71], s5 offen offset:1024 sc1\n"
    "buffer_load_dwordx4 v[132:135], v0, s[68:71], s5 offen offset:2048 sc1\n"
    "buffer_load_dwordx4 v[136:139], v0, s[68:71], s5 offen offset:3072 sc1\n"
    "s_waitcnt vmcnt(0)\n"
    "v_xor_b32 v65, s8, v65\n"
    "v_bitop3_b32 v65, s8, v67, v65 bitop3:0xbe\n"
    "v_bitop3_b32 v65, s8, v69, v65 bitop3:0xbe\n"
    "v_bitop3_b32 v65, s8, v71, v65 bitop3:0xbe\n"
    "v_bitop3_b32 v65, s8, v73, v65 bitop3:0xbe\n"
    "v_bitop3_b32 v65, s8, v75, v65 bitop3:0xbe\n"
    "v_bitop3_b32 v65, s8, v77, v65 bitop3:0xbe\n"
    "v_bitop3_b32 v65, s8, v79, v65 bitop3:0xbe\n"
    "v_bitop3_b32 v65, s8, v81, v65 bitop3:0xbe\n"
    "v_bitop3_b32 v65, s8, v83, v65 bitop3:0xbe\n"
    "v_bitop3_b32 v65, s8, v85, v65 bitop3:0xbe\n"
    "v_bitop3_b32 v65, s8, v87, v65 bitop3:0xbe\n"
    "v_bitop3_b32 v65, s8, v133, v65 bitop3:0xbe\n"
    "v_bitop3_b32 v65, s8, v135, v65 bitop3:0xbe\n"
    "v_bitop3_b32 v65, s8, v137, v65 bitop3:0xbe\n"
    "v_bitop3_b32 v65, s8, v139, v65 bitop3:0xbe\n"
    "v_cmp_ne_u32 vcc, 0, v65\n"
    "s_add_u32 s9, s9, 1\n"
    "s_cbranch_vccz B21_%=\n"
    "s_cmp_lt_u32 s9, 0x40000\n"
    "s_cbranch_scc1 R20_%=\n"
    "s_branch B21_%=\n"
    "R23_%=:\n"
    "s_sleep 1\n"
    "v_mov_b32 v28, 0\n"
    "s_mov_b64 exec, 0xf\n"
    "buffer_atomic_or v28, v9, s[68:71], s4 offen offset:0 sc0 sc1\n"
    "s_mov_b64 exec, -1\n"
    "s_waitcnt vmcnt(0)\n"
    "v_cmp_ne_u32 vcc, s8, v28\n"
    "s_nop 0\n"
    "s_and_b64 vcc, vcc, 0xf\n"
    "s_add_u32 s9, s9, 1\n"
    "s_cbranch_vccz B24_%=\n"
    "s_cmp_lt_u32 s9, 0x40000\n"
    "s_cbranch_scc1 R23_%=\n"
    "s_branch B24_%=\n"
    "R25_%=:\n"
    "s_sleep 1\n"
    "s_mov_b64 exec, 0x1\n"
    "buffer_load_dword v28, v9, s[68:71], s29 offen offset:32 sc1\n"
    "s_mov_b64 exec, -1\n"
    "s_waitcnt vmcnt(0)\n"
    "v_cmp_ne_u32 vcc, s8, v28\n"
    "PAD10_%=:\n"
    "s_cbranch_execz PADRET2_%=\n"
    "s_nop 0\n"
    "s_and_b64 vcc, vcc, 0x1\n"
    "s_add_u32 s9, s9, 1\n"
    "s_cbranch_vccz B26_%=\n"
    "s_cmp_lt_u32 s9, 0x40000\n"
    "s_cbranch_scc1 R25_%=\n"
    "s_branch B26_%=\n"
    "R27_%=:\n"
    "s_sleep 1\n"
    "buffer_load_dwordx4 v[132:135], v2, s[68:71], s28 offen offset:2048 sc1\n"
    "buffer_load_dwordx4 v[136:139], v2, s[68:71], s28 offen offset:3072 sc1\n"
    "s_waitcnt vmcnt(0)\n"
    "v_xor_b32 v133, s8, v133\n"
    "v_bitop3_b32 v133, s8, v135, v133 bitop3:0xbe\n"
    "v_bitop3_b32 v133, s8, v137, v133 bitop3:0xbe\n"
    "v_bitop3_b32 v133, s8, v139, v133 bitop3:0xbe\n"
    "v_cmp_ne_u32 vcc, 0, v133\n"
    "s_add_u32 s9, s9, 1\n"
    "s_cbranch_vccz B28_%=\n"
    "s_cmp_lt_u32 s9, 0x40000\n"
    "s_cbranch_scc1 R27_%=\n"
    "s_branch B28_%=\n"
    "R29_%=:\n"
    "s_sleep 1\n"
    "buffer_load_dwordx4 v[56:59], v10, s[68:71], s28 offen offset:0 sc1\n"
    "buffer_load_dwordx4 v[60:63], v10, s[68:71], s28 offen offset:1024 sc1\n"
    "s_waitcnt vmcnt(0)\n"
    "v_xor_b32 v57, s8, v57\n"
    "v_bitop3_b32 v57, s8, v59, v57 bitop3:0xbe\n"
    "v_bitop3_b32 v57, s8, v61, v57 bitop3:0xbe\n"
    "v_bitop3_b32 v57, s8, v63, v57 bitop3:0xbe\n"
    "v_cmp_ne_u32 vcc, 0, v57\n"
    "s_add_u32 s9, s9, 1\n"
    "s_cbranch_vccz B30_%=\n"
    "s_cmp_lt_u32 s9, 0x40000\n"
    "s_cbranch_scc1 R29_%=\n"
    "s_branch B30_%=\n"
    "R32_%=:\n"
    "s_sleep 1\n"
    "v_mov_b32 v28, 0\n"
    "s_mov_b64 exec, 0xf\n"
    "buffer_atomic_or v28, v9, s[68:71], s12 offen offset:0 sc0 sc1\n"
    "s_mov_b64 exec, -1\n"
    "s_waitcnt vmcnt(0)\n"
    "v_cmp_ne_u32 vcc, s8, v28\n"
    "s_nop 0\n"
    "s_and_b64 vcc, vcc, 0xf\n"
    "s_add_u32 s9, s9, 1\n"
    "s_cbranch_vccz B33_%=\n"
    "s_cmp_lt_u32 s9, 0x40000\n"
    "s_cbranch_scc1 R32_%=\n"
    "s_branch B33_%=\n"
    "END_%=:\n"
    : "+v"(tid) : "s"(__builtin_amdgcn_kernarg_segment_ptr()), "s"(blockIdx.x)
    : "v1", "v2", "v3", "v4", "v5", "v6", "v7", "v8", "v9", "v10", "v11", "v12", "v13", "v14", "v15", "v16", "v17", "v18", "v19", "v20", "v21", "v22", "v23", "v24", "v25", "v26", "v27", "v28", "v29", "v30", "v31", "v32", "v33", "v34", "v35", "v36", "v37", "v38", "v39", "v40", "v41", "v42", "v43", "v44", "v45", "v46", "v47", "v48", "v49", "v50", "v51", "v52", "v53", "v54", "v55", "v56", "v57", "v58", "v59", "v60", "v61", "v62", "v63", "v64", "v65", "v66", "v67", "v68", "v69", "v70", "v71", "v72", "v73", "v74", "v75", "v76", "v77", "v78", "v79", "v80", "v81", "v82", "v83", "v84", "v85", "v86", "v87", "v88", "v89", "v90", "v91", "v92", "v93", "v94", "v95", "v96", "v97", "v98", "v99", "v100", "v101", "v102", "v103", "v104", "v105", "v106", "v107", "v108", "v109", "v110", "v111", "v112", "v113", "v114", "v115", "v116", "v117", "v118", "v119", "v120", "v121", "v122", "v123", "v124", "v125", "v126", "v127", "v128", "v129", "v130", "v131", "v132", "v133", "v134", "v135", "v136", "v137", "v138", "v139", "v140", "v141", "v142", "v143", "v144", "v145", "v146", "v147", "v148", "v149", "v150", "v151", "v152", "v153", "v154", "v155", "v156", "v157", "v158", "v159", "v160", "v161", "v162", "v163", "v164", "v165", "v166", "v167", "v168", "v169", "v170", "v171", "v172", "v173", "v174", "v175", "v176", "v177", "v178", "v179", "v180", "v181", "v182", "v183", "v184", "v185", "v186", "v187", "v188", "v189", "v190", "v191", "v192", "v193", "v194", "v195", "v196", "v197", "v198", "v199", "v200", "v201", "v202", "v203", "v204", "v205", "v206", "v207", "v208", "v209", "v210", "v211", "v212", "v213", "v214", "v215", "v216", "v217", "v218", "v219", "v220", "v221", "v222", "v223", "v224", "v225", "v226", "v227", "v228", "v229", "v230", "v231", "v232", "v233", "v234", "v235", "v236", "v237", "v238", "v239", "v240", "v241", "v242", "v243", "v244", "v245", "v246", "v247", "v248", "v249", "v250", "v251", "v252", "v253", "v254", "v255", "a0", "a1", "a2", "a3", "a4", "a5", "a6", "a7", "a8", "a9", "a10", "a11", "a12", "a13", "a14", "a15", "a16", "a17", "a18", "a19", "a20", "a21", "a22", "a23", "a24", "a25", "a26", "a27", "a28", "a29", "a30", "a31", "a32", "a33", "a34", "a35", "a36", "a37", "a38", "a39", "a40", "a41", "a42", "a43", "a44", "a45", "a46", "a47", "a48", "a49", "a50", "a51", "a52", "a53", "a54", "a55", "a56", "a57", "a58", "a59", "a60", "a61", "a62", "a63", "a64", "a65", "a66", "a67", "a68", "a69", "a70", "a71", "a72", "a73", "a74", "a75", "a76", "a77", "a78", "a79", "a80", "a81", "a82", "a83", "a84", "a85", "a86", "a87", "a88", "a89", "a90", "a91", "a92", "a93", "a94", "a95", "a96", "a97", "a98", "a99", "a100", "a101", "a102", "a103", "a104", "a105", "a106", "a107", "a108", "a109", "a110", "a111", "a112", "a113", "a114", "a115", "a116", "a117", "a118", "a119", "a120", "a121", "a122", "a123", "a124", "a125", "a126", "a127", "a128", "a129", "a130", "a131", "a132", "a133", "a134", "a135", "a136", "a137", "a138", "a139", "a140", "a141", "a142", "a143", "a144", "a145", "a146", "a147", "a148", "a149", "a150", "a151", "a152", "a153", "a154", "a155", "a156", "a157", "a158", "a159", "a160", "a161", "a162", "a163", "a164", "a165", "a166", "a167", "a168", "a169", "a170", "a171", "a172", "a173", "a174", "a175", "a176", "a177", "a178", "a179", "a180", "a181", "a182", "a183", "a184", "a185", "a186", "a187", "a188", "a189", "a190", "a191", "a192", "a193", "a194", "a195", "a196", "a197", "a198", "a199", "a200", "a201", "a202", "a203", "a204", "a205", "a206", "a207", "a208", "a209", "a210", "a211", "a212", "a213", "a214", "a215", "a216", "a217", "a218", "a219", "a220", "a221", "a222", "a223", "a224", "a225", "a226", "a227", "a228", "a229", "a230", "a231", "a232", "a233", "a234", "a235", "a236", "a237", "a238", "a239", "a240", "a241", "a242", "a243", "a244", "a245", "a246", "a247", "a248", "a249", "a250", "a251", "a252", "a253", "a254", "a255", "s3", "s4", "s5", "s6", "s7", "s8", "s9", "s10", "s11", "s12", "s13", "s14", "s15", "s16", "s17", "s18", "s19", "s20", "s21", "s22", "s23", "s24", "s25", "s26", "s27", "s28", "s29", "s30", "s31", "s33", "s34", "s35", "s36", "s37", "s38", "s39", "s40", "s41", "s42", "s43", "s44", "s45", "s46", "s47", "s48", "s49", "s50", "s51", "s52", "s53", "s54", "s55", "s56", "s57", "s58", "s59", "s60", "s61", "s62", "s63", "s64", "s65", "s66", "s67", "s68", "s69", "s70", "s71", "s72", "s73", "s74", "s75", "s76", "s77", "s78", "s79", "s80", "s81", "s82", "s83", "s84", "s85", "s86", "s87", "s88", "s89", "s90", "s91", "s92", "s93", "s94", "s95", "s96", "s97", "s98", "s99", "vcc", "scc", "memory");
}

extern "C" void kernel_launch(void* const* d_in, const int* in_sizes, int n_in, void* d_out, int out_size, void* d_ws,
                              size_t ws_size, hipStream_t stream) {
  (void)hipFuncSetAttribute((const void*)fused_block, hipFuncAttributeMaxDynamicSharedMemorySize, 163840);
  fused_block<<<256, 256, 163840, stream>>>(d_in[0], d_in[1], d_in[2], d_in[3], d_in[4], d_in[6], d_in[7], d_in[8], d_in[9],
                                            d_out, d_ws, d_ws);
}
```
